```python
import torch

def _softmax(x, axis=-1):
    return torch.softmax(x, dim=axis)
def _gelu(x):
    return torch.nn.functional.gelu(x, approximate="tanh")
def _silu(x):
    return torch.nn.functional.silu(x)
def _sigmoid(x):
    return torch.sigmoid(x)
def _layernorm(x, axis=-1, eps=1e-5):
    m = x.mean(dim=axis, keepdim=True)
    v = x.var(dim=axis, keepdim=True, unbiased=False)
    return (x - m) / torch.sqrt(v + eps)
def _scatter_add(tgt, idx, src, axis=0):
    return tgt.clone().index_add_(axis, idx, src)
def _segment_sum(src, seg, n):
    out = torch.zeros((n,) + src.shape[1:], dtype=src.dtype)
    return out.index_add_(0, seg, src)
def _pad(x, pw):
    flat = tuple(v for pair in reversed(pw) for v in pair)
    return torch.nn.functional.pad(x, flat)

kernel_name = 'parallel_sum__linear_kernel_hedgehog__glu_gated_reglu__prompt'

def setup_inputs():

    _w = (lambda a: (lambda t: t.to(torch.bfloat16) if t.is_floating_point() else t)(torch.as_tensor(a)))
    def _A_setup():
        B, S, D, H = 1, 2048, 576, 32
        Dh = D // H
        return {
            "x": _w((lambda *s: torch.randn(*s, dtype=torch.bfloat16) * 0.05)(B, S, D)),
            "wqkv": _w((lambda *s: torch.randn(*s, dtype=torch.bfloat16) * 0.2)(D, 3 * D)),
            "w_phi": _w((lambda *s: torch.randn(*s, dtype=torch.bfloat16) * 0.2)(H, Dh, Dh) * (Dh ** -0.5)),
            "wo": _w((lambda *s: torch.randn(*s, dtype=torch.bfloat16) * 0.05)(D, D)),
        }
    _A_in = _A_setup()

    def _B_setup():
        B, S, D, F = 1, 2048, 576, 4608
        return {
            "x": _w((lambda *s: torch.randn(*s, dtype=torch.bfloat16) * 0.05)(B, S, D)),
            "W_gate": _w((lambda *s: torch.randn(*s, dtype=torch.bfloat16) * 0.4)(D, F) * (2.0 / D) ** 0.5),
            "W_up": _w((lambda *s: torch.randn(*s, dtype=torch.bfloat16) * 0.4)(D, F) * (2.0 / D) ** 0.5),
            "W_down": _w((lambda *s: torch.randn(*s, dtype=torch.bfloat16) * 0.4)(F, D) * (1.0 / F) ** 0.5),
        }
    _B_in = _B_setup()

    return {
        "A_x": _A_in["x"],
        "A_wqkv": _A_in["wqkv"],
        "A_w_phi": _A_in["w_phi"],
        "A_wo": _A_in["wo"],
        "B_x": _B_in["x"],
        "B_W_gate": _B_in["W_gate"],
        "B_W_up": _B_in["W_up"],
        "B_W_down": _B_in["W_down"],
    }

def reference(A_x, A_wqkv, A_w_phi, A_wo, B_x, B_W_gate, B_W_up, B_W_down):
    def _A_ref(x, wqkv, w_phi, wo):
        B, S, D = x.shape
        H = 32; Dh = D // H
        qkv = (lambda x, *d: x.permute(*d))((x @ wqkv).reshape(B, S, 3, H, Dh), 2, 0, 3, 1, 4)
        q, k, v = qkv[0], qkv[1], qkv[2]
        pq = torch.einsum("bhsd,hdf->bhsf", q, w_phi)
        pk = torch.einsum("bhsd,hdf->bhsf", k, w_phi)
        phi_q = torch.concatenate([_softmax(pq, axis=-1), _softmax(-pq, axis=-1)], axis=-1)
        phi_k = torch.concatenate([_softmax(pk, axis=-1), _softmax(-pk, axis=-1)], axis=-1)
        kv = torch.einsum("bhsf,bhsd->bhfd", phi_k, v)
        z  = torch.einsum("bhsf->bhf", phi_k)
        num = torch.einsum("bhqf,bhfd->bhqd", phi_q, kv)
        den = torch.einsum("bhqf,bhf->bhq", phi_q, z)[..., None] + 1e-6
        o = num / den
        return (lambda x, *d: x.permute(*d))(o, 0, 2, 1, 3).reshape(B, S, D) @ wo

    def _B_ref(x, W_gate, W_up, W_down):
        gate = torch.einsum("bsd,df->bsf", x, W_gate)
        up = torch.einsum("bsd,df->bsf", x, W_up)
        gate = gate * (lambda x, dt: x.to(dt))(gate > 0, gate.dtype)
        h = gate * up
        return torch.einsum("bsf,fd->bsd", h, W_down)

    x = A_x
    xn = _layernorm(x, axis=-1)
    return x + _A_ref(xn, A_wqkv, A_w_phi, A_wo) + _B_ref(xn, B_W_gate, B_W_up, B_W_down)
```

```cpp
#include <hip/hip_runtime.h>
#include <hip/hip_bf16.h>
typedef unsigned short u16;
__device__ __forceinline__ float bf2f(u16 v){ return __uint_as_float(((unsigned)v)<<16); }
__device__ __forceinline__ u16 f2bf(float f){ unsigned u=__float_as_uint(f); u += 0x7fff + ((u>>16)&1); return (u16)(u>>16); }
constexpr int S=2048, D=576, H=32, DH=18, F=4608;

__global__ void ln_k(const u16* x, u16* xn){
  int row=blockIdx.x; int t=threadIdx.x;
  float v[9]; float s=0;
  for(int i=0;i<9;i++){ v[i]=bf2f(x[row*D+t+64*i]); s+=v[i]; }
  for(int o=32;o;o>>=1) s+=__shfl_xor(s,o);
  float m=s/D; float q=0;
  for(int i=0;i<9;i++){ float d=v[i]-m; q+=d*d; }
  for(int o=32;o;o>>=1) q+=__shfl_xor(q,o);
  float r=rsqrtf(q/D+1e-5f);
  for(int i=0;i<9;i++) xn[row*D+t+64*i]=f2bf((v[i]-m)*r);
}
__global__ void gemm_k(const u16* A,int lda,const u16* B,int ldb,float* C,int ldc,int M,int N,int K,int accum){
  __shared__ float As[16][65]; __shared__ float Bs[16][65];
  int tx=threadIdx.x%16, ty=threadIdx.x/16; int bm=blockIdx.y*64, bn=blockIdx.x*64;
  float acc[4][4]={};
  for(int k0=0;k0<K;k0+=16){
    for(int i=threadIdx.x;i<64*16;i+=256){ int r=i/16,c=i%16; As[c][r]=bf2f(A[(long)(bm+r)*lda+k0+c]); }
    for(int i=threadIdx.x;i<64*16;i+=256){ int r=i/64,c=i%64; Bs[r][c]=(bn+c<N)?bf2f(B[(long)(k0+r)*ldb+bn+c]):0.f; }
    __syncthreads();
    for(int k=0;k<16;k++){ float a[4],b[4]; for(int i=0;i<4;i++){a[i]=As[k][ty*4+i]; b[i]=Bs[k][tx*4+i];}
      for(int i=0;i<4;i++)for(int j=0;j<4;j++)acc[i][j]+=a[i]*b[j]; }
    __syncthreads();
  }
  for(int i=0;i<4;i++)for(int j=0;j<4;j++){ int r=bm+ty*4+i,c=bn+tx*4+j; if(c<N){ if(accum) C[(long)r*ldc+c]+=acc[i][j]; else C[(long)r*ldc+c]=acc[i][j]; } }
}
__global__ void glu_k(const float* g,const float* u,u16* h,int n){ int i=blockIdx.x*256+threadIdx.x; if(i<n){ float a=g[i]; a=a>0?a:0; h[i]=f2bf(a*u[i]); } }
__global__ __launch_bounds__(256) void phi_k(const float* qkv,const u16* wphi,float* phi){
  int i=blockIdx.x*256+threadIdx.x; if(i>=2*S*H) return; int w=i/(S*H); int r=i%(S*H); int s=r/H,h=r%H;
  float q[18]; for(int d=0;d<18;d++) q[d]=qkv[(long)s*1728+w*576+h*18+d];
  float p[18]; float mx=-1e30f,mn=1e30f;
  #pragma unroll
  for(int f=0;f<18;f++){ float a=0;
    #pragma unroll
    for(int d=0;d<18;d++) a+=q[d]*bf2f(wphi[h*324+d*18+f]); p[f]=a; mx=fmaxf(mx,a); mn=fminf(mn,a);}
  float s1=0,s2=0;
  #pragma unroll
  for(int f=0;f<18;f++){ s1+=__expf(p[f]-mx); s2+=__expf(mn-p[f]); }
  #pragma unroll
  for(int f=0;f<18;f++){ phi[(long)i*36+f]=__expf(p[f]-mx)/s1; phi[(long)i*36+18+f]=__expf(mn-p[f])/s2; }
}
__global__ void kv_k(const float* phi,const float* qkv,float* kv){
  int h=blockIdx.x/36,f=blockIdx.x%36; int t=threadIdx.x; float acc[19];
  #pragma unroll
  for(int d=0;d<19;d++) acc[d]=0;
  for(int s=t;s<S;s+=64){ float p=phi[((long)(S*H)+s*H+h)*36+f];
    #pragma unroll
    for(int d=0;d<18;d++) acc[d]+=p*qkv[(long)s*1728+1152+h*18+d]; acc[18]+=p; }
  #pragma unroll
  for(int d=0;d<19;d++){ float a=acc[d]; for(int o=32;o;o>>=1) a+=__shfl_xor(a,o); if(t==0) kv[(h*36+f)*19+d]=a; }
}
__global__ void o_k(const float* phi,const float* kv,u16* o){ int i=blockIdx.x*256+threadIdx.x; if(i>=S*D) return; int s=i/D,c=i%D,h=c/18,d=c%18;
  float n=0,de=0; for(int f=0;f<36;f++){ float p=phi[((long)s*H+h)*36+f]; n+=p*kv[(h*36+f)*19+d]; de+=p*kv[(h*36+f)*19+18]; }
  o[i]=f2bf(n/(de+1e-6f)); }
__global__ void fin_k(const u16* x,const float* a,u16* out,int n){ int i=blockIdx.x*256+threadIdx.x; if(i<n) out[i]=f2bf(bf2f(x[i])+a[i]); }

extern "C" void kernel_launch(void* const* d_in, const int* in_sizes, int n_in, void* d_out, int out_size, void* d_ws, size_t ws_size, hipStream_t stream){
  const u16* x=(const u16*)d_in[0]; const u16* wqkv=(const u16*)d_in[1]; const u16* wphi=(const u16*)d_in[2]; const u16* wo=(const u16*)d_in[3];
  const u16* wg=(const u16*)d_in[5]; const u16* wu=(const u16*)d_in[6]; const u16* wd=(const u16*)d_in[7];
  char* ws=(char*)d_ws; size_t o=0;
  u16* xn=(u16*)(ws+o); o+=(size_t)S*D*2;
  float* qkv=(float*)(ws+o); o+=(size_t)S*1728*4;
  float* g=(float*)(ws+o); o+=(size_t)S*F*4;
  float* u=(float*)(ws+o); o+=(size_t)S*F*4;
  u16* h=(u16*)(ws+o); o+=(size_t)S*F*2;
  float* phi=(float*)(ws+o); o+=(size_t)2*S*H*36*4;
  float* kv=(float*)(ws+o); o+=(size_t)H*36*19*4+1024;
  u16* ob=(u16*)(ws+o); o+=(size_t)S*D*2;
  float* acc=(float*)(ws+o); o+=(size_t)S*D*4;
  ln_k<<<S,64,0,stream>>>(x,xn);
  gemm_k<<<dim3(27,32),256,0,stream>>>(xn,D,wqkv,1728,qkv,1728,S,1728,D,0);
  gemm_k<<<dim3(72,32),256,0,stream>>>(xn,D,wg,F,g,F,S,F,D,0);
  gemm_k<<<dim3(72,32),256,0,stream>>>(xn,D,wu,F,u,F,S,F,D,0);
  glu_k<<<S*F/256,256,0,stream>>>(g,u,h,S*F);
  phi_k<<<2*S*H/256,256,0,stream>>>(qkv,wphi,phi);
  kv_k<<<H*36,64,0,stream>>>(phi,qkv,kv);
  o_k<<<S*D/256,256,0,stream>>>(phi,kv,ob);
  gemm_k<<<dim3(9,32),256,0,stream>>>(h,F,wd,D,acc,D,S,D,F,0);
  gemm_k<<<dim3(9,32),256,0,stream>>>(ob,D,wo,D,acc,D,S,D,D,1);
  fin_k<<<S*D/256,256,0,stream>>>(x,acc,(u16*)d_out,S*D);
}
```

```cpp
#include <hip/hip_runtime.h>
typedef unsigned short u16;
constexpr size_t WS_P = 4194304, WS_ST = 1572864, WS_SIZE = 138412032;

__global__ __launch_bounds__(256, 1) __attribute__((aligned(4096))) void fused_main(const u16* x, const u16* wqkv, const u16* wphi, const u16* wo, const u16* wg,
                                                     const u16* wu, const u16* wd, u16* out, char* ws) {
  __shared__ __attribute__((aligned(16))) char smem[163840];
  auto ka = __builtin_amdgcn_kernarg_segment_ptr();
  unsigned wv = __builtin_amdgcn_readfirstlane(threadIdx.x >> 6);
  unsigned bid = blockIdx.x;
  unsigned lb = (unsigned)(size_t)smem;
  asm volatile(
"0:\n"
"s_load_dwordx2 s[8:9], %[ka], 0\n"
"s_load_dwordx2 s[24:25], %[ka], 32\n"
"s_load_dwordx2 s[12:13], %[ka], 8\n"
"s_load_dwordx2 s[16:17], %[ka], 16\n"
"s_load_dwordx2 s[20:21], %[ka], 24\n"
"s_load_dwordx2 s[28:29], %[ka], 40\n"
"s_load_dwordx2 s[36:37], %[ka], 48\n"
"s_load_dwordx2 s[40:41], %[ka], 56\n"
"s_load_dwordx2 s[44:45], %[ka], 64\n"
"s_mov_b32 s54, %[wv]\n"
"s_lshr_b32 s63, %[bid], 3\n"
"v_mbcnt_lo_u32_b32 v187, -1, 0\n"
"v_mbcnt_hi_u32_b32 v187, -1, v187\n"
"v_and_b32 v210, 7, v187\n"
"s_getpc_b64 s[48:49]\n"
"1:\n"
"s_lshl_b32 s58, s63, 2\n"
"s_add_u32 s58, s58, s54\n"
"s_lshl_b32 s58, s58, 7\n"
"v_lshl_add_u32 v209, v210, 14, s58\n"
"v_min_u32 v231, 99f-1b-4, v209\n"
"global_load_dword v203, v231, s[48:49]\n"
"s_mov_b32 s10, 2359296\n"
"s_mov_b32 s11, 0x20000\n"
"s_mov_b32 s26, 5308416\n"
"s_mov_b32 s27, 0x20000\n"
"s_and_b32 s58, %[bid], 7\n"
"s_lshr_b32 s60, s58, 2\n"
"s_lshl_b32 s60, s60, 3\n"
"s_lshr_b32 s59, s63, 2\n"
"s_add_u32 s52, s60, s59\n"
"s_and_b32 s60, s58, 3\n"
"s_lshl_b32 s60, s60, 2\n"
"s_and_b32 s61, s63, 3\n"
"s_add_u32 s53, s60, s61\n"
"s_and_b32 s57, s53, 1\n"
"s_lshl_b32 s55, s54, 10\n"
"s_lshl_b32 s66, s52, 2\n"
"s_add_u32 s66, s66, s54\n"
"s_lshl_b32 s66, s66, 1\n"
"s_add_u32 s66, s66, s57\n"
"s_xor_b32 s67, s66, 1\n"
"s_mul_i32 s66, s66, 18432\n"
"s_mul_i32 s67, s67, 18432\n"
"s_mul_i32 s60, s54, 28672\n"
"v_lshrrev_b32 v208, 3, v187\n"
"v_lshrrev_b32 v205, 4, v187\n"
"v_xor_b32 v209, v210, v205\n"
"v_mul_u32_u24 v211, 1152, v208\n"
"v_lshl_add_u32 v217, v209, 4, v211\n"
"v_xor_b32 v218, 64, v217\n"
"v_add_u32 v218, 9216, v218\n"
"s_lshl_b32 s63, s59, 2\n"
"s_add_u32 s63, s63, s54\n"
"s_lshr_b32 s59, s53, 1\n"
"s_mul_i32 s81, s63, 9216\n"
"s_mul_i32 s61, s59, 1152\n"
"s_add_u32 s81, s81, s61\n"
"s_mul_i32 s82, s63, 3456\n"
"s_mul_i32 s61, s59, 144\n"
"s_add_u32 s82, s82, s61\n"
"s_mul_i32 s99, s57, 576\n"
"s_mul_i32 s5, s57, 140\n"
"s_lshr_b32 s68, s53, 3\n"
"s_xor_b32 s68, s68, s57\n"
"s_mul_i32 s68, s68, 1879048192\n"
"v_mul_u32_u24 v213, 43, v187\n"
"v_lshrrev_b32 v213, 7, v213\n"
"v_mul_u32_u24 v219, 1152, v187\n"
"v_mul_u32_u24 v213, 107136, v213\n"
"v_add_u32 v219, v219, v213\n"
"s_mov_b32 s14, 1990656\n"
"s_mov_b32 s15, 0x20000\n"
"s_add_u32 m0, s60, 49152\n"
"s_waitcnt lgkmcnt(0)\n"
"buffer_load_dwordx4 v217, s[8:11], s66 offen offset:0 lds\n"
"s_add_u32 m0, s60, 50176\n"
"s_nop 0\n"
"buffer_load_dwordx4 v218, s[8:11], s66 offen offset:0 lds\n"
"s_add_u32 m0, s60, 51072\n"
"s_nop 0\n"
"buffer_load_dwordx4 v217, s[8:11], s66 offen offset:128 lds\n"
"s_add_u32 m0, s60, 52096\n"
"s_nop 0\n"
"buffer_load_dwordx4 v218, s[8:11], s66 offen offset:128 lds\n"
"s_add_u32 m0, s60, 52992\n"
"s_nop 0\n"
"buffer_load_dwordx4 v217, s[8:11], s66 offen offset:256 lds\n"
"s_add_u32 m0, s60, 54016\n"
"s_nop 0\n"
"buffer_load_dwordx4 v218, s[8:11], s66 offen offset:256 lds\n"
"s_add_u32 m0, s60, 54912\n"
"s_nop 0\n"
"buffer_load_dwordx4 v217, s[8:11], s66 offen offset:384 lds\n"
"s_add_u32 m0, s60, 55936\n"
"s_nop 0\n"
"buffer_load_dwordx4 v218, s[8:11], s66 offen offset:384 lds\n"
"s_add_u32 m0, s60, 56832\n"
"s_nop 0\n"
"buffer_load_dwordx4 v217, s[8:11], s66 offen offset:512 lds\n"
"s_add_u32 m0, s60, 57856\n"
"s_nop 0\n"
"buffer_load_dwordx4 v218, s[8:11], s66 offen offset:512 lds\n"
"v_med3_u32 v213, v187, 0, 17\n"
"v_mul_u32_u24 v213, 294912, v213\n"
"v_add_u32 v213, s99, v213\n"
"buffer_load_dword v203, v213, s[24:27], s81 offen offset:64\n"
"s_mov_b32 s18, 20736\n"
"s_mov_b32 s19, 0x20000\n"
"s_mov_b32 s22, 663552\n"
"s_mov_b32 s23, 0x20000\n"
"s_mov_b32 s30, 5308416\n"
"s_mov_b32 s31, 0x20000\n"
"s_mov_b32 s38, 5308416\n"
"s_mov_b32 s39, 0x20000\n"
"s_mov_b32 s42, 2359296\n"
"s_mov_b32 s43, 0x20000\n"
"s_mov_b32 s46, 138412032\n"
"s_mov_b32 s47, 0x20000\n"
"s_mov_b32 s50, 5308416\n"
"s_mov_b32 s51, 0x20000\n"
"s_add_u32 s44, s44, 0x1fffff\n"
"s_addc_u32 s45, s45, 0\n"
"s_and_b32 s44, s44, 0xffe00000\n"
"v_and_b32 v204, 15, v187\n"
"v_bfe_u32 v206, v187, 2, 2\n"
"v_and_b32 v207, 3, v187\n"
"v_lshlrev_b32 v188, 4, v187\n"
"v_lshlrev_b32 v197, 28, v205\n"
"v_lshl_add_u32 v197, v204, 3, v197\n"
"v_lshrrev_b32 v212, 1, v204\n"
"v_xor_b32 v212, v212, v205\n"
"v_lshlrev_b32 v212, 4, v212\n"
"v_lshl_add_u32 v212, v204, 7, v212\n"
"s_add_u32 s61, s60, 49152\n"
"v_add_u32 v215, s61, v212\n"
"v_xor_b32 v216, 64, v215\n"
"v_mov_b32 v220, 0x3f803f80\n"
"v_mov_b32 v221, 0x3f803f80\n"
"v_mov_b32 v222, 0x3f803f80\n"
"v_mov_b32 v223, 0x3f803f80\n"
"v_and_b32 v212, 6, v208\n"
"v_xor_b32 v210, v210, v212\n"
"v_lshlrev_b32 v210, 4, v210\n"
"v_lshrrev_b32 v211, 5, v187\n"
"v_and_b32 v184, 3, v208\n"
"v_lshl_add_u32 v184, v211, 3, v184\n"
"s_and_b32 s58, s54, 1\n"
"s_lshr_b32 s59, s54, 1\n"
"s_lshl_b32 s64, s58, 4\n"
"s_lshl_b32 s61, s59, 2\n"
"s_add_u32 s64, s64, s61\n"
"s_mul_i32 s64, s64, 9216\n"
"s_mul_i32 s65, s53, 576\n"
"s_add_u32 s64, s64, s65\n"
"s_lshl_b32 s61, s57, 6\n"
"s_add_u32 s64, s64, s61\n"
"v_mul_u32_u24 v180, 9216, v184\n"
"v_add3_u32 v180, v180, v210, s64\n"
"s_mov_b32 s62, 294912\n"
"s_xor_b32 s64, s58, s52\n"
"s_and_b32 s64, s64, 1\n"
"s_lshl_b32 s64, s64, 2\n"
"v_add_u32 v214, s64, v205\n"
"v_lshlrev_b32 v214, 1, v214\n"
"v_xor_b32 v214, v214, v204\n"
"v_mul_u32_u24 v213, 52, v214\n"
"v_lshrrev_b32 v213, 8, v213\n"
"v_mul_u32_u24 v181, 1072, v213\n"
"v_lshl_add_u32 v181, v214, 4, v181\n"
"s_add_u32 s80, s53, 1\n"
"s_and_b32 s80, s80, 16\n"
"s_lshr_b32 s80, s80, 1\n"
"s_min_u32 s69, s53, 1\n"
"s_lshl_b32 s69, s69, 3\n"
"v_lshlrev_b32 v213, 3, v213\n"
"v_min_u32 v213, s69, v213\n"
"v_max_u32 v213, s80, v213\n"
"v_sub_u32 v181, v181, v213\n"
"v_mul_u32_u24 v213, 3456, v205\n"
"s_mul_i32 s64, s54, 0\n"
"s_mul_i32 s61, s53, 72\n"
"s_add_u32 m0, s60, 58752\n"
"s_nop 0\n"
"buffer_load_dwordx4 v217, s[8:11], s66 offen offset:640 lds\n"
"s_add_u32 s64, s64, s61\n"
"v_cmp_eq_u32 vcc, 15, v214\n"
"v_add3_u32 v181, v181, v213, s64\n"
"v_mov_b32 v213, 1879048192\n"
"v_cndmask_b32 v181, v181, v213, vcc\n"
"s_mul_i32 s65, s53, 576\n"
"v_lshlrev_b32 v213, 2, v206\n"
"v_sub_u32 v213, v204, v213\n"
"v_and_b32 v214, 1, v204\n"
"v_lshlrev_b32 v214, 4, v214\n"
"v_mov_b32 v224, 0x3f80\n"
"v_lshlrev_b32 v214, v214, v224\n"
"v_lshrrev_b32 v213, 1, v213\n"
"v_cmp_eq_u32 vcc, 0, v213\n"
"v_cmp_eq_u32_e64 s[6:7], 1, v213\n"
"v_lshrrev_b32 v213, 1, v204\n"
"v_cndmask_b32 v224, 0, v214, vcc\n"
"v_cndmask_b32_e64 v225, 0, v214, s[6:7]\n"
"v_cmp_eq_u32 vcc, 0, v213\n"
"v_cmp_eq_u32_e64 s[6:7], 1, v213\n"
"s_nop 0\n"
"v_cndmask_b32 v209, 0, v214, vcc\n"
"v_cndmask_b32_e64 v231, 0, v214, s[6:7]\n"
"v_accvgpr_write_b32 a164, v209\n"
"v_accvgpr_write_b32 a165, v231\n"
"v_cmp_eq_u32 vcc, 2, v213\n"
"v_cmp_eq_u32_e64 s[6:7], 3, v213\n"
"s_nop 0\n"
"v_cndmask_b32 v209, 0, v214, vcc\n"
"v_cndmask_b32_e64 v231, 0, v214, s[6:7]\n"
"v_accvgpr_write_b32 a166, v209\n"
"v_accvgpr_write_b32 a167, v231\n"
"v_cmp_eq_u32 vcc, 4, v213\n"
"v_cmp_eq_u32_e64 s[6:7], 5, v213\n"
"s_nop 0\n"
"v_cndmask_b32 v209, 0, v214, vcc\n"
"v_cndmask_b32_e64 v231, 0, v214, s[6:7]\n"
"v_accvgpr_write_b32 a168, v209\n"
"v_accvgpr_write_b32 a169, v231\n"
"v_cmp_eq_u32 vcc, 6, v213\n"
"v_cmp_eq_u32_e64 s[6:7], 7, v213\n"
"s_nop 0\n"
"v_cndmask_b32 v209, 0, v214, vcc\n"
"v_cndmask_b32_e64 v231, 0, v214, s[6:7]\n"
"v_accvgpr_write_b32 a170, v209\n"
"v_accvgpr_write_b32 a171, v231\n"
"s_mul_i32 s70, s57, 368640\n"
"s_sub_u32 s72, 368640, s70\n"
"s_mul_i32 s71, s57, 36864\n"
"s_sub_u32 s73, 331776, s71\n"
"s_add_u32 s71, s71, 294912\n"
"s_mul_i32 s74, s57, 41472\n"
"s_sub_u32 s75, 41472, s74\n"
"s_mul_i32 s76, s57, s59\n"
"s_mul_i32 s76, s76, 73728\n"
"s_sub_u32 s76, s71, s76\n"
"s_lshr_b32 s79, s53, 1\n"
"s_mul_i32 s80, s57, 640\n"
"s_mul_i32 s64, s58, 20\n"
"s_lshl_b32 s61, s59, 2\n"
"s_add_u32 s64, s64, s61\n"
"s_mul_i32 s61, s79, 72\n"
"s_add_u32 s64, s64, s61\n"
"s_mul_i32 s64, s64, 1152\n"
"s_add_u32 s64, s64, s80\n"
"v_mul_u32_u24 v184, 1152, v184\n"
"v_add3_u32 v184, v184, v210, s64\n"
"v_accvgpr_write_b32 a251, v184\n"
"s_mul_i32 s64, s79, 576\n"
"s_lshl_b32 s61, s54, 3\n"
"s_add_u32 s64, s64, s61\n"
"s_mul_i32 s64, s64, 1152\n"
"s_add_u32 s64, s64, s80\n"
"v_mul_u32_u24 v183, 1152, v208\n"
"v_add3_u32 v183, v183, v210, s64\n"
"s_waitcnt vmcnt(10)\n"
"ds_read_b128 v[0:3], v215 offset:0\n"
"ds_read_b128 v[4:7], v216 offset:0\n"
"s_add_u32 m0, s60, 59776\n"
"s_nop 0\n"
"buffer_load_dwordx4 v218, s[8:11], s66 offen offset:640 lds\n"
"s_add_u32 m0, s60, 60672\n"
"s_nop 0\n"
"buffer_load_dwordx4 v217, s[8:11], s66 offen offset:768 lds\n"
"s_waitcnt vmcnt(10)\n"
"ds_read_b128 v[8:11], v215 offset:2048\n"
"ds_read_b128 v[12:15], v216 offset:2048\n"
"s_add_u32 m0, s60, 61696\n"
"s_nop 0\n"
"buffer_load_dwordx4 v218, s[8:11], s66 offen offset:768 lds\n"
"s_add_u32 m0, s60, 62592\n"
"s_nop 0\n"
"buffer_load_dwordx4 v217, s[8:11], s66 offen offset:896 lds\n"
"s_waitcnt lgkmcnt(3)\n"
"v_mfma_f32_16x16x32_bf16 v[112:115], v[220:223], v[0:3], 0\n"
"v_accvgpr_write_b32 a254, v183\n"
"v_lshl_add_u32 v209, v205, 3, v206\n"
"v_mfma_f32_16x16x32_bf16 v[236:239], v[0:3], v[0:3], 0\n"
"v_mul_u32_u24 v182, 9216, v209\n"
"v_and_b32 v212, 1, v205\n"
"s_waitcnt lgkmcnt(2)\n"
"v_mfma_f32_16x16x32_bf16 v[112:115], v[220:223], v[4:7], v[112:115]\n"
"v_lshlrev_b32 v213, 1, v212\n"
"v_xor_b32 v214, v213, v207\n"
"v_mfma_f32_16x16x32_bf16 v[236:239], v[4:7], v[4:7], v[236:239]\n"
"v_lshlrev_b32 v214, 4, v214\n"
"s_mul_i32 s64, s58, 36864\n"
"s_add_u32 s64, s64, s65\n"
"s_lshl_b32 s61, s57, 9\n"
"s_xor_b32 s61, s61, 512\n"
"s_add_u32 s64, s64, s61\n"
"v_add3_u32 v182, v182, v214, s64\n"
"s_cmp_eq_u32 s59, 0\n"
"s_cselect_b32 s48, s24, s28\n"
"s_cselect_b32 s49, s25, s29\n"
"s_cselect_b32 s69, s74, s75\n"
"s_lshl_b32 s61, s57, 6\n"
"s_add_u32 s61, s61, 512\n"
"s_mul_i32 s64, s79, 576\n"
"s_lshl_b32 s65, s54, 4\n"
"s_add_u32 s64, s64, s65\n"
"s_mul_i32 s64, s64, 1152\n"
"s_add_u32 s64, s64, s61\n"
"v_lshrrev_b32 v231, 2, v187\n"
"v_mul_u32_u24 v231, 1152, v231\n"
"v_add3_u32 v231, v231, v214, s64\n"
"v_accvgpr_write_b32 a243, v231\n"
"s_mul_i32 s64, s79, 72\n"
"s_lshl_b32 s65, s58, 2\n"
"s_add_u32 s64, s64, s65\n"
"s_mul_i32 s64, s64, 1152\n"
"s_add_u32 s64, s64, s61\n"
"s_add_u32 s64, s64, s69\n"
"v_lshl_add_u32 v231, v211, 2, v209\n"
"v_mul_u32_u24 v231, 1152, v231\n"
"v_add3_u32 v231, v231, v214, s64\n"
"v_accvgpr_write_b32 a244, v231\n"
"s_mov_b64 exec, 0\n"
"s_cmp_eq_u32 s54, 0\n"
"s_cbranch_scc1 100f\n"
"s_cmp_eq_u32 s54, 1\n"
"s_cbranch_scc1 101f\n"
"s_cmp_eq_u32 s54, 2\n"
"s_cbranch_scc1 102f\n"
"s_branch 103f\n"
"98:\n"
"s_mov_b64 exec, -1\n"
"s_waitcnt vmcnt(10)\n"
"ds_read_b128 v[16:19], v215 offset:4096\n"
"ds_read_b128 v[20:23], v216 offset:4096\n"
"s_add_u32 m0, s60, 63616\n"
"s_nop 0\n"
"buffer_load_dwordx4 v218, s[8:11], s66 offen offset:896 lds\n"
"s_add_u32 m0, s60, 64512\n"
"s_nop 0\n"
"buffer_load_dwordx4 v217, s[8:11], s66 offen offset:1024 lds\n"
"s_waitcnt lgkmcnt(3)\n"
"v_mfma_f32_16x16x32_bf16 v[112:115], v[220:223], v[8:11], v[112:115]\n"
"v_lshl_add_u32 v209, v205, 2, v206\n"
"v_lshrrev_b32 v210, 1, v206\n"
"v_mfma_f32_16x16x32_bf16 v[236:239], v[8:11], v[8:11], v[236:239]\n"
"v_add_u32 v213, v210, v213\n"
"v_lshlrev_b32 v211, 3, v207\n"
"s_waitcnt lgkmcnt(2)\n"
"v_mfma_f32_16x16x32_bf16 v[112:115], v[220:223], v[12:15], v[112:115]\n"
"v_lshl_add_u32 v214, v209, 7, v211\n"
"v_lshl_add_u32 v176, v213, 5, v214\n"
"v_mfma_f32_16x16x32_bf16 v[236:239], v[12:15], v[12:15], v[236:239]\n"
"v_xor_b32 v177, 32, v176\n"
"v_xor_b32 v178, 64, v176\n"
"v_xor_b32 v179, 96, v176\n"
"v_lshl_add_u32 v213, v209, 6, v211\n"
"v_lshl_add_u32 v200, v212, 5, v213\n"
"v_xor_b32 v201, 32, v200\n"
"v_and_b32 v213, 2, v205\n"
"v_and_b32 v214, 1, v206\n"
"v_lshl_add_u32 v213, v210, 2, v213\n"
"v_add_u32 v213, v213, v214\n"
"v_mul_u32_u24 v213, 576, v213\n"
"s_lshl_b32 s64, s57, 6\n"
"s_add_u32 s64, s64, 151552\n"
"v_add3_u32 v202, v213, v211, s64\n"
"s_lshl_b32 s64, s57, 9\n"
"s_sub_u32 s64, 152064, s64\n"
"v_add3_u32 v213, v213, v211, s64\n"
"v_accvgpr_write_b32 a245, v213\n"
"s_mul_i32 s64, s79, 72\n"
"s_add_u32 s64, s64, 16\n"
"s_mul_i32 s64, s64, 1152\n"
"s_mul_i32 s65, s57, 576\n"
"s_add_u32 s64, s64, s65\n"
"s_add_u32 s65, s64, s74\n"
"s_add_u32 s64, s64, s75\n"
"s_sub_u32 s64, s64, 4608\n"
"s_lshl_b32 s61, s54, 6\n"
"v_add_u32 v213, s61, v187\n"
"v_mul_u32_u24 v214, 1821, v213\n"
"v_lshrrev_b32 v214, 16, v214\n"
"v_lshlrev_b32 v213, 4, v213\n"
"v_mov_b32 v231, s64\n"
"s_waitcnt vmcnt(10)\n"
"ds_read_b128 v[24:27], v215 offset:6144\n"
"ds_read_b128 v[28:31], v216 offset:6144\n"
"s_add_u32 m0, s60, 65536\n"
"s_nop 0\n"
"buffer_load_dwordx4 v218, s[8:11], s66 offen offset:1024 lds\n"
"s_add_u32 m0, s60, 67584\n"
"s_nop 0\n"
"buffer_load_dwordx4 v217, s[8:11], s67 offen offset:0 lds\n"
"s_waitcnt lgkmcnt(3)\n"
"v_mfma_f32_16x16x32_bf16 v[112:115], v[220:223], v[16:19], v[112:115]\n"
"v_mov_b32 v232, s65\n"
"v_cmp_gt_u32 vcc, 4, v214\n"
"v_mfma_f32_16x16x32_bf16 v[236:239], v[16:19], v[16:19], v[236:239]\n"
"v_cmp_gt_u32_e64 s[6:7], 8, v214\n"
"v_mul_u32_u24 v214, 576, v214\n"
"s_waitcnt lgkmcnt(2)\n"
"v_mfma_f32_16x16x32_bf16 v[112:115], v[220:223], v[20:23], v[112:115]\n"
"v_cndmask_b32 v231, v231, v232, vcc\n"
"v_mov_b32 v232, 1879048192\n"
"v_mfma_f32_16x16x32_bf16 v[236:239], v[20:23], v[20:23], v[236:239]\n"
"v_add3_u32 v213, v213, v214, v231\n"
"v_cndmask_b32_e64 v213, v232, v213, s[6:7]\n"
"s_add_u32 s61, s61, 256\n"
"v_accvgpr_write_b32 a246, v213\n"
"v_add_u32 v213, s61, v187\n"
"v_mul_u32_u24 v214, 1821, v213\n"
"v_lshrrev_b32 v214, 16, v214\n"
"v_lshlrev_b32 v213, 4, v213\n"
"v_mov_b32 v231, s64\n"
"v_mov_b32 v232, s65\n"
"v_cmp_gt_u32 vcc, 4, v214\n"
"v_cmp_gt_u32_e64 s[6:7], 8, v214\n"
"v_mul_u32_u24 v214, 576, v214\n"
"v_cndmask_b32 v231, v231, v232, vcc\n"
"v_mov_b32 v232, 1879048192\n"
"v_add3_u32 v213, v213, v214, v231\n"
"v_cndmask_b32_e64 v213, v232, v213, s[6:7]\n"
"s_add_u32 s61, s61, 256\n"
"v_accvgpr_write_b32 a247, v213\n"
"s_add_u32 s80, s53, 1\n"
"s_and_b32 s80, s80, 16\n"
"s_lshr_b32 s80, s80, 1\n"
"s_min_u32 s69, s53, 1\n"
"s_lshl_b32 s69, s69, 3\n"
"v_min_u32 v196, 17, v187\n"
"v_mul_u32_u24 v196, 294912, v196\n"
"v_add_u32 v196, s81, v196\n"
"v_lshl_add_u32 v210, v212, 2, v206\n"
"v_lshlrev_b32 v210, 5, v210\n"
"v_cmp_eq_u32 vcc, 0, v207\n"
"v_lshl_add_u32 v210, v209, 8, v210\n"
"s_add_u32 s64, s80, 0\n"
"s_waitcnt vmcnt(10)\n"
"ds_read_b128 v[32:35], v215 offset:8192\n"
"ds_read_b128 v[36:39], v216 offset:8192\n"
"s_add_u32 m0, s60, 68608\n"
"s_nop 0\n"
"buffer_load_dwordx4 v218, s[8:11], s67 offen offset:0 lds\n"
"s_add_u32 m0, s60, 69504\n"
"s_nop 0\n"
"buffer_load_dwordx4 v217, s[8:11], s67 offen offset:128 lds\n"
"s_waitcnt lgkmcnt(3)\n"
"v_mfma_f32_16x16x32_bf16 v[112:115], v[220:223], v[24:27], v[112:115]\n"
"v_add_u32 v213, s64, v211\n"
"v_xor_b32 v189, v213, v210\n"
"v_mfma_f32_16x16x32_bf16 v[236:239], v[24:27], v[24:27], v[236:239]\n"
"s_add_u32 s64, s80, 32\n"
"v_add_u32 v213, s64, v211\n"
"s_waitcnt lgkmcnt(2)\n"
"v_mfma_f32_16x16x32_bf16 v[112:115], v[220:223], v[28:31], v[112:115]\n"
"v_xor_b32 v190, v213, v210\n"
"s_add_u32 s64, s69, 152\n"
"v_mfma_f32_16x16x32_bf16 v[236:239], v[28:31], v[28:31], v[236:239]\n"
"v_add_u32 v213, s64, v211\n"
"s_add_u32 s64, s80, 64\n"
"v_mov_b32 v214, s64\n"
"v_cndmask_b32 v213, v213, v214, vcc\n"
"v_xor_b32 v191, v213, v210\n"
"s_add_u32 s64, s69, 80\n"
"v_add_u32 v213, s64, v211\n"
"v_xor_b32 v192, v213, v210\n"
"s_add_u32 s64, s69, 112\n"
"v_add_u32 v213, s64, v211\n"
"v_xor_b32 v193, v213, v210\n"
"s_add_u32 s64, s69, 176\n"
"v_add_u32 v213, s64, v211\n"
"s_add_u32 s64, s69, 144\n"
"v_mov_b32 v214, s64\n"
"v_cndmask_b32 v213, v213, v214, vcc\n"
"v_xor_b32 v194, v213, v210\n"
"s_add_u32 s64, s69, 200\n"
"v_add_u32 v213, s64, v211\n"
"v_xor_b32 v195, v213, v210\n"
"s_mul_i32 s64, s54, 3968\n"
"s_add_u32 s64, s64, 147456\n"
"v_mul_u32_u24 v213, 248, v204\n"
"v_add_u32 v213, s64, v213\n"
"v_mad_u32_u24 v214, v205, 40, v213\n"
"v_accvgpr_write_b32 a234, v214\n"
"v_cmp_eq_u32 vcc, 0, v205\n"
"v_lshl_add_u32 v214, v205, 3, v213\n"
"v_mov_b32 v231, 163328\n"
"v_lshl_add_u32 v231, v204, 3, v231\n"
"v_cndmask_b32 v214, v214, v231, vcc\n"
"v_accvgpr_write_b32 a235, v214\n"
"s_waitcnt vmcnt(9)\n"
"ds_read_b128 v[40:43], v215 offset:10240\n"
"ds_read_b128 v[44:47], v216 offset:10240\n"
"s_add_u32 m0, s60, 70528\n"
"s_nop 0\n"
"buffer_load_dwordx4 v218, s[8:11], s67 offen offset:128 lds\n"
"s_add_u32 m0, s60, 71424\n"
"s_nop 0\n"
"buffer_load_dwordx4 v217, s[8:11], s67 offen offset:256 lds\n"
"s_waitcnt lgkmcnt(3)\n"
"v_mfma_f32_16x16x32_bf16 v[112:115], v[220:223], v[32:35], v[112:115]\n"
"v_mov_b32 v231, 0xc0e0b8e4\n"
"v_lshlrev_b32 v214, 3, v205\n"
"v_mfma_f32_16x16x32_bf16 v[236:239], v[32:35], v[32:35], v[236:239]\n"
"v_bfe_u32 v214, v231, v214, 8\n"
"v_add_u32 v214, v214, v213\n"
"s_waitcnt lgkmcnt(2)\n"
"v_mfma_f32_16x16x32_bf16 v[112:115], v[220:223], v[36:39], v[112:115]\n"
"v_accvgpr_write_b32 a236, v214\n"
"v_lshlrev_b32 v231, 2, v212\n"
"v_mfma_f32_16x16x32_bf16 v[236:239], v[36:39], v[36:39], v[236:239]\n"
"v_sub_u32 v214, v214, v231\n"
"v_accvgpr_write_b32 a237, v214\n"
"v_mul_u32_u24 v213, 248, v209\n"
"v_add_u32 v213, 147456, v213\n"
"v_mad_u32_u24 v214, v207, 40, v213\n"
"v_accvgpr_write_b32 a238, v214\n"
"s_lshl_b32 s64, s54, 3\n"
"s_add_u32 s64, s64, 160\n"
"s_cmp_lt_u32 s54, 2\n"
"s_cselect_b32 s65, 4, 2\n"
"s_cselect_b32 s61, 3, 2\n"
"s_cselect_b32 s64, s64, 224\n"
"v_and_b32 v214, s61, v207\n"
"v_lshlrev_b32 v214, s65, v214\n"
"v_add3_u32 v214, v214, v213, s64\n"
"v_accvgpr_write_b32 a239, v214\n"
"v_lshrrev_b32 v214, 3, v204\n"
"v_lshrrev_b32 v231, 1, v205\n"
"v_cmp_eq_u32 vcc, v214, v231\n"
"v_and_b32 v213, 7, v204\n"
"v_lshl_add_u32 v213, v205, 3, v213\n"
"v_mov_b32 v214, 0x07000000\n"
"v_cndmask_b32 v213, v214, v213, vcc\n"
"v_accvgpr_write_b32 a240, v213\n"
"v_lshlrev_b32 v213, 12, v205\n"
"v_lshl_add_u32 v213, v204, 4, v213\n"
"v_accvgpr_write_b32 a241, v213\n"
"v_lshlrev_b32 v213, 28, v231\n"
"v_lshl_add_u32 v213, v204, 4, v213\n"
"v_lshl_add_u32 v213, v212, 3, v213\n"
"v_accvgpr_write_b32 a242, v213\n"
"v_lshl_add_u32 v213, v206, 2, v205\n"
"s_waitcnt vmcnt(9)\n"
"ds_read_b128 v[48:51], v215 offset:12288\n"
"ds_read_b128 v[52:55], v216 offset:12288\n"
"s_add_u32 m0, s60, 72448\n"
"s_nop 0\n"
"buffer_load_dwordx4 v218, s[8:11], s67 offen offset:256 lds\n"
"s_add_u32 m0, s60, 73344\n"
"s_nop 0\n"
"buffer_load_dwordx4 v217, s[8:11], s67 offen offset:384 lds\n"
"s_waitcnt lgkmcnt(3)\n"
"v_mfma_f32_16x16x32_bf16 v[112:115], v[220:223], v[40:43], v[112:115]\n"
"v_lshl_add_u32 v213, v213, 2, v207\n"
"v_lshlrev_b32 v213, 4, v213\n"
"v_mfma_f32_16x16x32_bf16 v[236:239], v[40:43], v[40:43], v[236:239]\n"
"v_accvgpr_write_b32 a248, v213\n"
"s_lshl_b32 s64, s52, 4\n"
"s_waitcnt lgkmcnt(2)\n"
"v_mfma_f32_16x16x32_bf16 v[112:115], v[220:223], v[44:47], v[112:115]\n"
"s_add_u32 s64, s64, s53\n"
"s_lshl_b32 s77, s64, 19\n"
"v_mfma_f32_16x16x32_bf16 v[236:239], v[44:47], v[44:47], v[236:239]\n"
"s_mul_i32 s61, s54, 81920\n"
"s_add_u32 s77, s77, s61\n"
"s_add_u32 s77, s77, 4194304\n"
"s_xor_b32 s78, s77, 524288\n"
"v_mul_u32_u24 v213, 57, v187\n"
"v_lshrrev_b32 v213, 9, v213\n"
"v_add_u32 v213, v213, v187\n"
"s_mul_i32 s64, s54, 760\n"
"s_add_u32 s64, s64, 147456\n"
"v_lshl_add_u32 v213, v213, 2, s64\n"
"s_add_u32 s64, s64, 24\n"
"v_cmp_gt_u32 vcc, 54, v187\n"
"v_lshl_add_u32 v214, v187, 2, s64\n"
"v_mov_b32 v231, 163328\n"
"v_cndmask_b32 v233, v214, v213, vcc\n"
"v_lshlrev_b32 v214, 2, v187\n"
"v_cndmask_b32 v210, v232, v214, vcc\n"
"v_cndmask_b32 v232, v231, v213, vcc\n"
"v_lshrrev_b32 v213, 1, v207\n"
"v_mul_u32_u24 v213, 18, v213\n"
"v_sub_u32 v208, v209, v213\n"
"v_mul_u32_u24 v211, 760, v207\n"
"v_add_u32 v211, 147456, v211\n"
"v_lshl_add_u32 v213, v206, 4, v204\n"
"v_lshlrev_b32 v213, 2, v213\n"
"v_mov_b32 v209, 0\n"
"s_lshl_b32 s61, s53, 1\n"
"s_add_u32 s61, s61, s59\n"
"s_mul_i32 s61, s61, 648\n"
"s_waitcnt vmcnt(9)\n"
"ds_read_b128 a[172:175], v215 offset:14336\n"
"ds_read_b128 a[176:179], v216 offset:14336\n"
"s_add_u32 m0, s60, 74368\n"
"s_nop 0\n"
"buffer_load_dwordx4 v218, s[8:11], s67 offen offset:384 lds\n"
"s_add_u32 m0, s60, 75264\n"
"s_nop 0\n"
"buffer_load_dwordx4 v217, s[8:11], s67 offen offset:512 lds\n"
"s_waitcnt lgkmcnt(3)\n"
"v_mfma_f32_16x16x32_bf16 v[112:115], v[220:223], v[48:51], v[112:115]\n"
"v_mfma_f32_16x16x32_bf16 v[236:239], v[48:51], v[48:51], v[236:239]\n"
"s_waitcnt lgkmcnt(2)\n"
"v_mfma_f32_16x16x32_bf16 v[112:115], v[220:223], v[52:55], v[112:115]\n"
"v_mfma_f32_16x16x32_bf16 v[236:239], v[52:55], v[52:55], v[236:239]\n"
"s_waitcnt vmcnt(9)\n"
"ds_read_b128 a[180:183], v215 offset:16384\n"
"ds_read_b128 a[184:187], v216 offset:16384\n"
"s_add_u32 m0, s60, 76288\n"
"s_nop 0\n"
"buffer_load_dwordx4 v218, s[8:11], s67 offen offset:512 lds\n"
"s_add_u32 m0, s60, 48512\n"
"s_nop 0\n"
"buffer_load_dwordx4 v217, s[8:11], s67 offen offset:640 lds\n"
"s_waitcnt lgkmcnt(3)\n"
"v_mfma_f32_16x16x32_bf16 v[112:115], v[220:223], a[172:175], v[112:115]\n"
"v_mfma_f32_16x16x32_bf16 v[236:239], a[172:175], a[172:175], v[236:239]\n"
"s_waitcnt lgkmcnt(2)\n"
"v_mfma_f32_16x16x32_bf16 v[112:115], v[220:223], a[176:179], v[112:115]\n"
"v_mfma_f32_16x16x32_bf16 v[236:239], a[176:179], a[176:179], v[236:239]\n"
"s_waitcnt vmcnt(9)\n"
"ds_read_b128 v[56:59], v215 offset:18432\n"
"ds_read_b128 v[60:63], v216 offset:18432\n"
"s_add_u32 m0, s60, 49536\n"
"s_nop 0\n"
"buffer_load_dwordx4 v218, s[8:11], s67 offen offset:640 lds\n"
"s_add_u32 m0, s60, 50432\n"
"s_nop 0\n"
"buffer_load_dwordx4 v217, s[8:11], s67 offen offset:768 lds\n"
"s_waitcnt lgkmcnt(3)\n"
"v_mfma_f32_16x16x32_bf16 v[112:115], v[220:223], a[180:183], v[112:115]\n"
"v_mfma_f32_16x16x32_bf16 v[236:239], a[180:183], a[180:183], v[236:239]\n"
"s_waitcnt lgkmcnt(2)\n"
"v_mfma_f32_16x16x32_bf16 v[112:115], v[220:223], a[184:187], v[112:115]\n"
"v_mfma_f32_16x16x32_bf16 v[236:239], a[184:187], a[184:187], v[236:239]\n"
"s_waitcnt vmcnt(9)\n"
"ds_read_b128 v[64:67], v215 offset:20480\n"
"ds_read_b128 v[68:71], v216 offset:20480\n"
"s_add_u32 m0, s60, 51456\n"
"s_nop 0\n"
"buffer_load_dwordx4 v218, s[8:11], s67 offen offset:768 lds\n"
"s_add_u32 m0, s60, 52352\n"
"s_nop 0\n"
"buffer_load_dwordx4 v217, s[8:11], s67 offen offset:896 lds\n"
"s_waitcnt lgkmcnt(3)\n"
"v_mfma_f32_16x16x32_bf16 v[116:119], v[220:223], v[56:59], 0\n"
"v_mfma_f32_16x16x32_bf16 v[240:243], v[56:59], v[56:59], 0\n"
"s_waitcnt lgkmcnt(2)\n"
"v_mfma_f32_16x16x32_bf16 v[116:119], v[220:223], v[60:63], v[116:119]\n"
"v_mfma_f32_16x16x32_bf16 v[240:243], v[60:63], v[60:63], v[240:243]\n"
"v_mul_f32 v112, 0xbae38e39, v112\n"
"v_mul_f32 v113, 0xbae38e39, v113\n"
"v_mul_f32 v114, 0xbae38e39, v114\n"
"v_mul_f32 v115, 0xbae38e39, v115\n"
"s_waitcnt vmcnt(9)\n"
"ds_read_b128 v[72:75], v215 offset:22528\n"
"ds_read_b128 v[76:79], v216 offset:22528\n"
"s_add_u32 m0, s60, 53376\n"
"s_nop 0\n"
"buffer_load_dwordx4 v218, s[8:11], s67 offen offset:896 lds\n"
"s_add_u32 m0, s60, 54272\n"
"s_nop 0\n"
"buffer_load_dwordx4 v217, s[8:11], s67 offen offset:1024 lds\n"
"v_mfma_f32_4x4x4_16b_bf16 v[128:131], v[224:225], v[0:1], v[112:115]\n"
"100:\n"
"s_cbranch_execz 104f\n"
"v_mfma_f32_4x4x4_16b_bf16 v[132:135], v[224:225], v[2:3], v[112:115]\n"
"v_mfma_f32_4x4x4_16b_bf16 v[136:139], v[224:225], v[4:5], v[112:115]\n"
"v_mfma_f32_4x4x4_16b_bf16 v[140:143], v[224:225], v[6:7], v[112:115]\n"
"s_nop 4\n"
"v_cvt_pk_bf16_f32 v0, v128, v129\n"
"v_cvt_pk_bf16_f32 v1, v130, v131\n"
"v_mfma_f32_4x4x4_16b_bf16 v[144:147], v[224:225], v[8:9], v[112:115]\n"
"v_cvt_pk_bf16_f32 v2, v132, v133\n"
"v_cvt_pk_bf16_f32 v3, v134, v135\n"
"s_waitcnt lgkmcnt(3)\n"
"v_mfma_f32_16x16x32_bf16 v[116:119], v[220:223], v[64:67], v[116:119]\n"
"v_mfma_f32_16x16x32_bf16 v[240:243], v[64:67], v[64:67], v[240:243]\n"
"s_waitcnt lgkmcnt(2)\n"
"v_mfma_f32_16x16x32_bf16 v[116:119], v[220:223], v[68:71], v[116:119]\n"
"v_mfma_f32_16x16x32_bf16 v[240:243], v[68:71], v[68:71], v[240:243]\n"
"s_waitcnt vmcnt(9)\n"
"ds_read_b128 v[80:83], v215 offset:24576\n"
"ds_read_b128 v[84:87], v216 offset:24576\n"
"s_add_u32 m0, s60, 55296\n"
"s_nop 0\n"
"buffer_load_dwordx4 v218, s[8:11], s67 offen offset:1024 lds\n"
"buffer_load_dwordx2 v[226:227], off, s[44:47], 0 sc1\n"
"v_mfma_f32_4x4x4_16b_bf16 v[148:151], v[224:225], v[10:11], v[112:115]\n"
"v_cvt_pk_bf16_f32 v4, v136, v137\n"
"v_cvt_pk_bf16_f32 v5, v138, v139\n"
"v_mfma_f32_4x4x4_16b_bf16 v[152:155], v[224:225], v[12:13], v[112:115]\n"
"v_cvt_pk_bf16_f32 v6, v140, v141\n"
"v_cvt_pk_bf16_f32 v7, v142, v143\n"
"v_mfma_f32_4x4x4_16b_bf16 v[156:159], v[224:225], v[14:15], v[112:115]\n"
"v_cvt_pk_bf16_f32 v8, v144, v145\n"
"v_cvt_pk_bf16_f32 v9, v146, v147\n"
"v_mfma_f32_4x4x4_16b_bf16 v[128:131], v[224:225], v[16:17], v[112:115]\n"
"v_cvt_pk_bf16_f32 v10, v148, v149\n"
"v_cvt_pk_bf16_f32 v11, v150, v151\n"
"v_mfma_f32_4x4x4_16b_bf16 v[132:135], v[224:225], v[18:19], v[112:115]\n"
"v_cvt_pk_bf16_f32 v12, v152, v153\n"
"v_cvt_pk_bf16_f32 v13, v154, v155\n"
"s_waitcnt lgkmcnt(3)\n"
"v_mfma_f32_16x16x32_bf16 v[116:119], v[220:223], v[72:75], v[116:119]\n"
"v_mfma_f32_16x16x32_bf16 v[240:243], v[72:75], v[72:75], v[240:243]\n"
"s_waitcnt lgkmcnt(2)\n"
"v_mfma_f32_16x16x32_bf16 v[116:119], v[220:223], v[76:79], v[116:119]\n"
"v_mfma_f32_16x16x32_bf16 v[240:243], v[76:79], v[76:79], v[240:243]\n"
"s_waitcnt vmcnt(9)\n"
"ds_read_b128 v[88:91], v215 offset:26624\n"
"ds_read_b128 v[92:95], v216 offset:26624\n"
"s_mul_i32 s64, s52, 9\n"
"s_add_u32 s64, s64, s54\n"
"s_add_u32 s64, s64, 0\n"
"s_lshr_b32 s65, s64, 3\n"
"s_lshl_b32 s65, s65, 2\n"
"s_and_b32 s56, s64, 3\n"
"s_add_u32 s65, s65, s56\n"
"s_lshl_b32 s65, s65, 1\n"
"s_bfe_u32 s64, s64, 0x10002\n"
"s_add_u32 s65, s65, s64\n"
"s_mul_i32 s65, s65, 13824\n"
"buffer_load_dwordx4 v[244:247], v181, s[12:15], s65 offen\n"
"s_mul_i32 s64, s52, 9\n"
"s_add_u32 s64, s64, s54\n"
"s_add_u32 s64, s64, 4\n"
"s_lshr_b32 s65, s64, 3\n"
"s_lshl_b32 s65, s65, 2\n"
"s_and_b32 s56, s64, 3\n"
"s_add_u32 s65, s65, s56\n"
"s_lshl_b32 s65, s65, 1\n"
"s_bfe_u32 s64, s64, 0x10002\n"
"s_add_u32 s65, s65, s64\n"
"s_mul_i32 s65, s65, 13824\n"
"buffer_load_dwordx4 v[248:251], v181, s[12:15], s65 offen\n"
"v_mfma_f32_4x4x4_16b_bf16 v[136:139], v[224:225], v[20:21], v[112:115]\n"
"v_cvt_pk_bf16_f32 v14, v156, v157\n"
"v_cvt_pk_bf16_f32 v15, v158, v159\n"
"v_mfma_f32_4x4x4_16b_bf16 v[140:143], v[224:225], v[22:23], v[112:115]\n"
"v_cvt_pk_bf16_f32 v16, v128, v129\n"
"v_cvt_pk_bf16_f32 v17, v130, v131\n"
"v_mfma_f32_4x4x4_16b_bf16 v[144:147], v[224:225], v[24:25], v[112:115]\n"
"v_cvt_pk_bf16_f32 v18, v132, v133\n"
"v_cvt_pk_bf16_f32 v19, v134, v135\n"
"v_mfma_f32_4x4x4_16b_bf16 v[148:151], v[224:225], v[26:27], v[112:115]\n"
"v_cvt_pk_bf16_f32 v20, v136, v137\n"
"v_cvt_pk_bf16_f32 v21, v138, v139\n"
"v_mfma_f32_4x4x4_16b_bf16 v[152:155], v[224:225], v[28:29], v[112:115]\n"
"v_cvt_pk_bf16_f32 v22, v140, v141\n"
"v_cvt_pk_bf16_f32 v23, v142, v143\n"
"s_waitcnt lgkmcnt(3)\n"
"v_mfma_f32_16x16x32_bf16 v[116:119], v[220:223], v[80:83], v[116:119]\n"
"v_mfma_f32_16x16x32_bf16 v[240:243], v[80:83], v[80:83], v[240:243]\n"
"s_waitcnt lgkmcnt(2)\n"
"v_mfma_f32_16x16x32_bf16 v[116:119], v[220:223], v[84:87], v[116:119]\n"
"v_mfma_f32_16x16x32_bf16 v[240:243], v[84:87], v[84:87], v[240:243]\n"
"s_waitcnt vmcnt(9)\n"
"ds_read_b128 v[96:99], v215 offset:0\n"
"ds_read_b128 v[100:103], v216 offset:0\n"
"s_mul_i32 s64, s52, 9\n"
"s_add_u32 s64, s64, s54\n"
"s_add_u32 s64, s64, 8\n"
"s_lshr_b32 s65, s64, 3\n"
"s_lshl_b32 s65, s65, 2\n"
"s_and_b32 s56, s64, 3\n"
"s_add_u32 s65, s65, s56\n"
"s_lshl_b32 s65, s65, 1\n"
"s_bfe_u32 s64, s64, 0x10002\n"
"s_add_u32 s65, s65, s64\n"
"s_mul_i32 s65, s65, 13824\n"
"s_lshl_b32 s64, s54, 28\n"
"v_or_b32 v181, s64, v181\n"
"buffer_load_dwordx4 v[252:255], v181, s[12:15], s65 offen\n"
"s_add_u32 m0, s55, 0\n"
"s_mov_b32 s56, 0\n"
"buffer_load_dwordx4 v180, s[24:27], s56 offen lds\n"
"v_mfma_f32_4x4x4_16b_bf16 v[156:159], v[224:225], v[30:31], v[112:115]\n"
"v_cvt_pk_bf16_f32 v24, v144, v145\n"
"v_cvt_pk_bf16_f32 v25, v146, v147\n"
"v_mfma_f32_4x4x4_16b_bf16 v[128:131], v[224:225], v[32:33], v[112:115]\n"
"v_cvt_pk_bf16_f32 v26, v148, v149\n"
"v_cvt_pk_bf16_f32 v27, v150, v151\n"
"v_mfma_f32_4x4x4_16b_bf16 v[132:135], v[224:225], v[34:35], v[112:115]\n"
"v_cvt_pk_bf16_f32 v28, v152, v153\n"
"v_cvt_pk_bf16_f32 v29, v154, v155\n"
"v_mfma_f32_4x4x4_16b_bf16 v[136:139], v[224:225], v[36:37], v[112:115]\n"
"v_cvt_pk_bf16_f32 v30, v156, v157\n"
"v_cvt_pk_bf16_f32 v31, v158, v159\n"
"v_mfma_f32_4x4x4_16b_bf16 v[140:143], v[224:225], v[38:39], v[112:115]\n"
"v_cvt_pk_bf16_f32 v32, v128, v129\n"
"v_cvt_pk_bf16_f32 v33, v130, v131\n"
"s_waitcnt lgkmcnt(3)\n"
"v_mfma_f32_16x16x32_bf16 v[116:119], v[220:223], v[88:91], v[116:119]\n"
"v_mfma_f32_16x16x32_bf16 v[240:243], v[88:91], v[88:91], v[240:243]\n"
"s_waitcnt lgkmcnt(2)\n"
"v_mfma_f32_16x16x32_bf16 v[116:119], v[220:223], v[92:95], v[116:119]\n"
"v_mfma_f32_16x16x32_bf16 v[240:243], v[92:95], v[92:95], v[240:243]\n"
"s_waitcnt vmcnt(9)\n"
"ds_read_b128 v[104:107], v215 offset:2048\n"
"ds_read_b128 v[108:111], v216 offset:2048\n"
"s_addk_i32 m0, 4096\n"
"s_add_u32 s56, s56, s62\n"
"buffer_load_dwordx4 v180, s[24:27], s56 offen lds\n"
"s_addk_i32 m0, 4096\n"
"s_add_u32 s56, s56, s62\n"
"buffer_load_dwordx4 v180, s[24:27], s56 offen lds\n"
"v_mfma_f32_4x4x4_16b_bf16 v[144:147], v[224:225], v[40:41], v[112:115]\n"
"v_cvt_pk_bf16_f32 v34, v132, v133\n"
"v_cvt_pk_bf16_f32 v35, v134, v135\n"
"v_mfma_f32_4x4x4_16b_bf16 v[148:151], v[224:225], v[42:43], v[112:115]\n"
"v_cvt_pk_bf16_f32 v36, v136, v137\n"
"v_cvt_pk_bf16_f32 v37, v138, v139\n"
"v_mfma_f32_4x4x4_16b_bf16 v[152:155], v[224:225], v[44:45], v[112:115]\n"
"v_cvt_pk_bf16_f32 v38, v140, v141\n"
"v_cvt_pk_bf16_f32 v39, v142, v143\n"
"v_mfma_f32_4x4x4_16b_bf16 v[156:159], v[224:225], v[46:47], v[112:115]\n"
"v_cvt_pk_bf16_f32 v40, v144, v145\n"
"v_cvt_pk_bf16_f32 v41, v146, v147\n"
"v_mfma_f32_4x4x4_16b_bf16 v[128:131], v[224:225], v[48:49], v[112:115]\n"
"v_cvt_pk_bf16_f32 v42, v148, v149\n"
"v_cvt_pk_bf16_f32 v43, v150, v151\n"
"s_waitcnt lgkmcnt(3)\n"
"v_mfma_f32_16x16x32_bf16 v[116:119], v[220:223], v[96:99], v[116:119]\n"
"v_mfma_f32_16x16x32_bf16 v[240:243], v[96:99], v[96:99], v[240:243]\n"
"s_waitcnt lgkmcnt(2)\n"
"v_mfma_f32_16x16x32_bf16 v[116:119], v[220:223], v[100:103], v[116:119]\n"
"v_mfma_f32_16x16x32_bf16 v[240:243], v[100:103], v[100:103], v[240:243]\n"
"s_waitcnt vmcnt(9)\n"
"ds_read_b128 a[188:191], v215 offset:4096\n"
"ds_read_b128 a[192:195], v216 offset:4096\n"
"s_addk_i32 m0, 4096\n"
"s_add_u32 s56, s56, s62\n"
"buffer_load_dwordx4 v180, s[24:27], s56 offen lds\n"
"s_addk_i32 m0, 4096\n"
"s_add_u32 s56, s56, s62\n"
"buffer_load_dwordx4 v180, s[24:27], s56 offen lds\n"
"v_mfma_f32_4x4x4_16b_bf16 v[132:135], v[224:225], v[50:51], v[112:115]\n"
"v_cvt_pk_bf16_f32 v44, v152, v153\n"
"v_cvt_pk_bf16_f32 v45, v154, v155\n"
"v_mfma_f32_4x4x4_16b_bf16 v[136:139], v[224:225], v[52:53], v[112:115]\n"
"v_cvt_pk_bf16_f32 v46, v156, v157\n"
"v_cvt_pk_bf16_f32 v47, v158, v159\n"
"v_mfma_f32_4x4x4_16b_bf16 v[140:143], v[224:225], v[54:55], v[112:115]\n"
"v_cvt_pk_bf16_f32 v48, v128, v129\n"
"v_cvt_pk_bf16_f32 v49, v130, v131\n"
"v_mfma_f32_4x4x4_16b_bf16 v[144:147], v[224:225], a[172:173], v[112:115]\n"
"v_cvt_pk_bf16_f32 v50, v132, v133\n"
"v_cvt_pk_bf16_f32 v51, v134, v135\n"
"v_mfma_f32_4x4x4_16b_bf16 v[148:151], v[224:225], a[174:175], v[112:115]\n"
"v_cvt_pk_bf16_f32 v52, v136, v137\n"
"v_cvt_pk_bf16_f32 v53, v138, v139\n"
"s_waitcnt lgkmcnt(3)\n"
"v_mfma_f32_16x16x32_bf16 v[116:119], v[220:223], v[104:107], v[116:119]\n"
"v_mfma_f32_16x16x32_bf16 v[240:243], v[104:107], v[104:107], v[240:243]\n"
"s_waitcnt lgkmcnt(2)\n"
"v_mfma_f32_16x16x32_bf16 v[116:119], v[220:223], v[108:111], v[116:119]\n"
"v_mfma_f32_16x16x32_bf16 v[240:243], v[108:111], v[108:111], v[240:243]\n"
"s_waitcnt vmcnt(9)\n"
"ds_read_b128 a[196:199], v215 offset:6144\n"
"ds_read_b128 a[200:203], v216 offset:6144\n"
"s_addk_i32 m0, 4096\n"
"s_add_u32 s56, s56, s62\n"
"buffer_load_dwordx4 v180, s[24:27], s56 offen lds\n"
"buffer_load_dword v228, v210, s[16:19], s61 offen\n"
"v_mfma_f32_4x4x4_16b_bf16 v[152:155], v[224:225], a[176:177], v[112:115]\n"
"v_cvt_pk_bf16_f32 v54, v140, v141\n"
"v_cvt_pk_bf16_f32 v55, v142, v143\n"
"v_mfma_f32_4x4x4_16b_bf16 v[156:159], v[224:225], a[178:179], v[112:115]\n"
"v_cvt_pk_bf16_f32 v144, v144, v145\n"
"v_cvt_pk_bf16_f32 v145, v146, v147\n"
"v_accvgpr_write_b32 a172, v144\n"
"v_accvgpr_write_b32 a173, v145\n"
"v_mfma_f32_4x4x4_16b_bf16 v[128:131], v[224:225], a[180:181], v[112:115]\n"
"v_cvt_pk_bf16_f32 v148, v148, v149\n"
"v_cvt_pk_bf16_f32 v149, v150, v151\n"
"v_accvgpr_write_b32 a174, v148\n"
"v_accvgpr_write_b32 a175, v149\n"
"v_mfma_f32_4x4x4_16b_bf16 v[132:135], v[224:225], a[182:183], v[112:115]\n"
"v_cvt_pk_bf16_f32 v152, v152, v153\n"
"v_cvt_pk_bf16_f32 v153, v154, v155\n"
"v_accvgpr_write_b32 a176, v152\n"
"v_accvgpr_write_b32 a177, v153\n"
"v_mfma_f32_4x4x4_16b_bf16 v[136:139], v[224:225], a[184:185], v[112:115]\n"
"v_cvt_pk_bf16_f32 v156, v156, v157\n"
"v_cvt_pk_bf16_f32 v157, v158, v159\n"
"v_accvgpr_write_b32 a178, v156\n"
"v_accvgpr_write_b32 a179, v157\n"
"s_waitcnt lgkmcnt(3)\n"
"v_mfma_f32_16x16x32_bf16 v[116:119], v[220:223], a[188:191], v[116:119]\n"
"v_mfma_f32_16x16x32_bf16 v[240:243], a[188:191], a[188:191], v[240:243]\n"
"s_waitcnt lgkmcnt(2)\n"
"v_mfma_f32_16x16x32_bf16 v[116:119], v[220:223], a[192:195], v[116:119]\n"
"v_mfma_f32_16x16x32_bf16 v[240:243], a[192:195], a[192:195], v[240:243]\n"
"s_waitcnt lgkmcnt(0)\n"
"s_barrier\n"
"v_mfma_f32_16x16x32_bf16 v[116:119], v[220:223], a[196:199], v[116:119]\n"
"v_mfma_f32_16x16x32_bf16 v[240:243], a[196:199], a[196:199], v[240:243]\n"
"v_mfma_f32_16x16x32_bf16 v[116:119], v[220:223], a[200:203], v[116:119]\n"
"v_mfma_f32_16x16x32_bf16 v[240:243], a[200:203], a[200:203], v[240:243]\n"
"buffer_load_dword v229, v210, s[16:19], s61 offen offset:216\n"
"buffer_load_dword v230, v210, s[16:19], s61 offen offset:432\n"
"v_mfma_f32_4x4x4_16b_bf16 v[140:143], v[224:225], a[186:187], v[112:115]\n"
"v_cvt_pk_bf16_f32 v128, v128, v129\n"
"v_cvt_pk_bf16_f32 v129, v130, v131\n"
"v_accvgpr_write_b32 a180, v128\n"
"v_accvgpr_write_b32 a181, v129\n"
"v_cvt_pk_bf16_f32 v132, v132, v133\n"
"v_cvt_pk_bf16_f32 v133, v134, v135\n"
"v_accvgpr_write_b32 a182, v132\n"
"v_accvgpr_write_b32 a183, v133\n"
"v_cvt_pk_bf16_f32 v136, v136, v137\n"
"v_cvt_pk_bf16_f32 v137, v138, v139\n"
"v_accvgpr_write_b32 a184, v136\n"
"v_accvgpr_write_b32 a185, v137\n"
"v_cvt_pk_bf16_f32 v140, v140, v141\n"
"v_cvt_pk_bf16_f32 v141, v142, v143\n"
"v_accvgpr_write_b32 a186, v140\n"
"v_accvgpr_write_b32 a187, v141\n"
"v_mul_f32 v116, 0xbae38e39, v116\n"
"v_mul_f32 v117, 0xbae38e39, v117\n"
"v_mul_f32 v118, 0xbae38e39, v118\n"
"v_mul_f32 v119, 0xbae38e39, v119\n"
"s_nop 1\n"
"v_mfma_f32_4x4x4_16b_bf16 v[144:147], v[224:225], v[56:57], v[116:119]\n"
"s_mul_i32 s6, s53, 147456\n"
"v_mfma_f32_4x4x4_16b_bf16 v[148:151], v[224:225], v[58:59], v[116:119]\n"
"s_add_u32 s6, s6, s55\n"
"v_mfma_f32_4x4x4_16b_bf16 v[152:155], v[224:225], v[60:61], v[116:119]\n"
"s_add_u32 s6, s6, 1703936\n"
"v_mfma_f32_4x4x4_16b_bf16 v[156:159], v[224:225], v[62:63], v[116:119]\n"
"s_mul_i32 s79, s52, 9216\n"
"s_nop 0\n"
"v_cvt_pk_bf16_f32 v56, v144, v145\n"
"v_cvt_pk_bf16_f32 v57, v146, v147\n"
"v_mfma_f32_4x4x4_16b_bf16 v[128:131], v[224:225], v[64:65], v[116:119]\n"
"s_add_u32 s79, s79, s6\n"
"v_cvt_pk_bf16_f32 v58, v148, v149\n"
"v_cvt_pk_bf16_f32 v59, v150, v151\n"
"v_mfma_f32_4x4x4_16b_bf16 v[132:135], v[224:225], v[66:67], v[116:119]\n"
"s_lshl_b32 s80, s54, 28\n"
"v_cvt_pk_bf16_f32 v60, v152, v153\n"
"v_cvt_pk_bf16_f32 v61, v154, v155\n"
"v_mfma_f32_4x4x4_16b_bf16 v[136:139], v[224:225], v[68:69], v[116:119]\n"
"v_or_b32 v181, s80, v188\n"
"v_cvt_pk_bf16_f32 v62, v156, v157\n"
"v_cvt_pk_bf16_f32 v63, v158, v159\n"
"v_mfma_f32_4x4x4_16b_bf16 v[140:143], v[224:225], v[70:71], v[116:119]\n"
"s_waitcnt vmcnt(11)\n"
"buffer_store_dwordx4 v[244:247], v188, s[44:47], s79 offen sc1\n"
"v_cvt_pk_bf16_f32 v64, v128, v129\n"
"v_cvt_pk_bf16_f32 v65, v130, v131\n"
"v_mfma_f32_4x4x4_16b_bf16 v[144:147], v[224:225], v[72:73], v[116:119]\n"
"s_addk_i32 s79, 4096\n"
"v_cvt_pk_bf16_f32 v66, v132, v133\n"
"v_cvt_pk_bf16_f32 v67, v134, v135\n"
"v_mfma_f32_4x4x4_16b_bf16 v[148:151], v[224:225], v[74:75], v[116:119]\n"
"s_waitcnt vmcnt(11)\n"
"buffer_store_dwordx4 v[248:251], v188, s[44:47], s79 offen sc1\n"
"v_cvt_pk_bf16_f32 v68, v136, v137\n"
"v_cvt_pk_bf16_f32 v69, v138, v139\n"
"v_mfma_f32_4x4x4_16b_bf16 v[152:155], v[224:225], v[76:77], v[116:119]\n"
"s_addk_i32 s79, 4096\n"
"v_cvt_pk_bf16_f32 v70, v140, v141\n"
"v_cvt_pk_bf16_f32 v71, v142, v143\n"
"v_mfma_f32_4x4x4_16b_bf16 v[156:159], v[224:225], v[78:79], v[116:119]\n"
"s_waitcnt vmcnt(11)\n"
"buffer_store_dwordx4 v[252:255], v181, s[44:47], s79 offen sc1\n"
"v_cvt_pk_bf16_f32 v72, v144, v145\n"
"v_cvt_pk_bf16_f32 v73, v146, v147\n"
"v_mfma_f32_4x4x4_16b_bf16 v[128:131], v[224:225], v[80:81], v[116:119]\n"
"s_addk_i32 m0, 4096\n"
"v_cvt_pk_bf16_f32 v74, v148, v149\n"
"v_cvt_pk_bf16_f32 v75, v150, v151\n"
"v_mfma_f32_4x4x4_16b_bf16 v[132:135], v[224:225], v[82:83], v[116:119]\n"
"s_mov_b32 s56, 1769472\n"
"v_cvt_pk_bf16_f32 v76, v152, v153\n"
"v_cvt_pk_bf16_f32 v77, v154, v155\n"
"v_mfma_f32_4x4x4_16b_bf16 v[136:139], v[224:225], v[84:85], v[116:119]\n"
"buffer_load_dwordx4 v180, s[24:27], s56 offen lds\n"
"v_cvt_pk_bf16_f32 v78, v156, v157\n"
"v_cvt_pk_bf16_f32 v79, v158, v159\n"
"v_mfma_f32_4x4x4_16b_bf16 v[140:143], v[224:225], v[86:87], v[116:119]\n"
"s_addk_i32 m0, 4096\n"
"v_cvt_pk_bf16_f32 v80, v128, v129\n"
"v_cvt_pk_bf16_f32 v81, v130, v131\n"
"v_mfma_f32_4x4x4_16b_bf16 v[144:147], v[224:225], v[88:89], v[116:119]\n"
"s_add_u32 s56, s56, s62\n"
"v_cvt_pk_bf16_f32 v82, v132, v133\n"
"v_cvt_pk_bf16_f32 v83, v134, v135\n"
"v_mfma_f32_4x4x4_16b_bf16 v[148:151], v[224:225], v[90:91], v[116:119]\n"
"buffer_load_dwordx4 v180, s[24:27], s56 offen lds\n"
"v_cvt_pk_bf16_f32 v84, v136, v137\n"
"v_cvt_pk_bf16_f32 v85, v138, v139\n"
"v_mfma_f32_4x4x4_16b_bf16 v[152:155], v[224:225], v[92:93], v[116:119]\n"
"s_addk_i32 m0, 4096\n"
"v_cvt_pk_bf16_f32 v86, v140, v141\n"
"v_cvt_pk_bf16_f32 v87, v142, v143\n"
"v_mfma_f32_4x4x4_16b_bf16 v[156:159], v[224:225], v[94:95], v[116:119]\n"
"s_add_u32 s56, s56, s62\n"
"v_cvt_pk_bf16_f32 v88, v144, v145\n"
"v_cvt_pk_bf16_f32 v89, v146, v147\n"
"v_mfma_f32_4x4x4_16b_bf16 v[128:131], v[224:225], v[96:97], v[116:119]\n"
"buffer_load_dwordx4 v180, s[24:27], s56 offen lds\n"
"v_cvt_pk_bf16_f32 v90, v148, v149\n"
"v_cvt_pk_bf16_f32 v91, v150, v151\n"
"v_mfma_f32_4x4x4_16b_bf16 v[132:135], v[224:225], v[98:99], v[116:119]\n"
"s_addk_i32 m0, 4096\n"
"v_cvt_pk_bf16_f32 v92, v152, v153\n"
"v_cvt_pk_bf16_f32 v93, v154, v155\n"
"v_mfma_f32_4x4x4_16b_bf16 v[136:139], v[224:225], v[100:101], v[116:119]\n"
"s_add_u32 s56, s56, s62\n"
"v_cvt_pk_bf16_f32 v94, v156, v157\n"
"v_cvt_pk_bf16_f32 v95, v158, v159\n"
"v_mfma_f32_4x4x4_16b_bf16 v[140:143], v[224:225], v[102:103], v[116:119]\n"
"buffer_load_dwordx4 v180, s[24:27], s56 offen lds\n"
"v_cvt_pk_bf16_f32 v96, v128, v129\n"
"v_cvt_pk_bf16_f32 v97, v130, v131\n"
"v_mfma_f32_4x4x4_16b_bf16 v[144:147], v[224:225], v[104:105], v[116:119]\n"
"s_addk_i32 m0, 4096\n"
"v_cvt_pk_bf16_f32 v98, v132, v133\n"
"v_cvt_pk_bf16_f32 v99, v134, v135\n"
"v_mfma_f32_4x4x4_16b_bf16 v[148:151], v[224:225], v[106:107], v[116:119]\n"
"s_add_u32 s56, s56, s62\n"
"v_cvt_pk_bf16_f32 v100, v136, v137\n"
"v_cvt_pk_bf16_f32 v101, v138, v139\n"
"v_mfma_f32_4x4x4_16b_bf16 v[152:155], v[224:225], v[108:109], v[116:119]\n"
"buffer_load_dwordx4 v180, s[24:27], s56 offen lds\n"
"v_cvt_pk_bf16_f32 v102, v140, v141\n"
"v_cvt_pk_bf16_f32 v103, v142, v143\n"
"v_mfma_f32_4x4x4_16b_bf16 v[156:159], v[224:225], v[110:111], v[116:119]\n"
"s_addk_i32 m0, 4096\n"
"v_cvt_pk_bf16_f32 v104, v144, v145\n"
"v_cvt_pk_bf16_f32 v105, v146, v147\n"
"v_mfma_f32_4x4x4_16b_bf16 v[128:131], v[224:225], a[188:189], v[116:119]\n"
"s_add_u32 s56, s56, s62\n"
"v_cvt_pk_bf16_f32 v106, v148, v149\n"
"v_cvt_pk_bf16_f32 v107, v150, v151\n"
"v_mfma_f32_4x4x4_16b_bf16 v[132:135], v[224:225], a[190:191], v[116:119]\n"
"buffer_load_dwordx4 v180, s[24:27], s56 offen lds\n"
"v_cvt_pk_bf16_f32 v108, v152, v153\n"
"v_cvt_pk_bf16_f32 v109, v154, v155\n"
"v_mfma_f32_4x4x4_16b_bf16 v[136:139], v[224:225], a[192:193], v[116:119]\n"
"s_addk_i32 m0, 4096\n"
"v_cvt_pk_bf16_f32 v110, v156, v157\n"
"v_cvt_pk_bf16_f32 v111, v158, v159\n"
"v_mfma_f32_4x4x4_16b_bf16 v[140:143], v[224:225], a[194:195], v[116:119]\n"
"s_mov_b32 s56, 3538944\n"
"v_cvt_pk_bf16_f32 v128, v128, v129\n"
"v_cvt_pk_bf16_f32 v129, v130, v131\n"
"v_accvgpr_write_b32 a188, v128\n"
"v_accvgpr_write_b32 a189, v129\n"
"v_mfma_f32_4x4x4_16b_bf16 v[144:147], v[224:225], a[196:197], v[116:119]\n"
"buffer_load_dwordx4 v180, s[24:27], s56 offen lds\n"
"v_cvt_pk_bf16_f32 v132, v132, v133\n"
"v_cvt_pk_bf16_f32 v133, v134, v135\n"
"v_accvgpr_write_b32 a190, v132\n"
"v_accvgpr_write_b32 a191, v133\n"
"v_mfma_f32_4x4x4_16b_bf16 v[148:151], v[224:225], a[198:199], v[116:119]\n"
"s_addk_i32 m0, 4096\n"
"v_cvt_pk_bf16_f32 v136, v136, v137\n"
"v_cvt_pk_bf16_f32 v137, v138, v139\n"
"v_accvgpr_write_b32 a192, v136\n"
"v_accvgpr_write_b32 a193, v137\n"
"v_mfma_f32_4x4x4_16b_bf16 v[152:155], v[224:225], a[200:201], v[116:119]\n"
"s_add_u32 s56, s56, s62\n"
"v_cvt_pk_bf16_f32 v140, v140, v141\n"
"v_cvt_pk_bf16_f32 v141, v142, v143\n"
"v_accvgpr_write_b32 a194, v140\n"
"v_accvgpr_write_b32 a195, v141\n"
"v_mfma_f32_4x4x4_16b_bf16 v[156:159], v[224:225], a[202:203], v[116:119]\n"
"buffer_load_dwordx4 v180, s[24:27], s56 offen lds\n"
"v_cvt_pk_bf16_f32 v144, v144, v145\n"
"v_cvt_pk_bf16_f32 v145, v146, v147\n"
"v_accvgpr_write_b32 a196, v144\n"
"v_accvgpr_write_b32 a197, v145\n"
"v_cvt_pk_bf16_f32 v148, v148, v149\n"
"v_cvt_pk_bf16_f32 v149, v150, v151\n"
"v_accvgpr_write_b32 a198, v148\n"
"v_accvgpr_write_b32 a199, v149\n"
"v_cvt_pk_bf16_f32 v152, v152, v153\n"
"v_cvt_pk_bf16_f32 v153, v154, v155\n"
"v_accvgpr_write_b32 a200, v152\n"
"v_accvgpr_write_b32 a201, v153\n"
"v_cvt_pk_bf16_f32 v156, v156, v157\n"
"v_cvt_pk_bf16_f32 v157, v158, v159\n"
"v_accvgpr_write_b32 a202, v156\n"
"v_accvgpr_write_b32 a203, v157\n"
"s_addk_i32 m0, 4096\n"
"s_add_u32 s56, s56, s62\n"
"buffer_load_dwordx4 v180, s[24:27], s56 offen lds\n"
"s_addk_i32 m0, 4096\n"
"s_add_u32 s56, s56, s62\n"
"buffer_load_dwordx4 v180, s[24:27], s56 offen lds\n"
"s_addk_i32 m0, 4096\n"
"s_add_u32 s56, s56, s62\n"
"buffer_load_dwordx4 v180, s[24:27], s56 offen lds\n"
"s_addk_i32 m0, 4096\n"
"s_add_u32 s56, s56, s62\n"
"buffer_load_dwordx4 v180, s[24:27], s56 offen lds\n"
"v_mul_f32 v185, v112, v112\n"
"v_mul_f32 v186, v116, v116\n"
"s_waitcnt vmcnt(18)\n"
"s_barrier\n"
"ds_read_b64_tr_b16 a[0:1], v176 offset:0\n"
"ds_read_b64_tr_b16 a[2:3], v176 offset:2048\n"
"ds_read_b64_tr_b16 a[4:5], v176 offset:4096\n"
"ds_read_b64_tr_b16 a[6:7], v176 offset:6144\n"
"ds_read_b64_tr_b16 a[8:9], v176 offset:8192\n"
"ds_read_b64_tr_b16 a[10:11], v176 offset:10240\n"
"ds_read_b64_tr_b16 a[12:13], v176 offset:12288\n"
"ds_read_b64_tr_b16 a[14:15], v176 offset:14336\n"
"ds_read_b64_tr_b16 a[16:17], v176 offset:16384\n"
"ds_read_b64_tr_b16 a[18:19], v176 offset:18432\n"
"ds_read_b64_tr_b16 a[20:21], v176 offset:20480\n"
"ds_read_b64_tr_b16 a[22:23], v176 offset:22528\n"
"ds_read_b64_tr_b16 a[24:25], v177 offset:0\n"
"ds_read_b64_tr_b16 a[26:27], v177 offset:2048\n"
"s_waitcnt lgkmcnt(10)\n"
"v_mfma_f32_16x16x32_bf16 v[112:115], a[0:3], v[0:3], 0\n"
"s_nop 0\n"
"ds_read_b64_tr_b16 a[28:29], v177 offset:4096\n"
"ds_read_b64_tr_b16 a[30:31], v177 offset:6144\n"
"v_mfma_f32_16x16x32_bf16 v[112:115], a[4:7], v[4:7], v[112:115]\n"
"s_waitcnt lgkmcnt(8)\n"
"buffer_load_dword v203, v196, s[28:31], s99 offen offset:64\n"
"v_mfma_f32_16x16x32_bf16 v[112:115], a[8:11], v[8:11], v[112:115]\n"
"ds_read_b64_tr_b16 a[32:33], v177 offset:8192\n"
"v_readfirstlane_b32 s64, v226\n"
"v_mfma_f32_16x16x32_bf16 v[112:115], a[12:15], v[12:15], v[112:115]\n"
"s_waitcnt lgkmcnt(5)\n"
"ds_read_b64_tr_b16 a[34:35], v177 offset:10240\n"
"ds_read_b64_tr_b16 a[36:37], v177 offset:12288\n"
"v_mfma_f32_16x16x32_bf16 v[112:115], a[16:19], v[16:19], v[112:115]\n"
"s_nop 0\n"
"buffer_load_dword v203, v196, s[24:27], s99 offen offset:192\n"
"v_mfma_f32_16x16x32_bf16 v[112:115], a[20:23], v[20:23], v[112:115]\n"
"s_addk_i32 m0, 4096\n"
"ds_read_b64_tr_b16 a[38:39], v177 offset:14336\n"
"v_readfirstlane_b32 s65, v227\n"
"v_mfma_f32_16x16x32_bf16 v[116:119], a[0:3], v[56:59], 0\n"
"s_mov_b32 s56, 0\n"
"ds_read_b64_tr_b16 a[40:41], v177 offset:16384\n"
"ds_read_b64_tr_b16 a[42:43], v177 offset:18432\n"
"v_mfma_f32_16x16x32_bf16 v[116:119], a[4:7], v[60:63], v[116:119]\n"
"s_mul_i32 s64, s64, 0x9E3779B1\n"
"buffer_load_dwordx4 v180, s[28:31], s56 offen lds\n"
"v_mfma_f32_16x16x32_bf16 v[116:119], a[8:11], v[64:67], v[116:119]\n"
"s_add_u32 s64, s64, 0x7F4A7C15\n"
"ds_read_b64_tr_b16 a[44:45], v177 offset:20480\n"
"s_mul_i32 s65, s65, 0x85EBCA6B\n"
"v_mfma_f32_16x16x32_bf16 v[116:119], a[12:15], v[68:71], v[116:119]\n"
"s_add_u32 s65, s65, 0x1B873593\n"
"ds_read_b64_tr_b16 a[46:47], v177 offset:22528\n"
"v_mov_b32 v198, s64\n"
"v_mfma_f32_16x16x32_bf16 v[116:119], a[16:19], v[72:75], v[116:119]\n"
"s_addk_i32 m0, 4096\n"
"ds_read_b64_tr_b16 a[48:49], v178 offset:0\n"
"s_lshl_b32 s79, s53, 4\n"
"v_mfma_f32_16x16x32_bf16 v[116:119], a[20:23], v[76:79], v[116:119]\n"
"s_waitcnt lgkmcnt(9)\n"
"ds_read_b64_tr_b16 a[50:51], v178 offset:2048\n"
"s_add_u32 s79, s79, s52\n"
"v_mfma_f32_16x16x32_bf16 v[120:123], a[24:27], v[0:3], 0\n"
"s_add_u32 s56, s56, s62\n"
"ds_read_b64_tr_b16 a[52:53], v178 offset:4096\n"
"s_lshl_b32 s79, s79, 7\n"
"v_mfma_f32_16x16x32_bf16 v[120:123], a[28:31], v[4:7], v[120:123]\n"
"s_waitcnt lgkmcnt(7)\n"
"ds_read_b64_tr_b16 a[54:55], v178 offset:6144\n"
"ds_read_b64_tr_b16 a[56:57], v178 offset:8192\n"
"v_mfma_f32_16x16x32_bf16 v[120:123], a[32:35], v[8:11], v[120:123]\n"
"s_add_u32 s79, s79, 200704\n"
"buffer_load_dwordx4 v180, s[28:31], s56 offen lds\n"
"v_mfma_f32_16x16x32_bf16 v[120:123], a[36:39], v[12:15], v[120:123]\n"
"s_waitcnt lgkmcnt(5)\n"
"ds_read_b64_tr_b16 a[58:59], v178 offset:10240\n"
"s_lshl_b32 s80, s54, 28\n"
"v_mfma_f32_16x16x32_bf16 v[120:123], a[40:43], v[16:19], v[120:123]\n"
"s_addk_i32 m0, 4096\n"
"ds_read_b64_tr_b16 a[0:1], v178 offset:12288\n"
"v_or_b32 v181, s80, v197\n"
"v_mfma_f32_16x16x32_bf16 v[120:123], a[44:47], v[20:23], v[120:123]\n"
"s_add_u32 s56, s56, s62\n"
"ds_read_b64_tr_b16 a[2:3], v178 offset:14336\n"
"v_cmp_eq_u32 vcc, 1, v207\n"
"v_mfma_f32_16x16x32_bf16 v[124:127], a[24:27], v[56:59], 0\n"
"ds_read_b64_tr_b16 a[4:5], v178 offset:16384\n"
"v_cndmask_b32 v236, v236, v237, vcc\n"
"v_mfma_f32_16x16x32_bf16 v[124:127], a[28:31], v[60:63], v[124:127]\n"
"ds_read_b64_tr_b16 a[6:7], v178 offset:18432\n"
"v_cmp_eq_u32 vcc, 2, v207\n"
"v_mfma_f32_16x16x32_bf16 v[124:127], a[32:35], v[64:67], v[124:127]\n"
"ds_read_b64_tr_b16 a[8:9], v178 offset:20480\n"
"v_cndmask_b32 v236, v236, v238, vcc\n"
"v_mfma_f32_16x16x32_bf16 v[124:127], a[36:39], v[68:71], v[124:127]\n"
"ds_read_b64_tr_b16 a[10:11], v178 offset:22528\n"
"v_cmp_eq_u32 vcc, 3, v207\n"
"v_mfma_f32_16x16x32_bf16 v[124:127], a[40:43], v[72:75], v[124:127]\n"
"ds_read_b64_tr_b16 a[12:13], v179 offset:0\n"
"v_cndmask_b32 v236, v236, v239, vcc\n"
"v_mfma_f32_16x16x32_bf16 v[124:127], a[44:47], v[76:79], v[124:127]\n"
"s_waitcnt lgkmcnt(9)\n"
"ds_read_b64_tr_b16 a[14:15], v179 offset:2048\n"
"ds_bpermute_b32 v236, v213, v236\n"
"v_mfma_f32_16x16x32_bf16 v[128:131], a[48:51], v[0:3], 0\n"
"s_nop 0\n"
"ds_read_b64_tr_b16 a[16:17], v179 offset:4096\n"
"ds_read_b64_tr_b16 a[18:19], v179 offset:6144\n"
"v_mfma_f32_16x16x32_bf16 v[128:131], a[52:55], v[4:7], v[128:131]\n"
"s_waitcnt lgkmcnt(9)\n"
"ds_read_b64_tr_b16 a[20:21], v179 offset:8192\n"
"ds_read_b64_tr_b16 a[22:23], v179 offset:10240\n"
"v_mfma_f32_16x16x32_bf16 v[128:131], a[56:59], v[8:11], v[128:131]\n"
"s_nop 0\n"
"ds_read_b64_tr_b16 a[24:25], v179 offset:12288\n"
"ds_read_b64_tr_b16 a[26:27], v179 offset:14336\n"
"v_mfma_f32_16x16x32_bf16 v[128:131], a[0:3], v[12:15], v[128:131]\n"
"s_waitcnt lgkmcnt(9)\n"
"ds_read_b64_tr_b16 a[28:29], v179 offset:16384\n"
"ds_read_b64_tr_b16 a[30:31], v179 offset:18432\n"
"v_mfma_f32_16x16x32_bf16 v[128:131], a[4:7], v[16:19], v[128:131]\n"
"s_nop 0\n"
"ds_read_b64_tr_b16 a[32:33], v179 offset:20480\n"
"ds_read_b64_tr_b16 a[34:35], v179 offset:22528\n"
"v_mfma_f32_16x16x32_bf16 v[128:131], a[8:11], v[20:23], v[128:131]\n"
"s_nop 0\n"
"buffer_load_dwordx4 v180, s[28:31], s56 offen lds\n"
"v_mfma_f32_16x16x32_bf16 v[132:135], a[48:51], v[56:59], 0\n"
"s_addk_i32 m0, 4096\n"
"s_waitcnt vmcnt(17)\n"
"v_cmp_eq_u32 vcc, 1, v207\n"
"v_mfma_f32_16x16x32_bf16 v[132:135], a[52:55], v[60:63], v[132:135]\n"
"s_add_u32 s56, s56, s62\n"
"buffer_load_dwordx4 v180, s[28:31], s56 offen lds\n"
"v_mfma_f32_16x16x32_bf16 v[132:135], a[56:59], v[64:67], v[132:135]\n"
"s_addk_i32 m0, 4096\n"
"v_cndmask_b32 v240, v240, v241, vcc\n"
"v_cmp_eq_u32 vcc, 2, v207\n"
"v_mfma_f32_16x16x32_bf16 v[132:135], a[0:3], v[68:71], v[132:135]\n"
"s_waitcnt vmcnt(12) lgkmcnt(0)\n"
"v_cndmask_b32 v240, v240, v242, vcc\n"
"v_cmp_eq_u32 vcc, 3, v207\n"
"v_mfma_f32_16x16x32_bf16 v[132:135], a[4:7], v[72:75], v[132:135]\n"
"s_barrier\n"
"ds_read_b64_tr_b16 a[36:37], v176 offset:24576\n"
"ds_read_b64_tr_b16 a[38:39], v176 offset:26624\n"
"v_mfma_f32_16x16x32_bf16 v[132:135], a[8:11], v[76:79], v[132:135]\n"
"s_add_u32 s56, s56, s62\n"
"v_add_u32_e64 v199, s65, 0\n"
"buffer_store_dwordx2 v[198:199], v181, s[44:47], s79 offen offset:0 sc1\n"
"v_mfma_f32_16x16x32_bf16 v[136:139], a[12:15], v[0:3], 0\n"
"101:\n"
"s_cbranch_execz 105f\n"
"ds_read_b64_tr_b16 a[40:41], v176 offset:28672\n"
"v_lshlrev_b32 v181, 7, v204\n"
"v_mfma_f32_16x16x32_bf16 v[136:139], a[16:19], v[4:7], v[136:139]\n"
"s_lshl_b32 s79, s53, 11\n"
"ds_read_b64_tr_b16 a[42:43], v176 offset:30720\n"
"s_add_u32 s79, s79, 200704\n"
"v_mfma_f32_16x16x32_bf16 v[136:139], a[20:23], v[8:11], v[136:139]\n"
"ds_read_b64_tr_b16 a[44:45], v176 offset:32768\n"
"v_med3_u32 v246, v187, 12, 35\n"
"v_mfma_f32_16x16x32_bf16 v[136:139], a[24:27], v[12:15], v[136:139]\n"
"ds_read_b64_tr_b16 a[46:47], v176 offset:34816\n"
"v_lshlrev_b32 v246, 12, v246\n"
"v_mfma_f32_16x16x32_bf16 v[136:139], a[28:31], v[16:19], v[136:139]\n"
"s_lshl_b32 s80, s63, 7\n"
"ds_read_b64_tr_b16 a[48:49], v176 offset:36864\n"
"s_add_u32 s80, s80, s6\n"
"v_mfma_f32_16x16x32_bf16 v[136:139], a[32:35], v[20:23], v[136:139]\n"
"s_sub_u32 s80, s80, s55\n"
"ds_read_b64_tr_b16 a[50:51], v176 offset:38912\n"
"v_cndmask_b32 v240, v240, v243, vcc\n"
"v_mfma_f32_16x16x32_bf16 v[140:143], a[12:15], v[56:59], 0\n"
"s_nop 0\n"
"ds_read_b64_tr_b16 a[52:53], v176 offset:40960\n"
"ds_bpermute_b32 v240, v213, v240\n"
"v_mfma_f32_16x16x32_bf16 v[140:143], a[16:19], v[60:63], v[140:143]\n"
"s_and_b32 s58, s54, 1\n"
"ds_read_b64_tr_b16 a[54:55], v176 offset:43008\n"
"s_mul_i32 s58, s58, 0x80008000\n"
"v_mfma_f32_16x16x32_bf16 v[140:143], a[20:23], v[64:67], v[140:143]\n"
"ds_read_b64_tr_b16 a[56:57], v176 offset:45056\n"
"v_xor_b32 v228, s58, v228\n"
"v_mfma_f32_16x16x32_bf16 v[140:143], a[24:27], v[68:71], v[140:143]\n"
"ds_read_b64_tr_b16 a[58:59], v176 offset:47104\n"
"v_xor_b32 v229, s58, v229\n"
"v_mfma_f32_16x16x32_bf16 v[140:143], a[28:31], v[72:75], v[140:143]\n"
"ds_read_b64_tr_b16 a[0:1], v177 offset:24576\n"
"v_xor_b32 v230, s58, v230\n"
"v_mfma_f32_16x16x32_bf16 v[140:143], a[32:35], v[76:79], v[140:143]\n"
"s_waitcnt lgkmcnt(10)\n"
"ds_read_b64_tr_b16 a[2:3], v177 offset:26624\n"
"ds_read_b64_tr_b16 a[4:5], v177 offset:28672\n"
"v_mfma_f32_16x16x32_bf16 v[112:115], a[36:39], v[24:27], v[112:115]\n"
"s_nop 0\n"
"ds_write_b32 v232, v228\n"
"v_mfma_f32_16x16x32_bf16 v[112:115], a[40:43], v[28:31], v[112:115]\n"
"s_waitcnt lgkmcnt(9)\n"
"ds_read_b64_tr_b16 a[6:7], v177 offset:30720\n"
"ds_read_b64_tr_b16 a[8:9], v177 offset:32768\n"
"v_mfma_f32_16x16x32_bf16 v[112:115], a[44:47], v[32:35], v[112:115]\n"
"s_nop 0\n"
"ds_write_b32 v232, v229 offset:240\n"
"v_mfma_f32_16x16x32_bf16 v[112:115], a[48:51], v[36:39], v[112:115]\n"
"s_waitcnt lgkmcnt(7)\n"
"ds_read_b64_tr_b16 a[10:11], v177 offset:34816\n"
"ds_read_b64_tr_b16 a[12:13], v177 offset:36864\n"
"v_mfma_f32_16x16x32_bf16 v[112:115], a[52:55], v[40:43], v[112:115]\n"
"s_nop 0\n"
"ds_write_b32 v233, v230 offset:480\n"
"v_mfma_f32_16x16x32_bf16 v[112:115], a[56:59], v[44:47], v[112:115]\n"
"ds_read_b64_tr_b16 a[14:15], v177 offset:38912\n"
"v_mov_b32 v214, 0x3ae38e39\n"
"v_mfma_f32_16x16x32_bf16 v[116:119], a[36:39], v[80:83], v[116:119]\n"
"ds_read_b64_tr_b16 a[16:17], v177 offset:40960\n"
"v_fma_f32 v236, v236, v214, -v185\n"
"v_mfma_f32_16x16x32_bf16 v[116:119], a[40:43], v[84:87], v[116:119]\n"
"ds_read_b64_tr_b16 a[18:19], v177 offset:43008\n"
"v_max_f32 v236, 0, v236\n"
"v_mfma_f32_16x16x32_bf16 v[116:119], a[44:47], v[88:91], v[116:119]\n"
"ds_read_b64_tr_b16 a[20:21], v177 offset:45056\n"
"v_add_f32 v236, 0x3727c5ac, v236\n"
"v_mfma_f32_16x16x32_bf16 v[116:119], a[48:51], v[92:95], v[116:119]\n"
"s_nop 0\n"
"ds_read_b64_tr_b16 a[22:23], v177 offset:47104\n"
"ds_read_b64_tr_b16 a[24:25], v178 offset:24576\n"
"v_mfma_f32_16x16x32_bf16 v[116:119], a[52:55], v[96:99], v[116:119]\n"
"s_nop 0\n"
"v_rsq_f32 v185, v236\n"
"v_mfma_f32_16x16x32_bf16 v[116:119], a[56:59], v[100:103], v[116:119]\n"
"s_waitcnt lgkmcnt(11)\n"
"ds_read_b64_tr_b16 a[26:27], v178 offset:26624\n"
"v_fma_f32 v240, v240, v214, -v186\n"
"v_mfma_f32_16x16x32_bf16 v[120:123], a[0:3], v[24:27], v[120:123]\n"
"ds_read_b64_tr_b16 a[28:29], v178 offset:28672\n"
"v_max_f32 v240, 0, v240\n"
"v_mfma_f32_16x16x32_bf16 v[120:123], a[4:7], v[28:31], v[120:123]\n"
"s_waitcnt lgkmcnt(7)\n"
"ds_read_b64_tr_b16 a[30:31], v178 offset:30720\n"
"v_add_f32 v240, 0x3727c5ac, v240\n"
"v_mfma_f32_16x16x32_bf16 v[120:123], a[8:11], v[32:35], v[120:123]\n"
"s_nop 0\n"
"ds_read_b64_tr_b16 a[32:33], v178 offset:32768\n"
"ds_read_b64_tr_b16 a[34:35], v178 offset:34816\n"
"v_mfma_f32_16x16x32_bf16 v[120:123], a[12:15], v[36:39], v[120:123]\n"
"s_waitcnt lgkmcnt(6)\n"
"v_rsq_f32 v186, v240\n"
"v_mfma_f32_16x16x32_bf16 v[120:123], a[16:19], v[40:43], v[120:123]\n"
"ds_read_b64_tr_b16 a[36:37], v178 offset:36864\n"
"v_mul_f32 v236, v236, v185\n"
"v_mfma_f32_16x16x32_bf16 v[120:123], a[20:23], v[44:47], v[120:123]\n"
"ds_read_b64_tr_b16 a[38:39], v178 offset:38912\n"
"v_cvt_pk_bf16_f32 v231, v236, v236\n"
"v_mfma_f32_16x16x32_bf16 v[124:127], a[0:3], v[80:83], v[124:127]\n"
"ds_read_b64_tr_b16 a[40:41], v178 offset:40960\n"
"v_lshlrev_b32 v231, 16, v231\n"
"v_mfma_f32_16x16x32_bf16 v[124:127], a[4:7], v[84:87], v[124:127]\n"
"ds_read_b64_tr_b16 a[42:43], v178 offset:43008\n"
"v_sub_f32 v231, v236, v231\n"
"v_mfma_f32_16x16x32_bf16 v[124:127], a[8:11], v[88:91], v[124:127]\n"
"ds_read_b64_tr_b16 a[44:45], v178 offset:45056\n"
"v_add_u32 v176, 49152, v176\n"
"v_mfma_f32_16x16x32_bf16 v[124:127], a[12:15], v[92:95], v[124:127]\n"
"ds_read_b64_tr_b16 a[46:47], v178 offset:47104\n"
"v_cvt_pk_bf16_f32 v231, v236, v231\n"
"v_mfma_f32_16x16x32_bf16 v[124:127], a[16:19], v[96:99], v[124:127]\n"
"s_nop 0\n"
"ds_read_b64_tr_b16 a[48:49], v179 offset:24576\n"
"ds_read_b64_tr_b16 a[50:51], v179 offset:26624\n"
"v_mfma_f32_16x16x32_bf16 v[124:127], a[20:23], v[100:103], v[124:127]\n"
"s_waitcnt lgkmcnt(10)\n"
"buffer_load_dwordx4 v180, s[28:31], s56 offen lds\n"
"v_mfma_f32_16x16x32_bf16 v[128:131], a[24:27], v[24:27], v[128:131]\n"
"s_addk_i32 m0, 4096\n"
"ds_read_b64_tr_b16 a[52:53], v179 offset:28672\n"
"ds_read_b64_tr_b16 a[54:55], v179 offset:30720\n"
"v_mfma_f32_16x16x32_bf16 v[128:131], a[28:31], v[28:31], v[128:131]\n"
"s_waitcnt lgkmcnt(8)\n"
"ds_read_b64_tr_b16 a[56:57], v179 offset:32768\n"
"ds_read_b64_tr_b16 a[58:59], v179 offset:34816\n"
"v_mfma_f32_16x16x32_bf16 v[128:131], a[32:35], v[32:35], v[128:131]\n"
"s_add_u32 s56, s56, s62\n"
"ds_read_b64_tr_b16 a[0:1], v179 offset:36864\n"
"ds_read_b64_tr_b16 a[2:3], v179 offset:38912\n"
"v_mfma_f32_16x16x32_bf16 v[128:131], a[36:39], v[36:39], v[128:131]\n"
"s_waitcnt lgkmcnt(8)\n"
"ds_read_b64_tr_b16 a[4:5], v179 offset:40960\n"
"ds_read_b64_tr_b16 a[6:7], v179 offset:43008\n"
"v_mfma_f32_16x16x32_bf16 v[128:131], a[40:43], v[40:43], v[128:131]\n"
"s_nop 0\n"
"ds_read_b64_tr_b16 a[8:9], v179 offset:45056\n"
"ds_read_b64_tr_b16 a[10:11], v179 offset:47104\n"
"v_mfma_f32_16x16x32_bf16 v[128:131], a[44:47], v[44:47], v[128:131]\n"
"s_nop 0\n"
"buffer_load_dwordx4 v180, s[28:31], s56 offen lds\n"
"v_mfma_f32_16x16x32_bf16 v[132:135], a[24:27], v[80:83], v[132:135]\n"
"s_addk_i32 m0, 4096\n"
"v_add_u32 v177, 49152, v177\n"
"v_accvgpr_write_b32 a249, v231\n"
"v_mfma_f32_16x16x32_bf16 v[132:135], a[28:31], v[84:87], v[132:135]\n"
"s_mov_b32 s56, 1769472\n"
"buffer_load_dwordx4 v180, s[28:31], s56 offen lds\n"
"v_mfma_f32_16x16x32_bf16 v[132:135], a[32:35], v[88:91], v[132:135]\n"
"s_addk_i32 m0, 4096\n"
"v_mul_f32 v240, v240, v186\n"
"v_cvt_pk_bf16_f32 v231, v240, v240\n"
"v_mfma_f32_16x16x32_bf16 v[132:135], a[36:39], v[92:95], v[132:135]\n"
"s_waitcnt vmcnt(10) lgkmcnt(0)\n"
"v_lshlrev_b32 v231, 16, v231\n"
"v_sub_f32 v231, v240, v231\n"
"v_mfma_f32_16x16x32_bf16 v[132:135], a[40:43], v[96:99], v[132:135]\n"
"s_barrier\n"
"ds_read_b64_tr_b16 a[12:13], v176 offset:0\n"
"s_add_u32 s82, s77, 39936\n"
"v_mfma_f32_16x16x32_bf16 v[132:135], a[44:47], v[100:103], v[132:135]\n"
"s_add_u32 s56, s56, s62\n"
"ds_read_b64_tr_b16 a[14:15], v176 offset:2048\n"
"ds_read_b64_tr_b16 a[16:17], v176 offset:4096\n"
"v_mfma_f32_16x16x32_bf16 v[136:139], a[48:51], v[24:27], v[136:139]\n"
"s_lshl_b32 s81, s54, 28\n"
"buffer_load_dwordx4 v180, s[28:31], s56 offen lds\n"
"v_mfma_f32_16x16x32_bf16 v[136:139], a[52:55], v[28:31], v[136:139]\n"
"s_addk_i32 m0, 4096\n"
"ds_read_b64_tr_b16 a[18:19], v176 offset:6144\n"
"v_or_b32 v184, s81, v197\n"
"v_mfma_f32_16x16x32_bf16 v[136:139], a[56:59], v[32:35], v[136:139]\n"
"s_add_u32 s56, s56, s62\n"
"ds_read_b64_tr_b16 a[20:21], v176 offset:8192\n"
"ds_read_b64_tr_b16 a[22:23], v176 offset:10240\n"
"v_mfma_f32_16x16x32_bf16 v[136:139], a[0:3], v[36:39], v[136:139]\n"
"s_add_u32 s81, s82, 8192\n"
"buffer_load_dwordx4 v180, s[28:31], s56 offen lds\n"
"v_mfma_f32_16x16x32_bf16 v[136:139], a[4:7], v[40:43], v[136:139]\n"
"s_addk_i32 m0, 4096\n"
"ds_read_b64_tr_b16 a[24:25], v176 offset:12288\n"
"v_cvt_pk_bf16_f32 v231, v240, v231\n"
"v_mfma_f32_16x16x32_bf16 v[136:139], a[8:11], v[44:47], v[136:139]\n"
"s_add_u32 s56, s56, s62\n"
"ds_read_b64_tr_b16 a[26:27], v176 offset:14336\n"
"ds_read_b64_tr_b16 a[28:29], v176 offset:16384\n"
"v_mfma_f32_16x16x32_bf16 v[140:143], a[48:51], v[80:83], v[140:143]\n"
"s_nop 0\n"
"buffer_load_dwordx4 v180, s[28:31], s56 offen lds\n"
"v_mfma_f32_16x16x32_bf16 v[140:143], a[52:55], v[84:87], v[140:143]\n"
"s_addk_i32 m0, 4096\n"
"ds_read_b64_tr_b16 a[30:31], v176 offset:18432\n"
"v_accvgpr_write_b32 a250, v231\n"
"v_mfma_f32_16x16x32_bf16 v[140:143], a[56:59], v[88:91], v[140:143]\n"
"s_add_u32 s56, s56, s62\n"
"ds_read_b64_tr_b16 a[32:33], v176 offset:20480\n"
"v_add_u32 v178, 49152, v178\n"
"v_mfma_f32_16x16x32_bf16 v[140:143], a[0:3], v[92:95], v[140:143]\n"
"s_nop 0\n"
"ds_read_b64_tr_b16 a[34:35], v176 offset:22528\n"
"ds_read_b64_tr_b16 a[36:37], v177 offset:0\n"
"v_mfma_f32_16x16x32_bf16 v[140:143], a[4:7], v[96:99], v[140:143]\n"
"s_nop 0\n"
"buffer_load_dwordx4 v180, s[28:31], s56 offen lds\n"
"v_mfma_f32_16x16x32_bf16 v[140:143], a[8:11], v[100:103], v[140:143]\n"
"s_waitcnt lgkmcnt(9)\n"
"ds_read_b64_tr_b16 a[38:39], v177 offset:2048\n"
"v_add_u32 v231, 0, v208\n"
"v_mfma_f32_16x16x32_bf16 v[112:115], a[12:15], v[48:51], v[112:115]\n"
"s_addk_i32 m0, 4096\n"
"ds_read_b64_tr_b16 a[40:41], v177 offset:4096\n"
"v_min_u32 v231, 18, v231\n"
"v_mfma_f32_16x16x32_bf16 v[112:115], a[16:19], v[52:55], v[112:115]\n"
"s_waitcnt lgkmcnt(7)\n"
"ds_read_b64_tr_b16 a[42:43], v177 offset:6144\n"
"v_mad_u32_u24 v231, v231, 40, v211\n"
"v_mfma_f32_16x16x32_bf16 v[112:115], a[20:23], a[172:175], v[112:115]\n"
"s_add_u32 s56, s56, s62\n"
"ds_read_b64_tr_b16 a[44:45], v177 offset:8192\n"
"ds_read_b64_tr_b16 a[46:47], v177 offset:10240\n"
"v_mfma_f32_16x16x32_bf16 v[112:115], a[24:27], a[176:179], v[112:115]\n"
"s_waitcnt lgkmcnt(6)\n"
"buffer_load_dwordx4 v180, s[28:31], s56 offen lds\n"
"v_mfma_f32_16x16x32_bf16 v[112:115], a[28:31], a[180:183], v[112:115]\n"
"s_addk_i32 m0, 4096\n"
"ds_read_b64_tr_b16 a[48:49], v177 offset:12288\n"
"ds_read_b64_tr_b16 a[204:205], v231 offset:0\n"
"v_mfma_f32_16x16x32_bf16 v[112:115], a[32:35], a[184:187], v[112:115]\n"
"s_mov_b32 s56, 3538944\n"
"ds_read_b64_tr_b16 a[50:51], v177 offset:14336\n"
"ds_read_b64_tr_b16 a[210:211], v231 offset:8\n"
"v_mfma_f32_16x16x32_bf16 v[116:119], a[12:15], v[104:107], v[116:119]\n"
"s_nop 0\n"
"ds_read_b64_tr_b16 a[52:53], v177 offset:16384\n"
"ds_read_b64_tr_b16 a[216:217], v231 offset:16\n"
"v_mfma_f32_16x16x32_bf16 v[116:119], a[16:19], v[108:111], v[116:119]\n"
"s_nop 0\n"
"ds_read_b64_tr_b16 a[54:55], v177 offset:18432\n"
"ds_read_b64_tr_b16 a[56:57], v177 offset:20480\n"
"v_mfma_f32_16x16x32_bf16 v[116:119], a[20:23], a[188:191], v[116:119]\n"
"s_nop 0\n"
"buffer_load_dwordx4 v180, s[28:31], s56 offen lds\n"
"v_mfma_f32_16x16x32_bf16 v[116:119], a[24:27], a[192:195], v[116:119]\n"
"s_addk_i32 m0, 4096\n"
"ds_read_b64_tr_b16 a[58:59], v177 offset:22528\n"
"v_add_u32 v179, 49152, v179\n"
"v_mfma_f32_16x16x32_bf16 v[116:119], a[28:31], a[196:199], v[116:119]\n"
"s_add_u32 s56, s56, s62\n"
"ds_read_b64_tr_b16 a[0:1], v178 offset:0\n"
"ds_read_b64_tr_b16 a[222:223], v231 offset:24\n"
"v_mfma_f32_16x16x32_bf16 v[116:119], a[32:35], a[200:203], v[116:119]\n"
"s_waitcnt lgkmcnt(13)\n"
"ds_read_b64_tr_b16 a[2:3], v178 offset:2048\n"
"ds_read_b64_tr_b16 a[228:229], v231 offset:32\n"
"v_mfma_f32_16x16x32_bf16 v[120:123], a[36:39], v[48:51], v[120:123]\n"
"ds_read_b64_tr_b16 a[4:5], v178 offset:4096\n"
"v_add_u32 v231, 16, v208\n"
"v_mfma_f32_16x16x32_bf16 v[120:123], a[40:43], v[52:55], v[120:123]\n"
"s_waitcnt lgkmcnt(11)\n"
"ds_read_b64_tr_b16 a[6:7], v178 offset:6144\n"
"v_min_u32 v231, 18, v231\n"
"v_mfma_f32_16x16x32_bf16 v[120:123], a[44:47], a[172:175], v[120:123]\n"
"ds_read_b64_tr_b16 a[8:9], v178 offset:8192\n"
"v_mad_u32_u24 v231, v231, 40, v211\n"
"v_mfma_f32_16x16x32_bf16 v[120:123], a[48:51], a[176:179], v[120:123]\n"
"s_waitcnt lgkmcnt(7)\n"
"ds_read_b64_tr_b16 a[10:11], v178 offset:10240\n"
"ds_read_b64_tr_b16 a[206:207], v231 offset:0\n"
"v_mfma_f32_16x16x32_bf16 v[120:123], a[52:55], a[180:183], v[120:123]\n"
"s_nop 0\n"
"ds_read_b64_tr_b16 a[12:13], v178 offset:12288\n"
"ds_read_b64_tr_b16 a[212:213], v231 offset:8\n"
"v_mfma_f32_16x16x32_bf16 v[120:123], a[56:59], a[184:187], v[120:123]\n"
"s_nop 0\n"
"ds_read_b64_tr_b16 a[14:15], v178 offset:14336\n"
"ds_read_b64_tr_b16 a[218:219], v231 offset:16\n"
"v_mfma_f32_16x16x32_bf16 v[124:127], a[36:39], v[104:107], v[124:127]\n"
"s_nop 0\n"
"ds_read_b64_tr_b16 a[16:17], v178 offset:16384\n"
"ds_read_b64_tr_b16 a[224:225], v231 offset:24\n"
"v_mfma_f32_16x16x32_bf16 v[124:127], a[40:43], v[108:111], v[124:127]\n"
"s_nop 0\n"
"ds_read_b64_tr_b16 a[18:19], v178 offset:18432\n"
"ds_read_b64_tr_b16 a[230:231], v231 offset:32\n"
"v_mfma_f32_16x16x32_bf16 v[124:127], a[44:47], a[188:191], v[124:127]\n"
"ds_read_b64_tr_b16 a[20:21], v178 offset:20480\n"
"v_add_u32 v231, 32, v208\n"
"v_mfma_f32_16x16x32_bf16 v[124:127], a[48:51], a[192:195], v[124:127]\n"
"ds_read_b64_tr_b16 a[22:23], v178 offset:22528\n"
"v_min_u32 v231, 18, v231\n"
"v_mfma_f32_16x16x32_bf16 v[124:127], a[52:55], a[196:199], v[124:127]\n"
"ds_read_b64_tr_b16 a[24:25], v179 offset:0\n"
"v_mad_u32_u24 v231, v231, 40, v211\n"
"v_mfma_f32_16x16x32_bf16 v[124:127], a[56:59], a[200:203], v[124:127]\n"
"s_waitcnt lgkmcnt(14)\n"
"ds_read_b64_tr_b16 a[26:27], v179 offset:2048\n"
"ds_read_b64_tr_b16 a[208:209], v231 offset:0\n"
"v_mfma_f32_16x16x32_bf16 v[128:131], a[0:3], v[48:51], v[128:131]\n"
"s_nop 0\n"
"ds_read_b64_tr_b16 a[28:29], v179 offset:4096\n"
"ds_read_b64_tr_b16 a[30:31], v179 offset:6144\n"
"v_mfma_f32_16x16x32_bf16 v[128:131], a[4:7], v[52:55], v[128:131]\n"
"s_waitcnt lgkmcnt(12)\n"
"ds_read_b64_tr_b16 a[32:33], v179 offset:8192\n"
"ds_read_b64_tr_b16 a[34:35], v179 offset:10240\n"
"v_mfma_f32_16x16x32_bf16 v[128:131], a[8:11], a[172:175], v[128:131]\n"
"s_nop 0\n"
"ds_read_b64_tr_b16 a[36:37], v179 offset:12288\n"
"ds_read_b64_tr_b16 a[38:39], v179 offset:14336\n"
"v_mfma_f32_16x16x32_bf16 v[128:131], a[12:15], a[176:179], v[128:131]\n"
"s_waitcnt lgkmcnt(9)\n"
"ds_read_b64_tr_b16 a[40:41], v179 offset:16384\n"
"ds_read_b64_tr_b16 a[42:43], v179 offset:18432\n"
"v_mfma_f32_16x16x32_bf16 v[128:131], a[16:19], a[180:183], v[128:131]\n"
"s_nop 0\n"
"ds_read_b64_tr_b16 a[44:45], v179 offset:20480\n"
"ds_read_b64_tr_b16 a[46:47], v179 offset:22528\n"
"v_mfma_f32_16x16x32_bf16 v[128:131], a[20:23], a[184:187], v[128:131]\n"
"s_nop 0\n"
"buffer_load_dwordx2 v[244:245], v181, s[44:47], s79 offen sc1\n"
"v_mfma_f32_16x16x32_bf16 v[132:135], a[0:3], v[104:107], v[132:135]\n"
"s_nop 0\n"
"buffer_load_dwordx4 v180, s[28:31], s56 offen lds\n"
"v_mfma_f32_16x16x32_bf16 v[132:135], a[4:7], v[108:111], v[132:135]\n"
"s_addk_i32 m0, 4096\n"
"ds_read_b64_tr_b16 a[214:215], v231 offset:8\n"
"ds_read_b64_tr_b16 a[220:221], v231 offset:16\n"
"v_mfma_f32_16x16x32_bf16 v[132:135], a[8:11], a[188:191], v[132:135]\n"
"s_add_u32 s56, s56, s62\n"
"ds_read_b64_tr_b16 a[226:227], v231 offset:24\n"
"ds_read_b64_tr_b16 a[232:233], v231 offset:32\n"
"v_mfma_f32_16x16x32_bf16 v[132:135], a[12:15], a[192:195], v[132:135]\n"
"s_waitcnt vmcnt(9) lgkmcnt(4)\n"
"v_max_f32 v112, 0, v112\n"
"v_max_f32 v113, 0, v113\n"
"v_mfma_f32_16x16x32_bf16 v[132:135], a[16:19], a[196:199], v[132:135]\n"
"s_barrier\n"
"ds_read_b64_tr_b16 a[48:49], v176 offset:24576\n"
"ds_read_b64_tr_b16 a[50:51], v176 offset:26624\n"
"v_mfma_f32_16x16x32_bf16 v[132:135], a[20:23], a[200:203], v[132:135]\n"
"s_nop 0\n"
"buffer_load_dwordx4 v180, s[28:31], s56 offen lds\n"
"v_mfma_f32_16x16x32_bf16 v[136:139], a[24:27], v[48:51], v[136:139]\n"
"s_addk_i32 m0, 4096\n"
"ds_read_b64_tr_b16 a[52:53], v176 offset:28672\n"
"v_max_f32 v114, 0, v114\n"
"v_mfma_f32_16x16x32_bf16 v[136:139], a[28:31], v[52:55], v[136:139]\n"
"s_add_u32 s56, s56, s62\n"
"ds_read_b64_tr_b16 a[54:55], v176 offset:30720\n"
"ds_read_b64_tr_b16 a[56:57], v176 offset:32768\n"
"v_mfma_f32_16x16x32_bf16 v[136:139], a[32:35], a[172:175], v[136:139]\n"
"s_nop 0\n"
"buffer_load_dwordx4 v180, s[28:31], s56 offen lds\n"
"v_mfma_f32_16x16x32_bf16 v[136:139], a[36:39], a[176:179], v[136:139]\n"
"s_addk_i32 m0, 4096\n"
"ds_read_b64_tr_b16 a[58:59], v176 offset:34816\n"
"v_max_f32 v115, 0, v115\n"
"v_mfma_f32_16x16x32_bf16 v[136:139], a[40:43], a[180:183], v[136:139]\n"
"s_add_u32 s56, s56, s62\n"
"ds_read_b64_tr_b16 a[0:1], v176 offset:36864\n"
"ds_read_b64_tr_b16 a[2:3], v176 offset:38912\n"
"v_mfma_f32_16x16x32_bf16 v[136:139], a[44:47], a[184:187], v[136:139]\n"
"s_nop 0\n"
"buffer_load_dwordx4 v180, s[28:31], s56 offen lds\n"
"v_mfma_f32_16x16x32_bf16 v[140:143], a[24:27], v[104:107], v[140:143]\n"
"s_addk_i32 m0, 4096\n"
"ds_read_b64_tr_b16 a[4:5], v176 offset:40960\n"
"v_max_f32 v116, 0, v116\n"
"v_mfma_f32_16x16x32_bf16 v[140:143], a[28:31], v[108:111], v[140:143]\n"
"s_add_u32 s56, s56, s62\n"
"ds_read_b64_tr_b16 a[6:7], v176 offset:43008\n"
"ds_read_b64_tr_b16 a[8:9], v176 offset:45056\n"
"v_mfma_f32_16x16x32_bf16 v[140:143], a[32:35], a[188:191], v[140:143]\n"
"s_nop 0\n"
"buffer_load_dwordx4 v180, s[28:31], s56 offen lds\n"
"v_mfma_f32_16x16x32_bf16 v[140:143], a[36:39], a[192:195], v[140:143]\n"
"s_add_u32 m0, s55, 0\n"
"ds_read_b64_tr_b16 a[10:11], v176 offset:47104\n"
"v_max_f32 v117, 0, v117\n"
"v_mfma_f32_16x16x32_bf16 v[140:143], a[40:43], a[196:199], v[140:143]\n"
"s_add_u32 s56, s6, 0\n"
"ds_read_b64_tr_b16 a[12:13], v177 offset:24576\n"
"v_max_f32 v118, 0, v118\n"
"v_mfma_f32_16x16x32_bf16 v[140:143], a[44:47], a[200:203], v[140:143]\n"
"s_waitcnt lgkmcnt(9)\n"
"ds_read_b64_tr_b16 a[14:15], v177 offset:26624\n"
"v_max_f32 v119, 0, v119\n"
"v_mfma_f32_16x16x32_bf16 v[144:147], a[48:51], v[0:3], 0\n"
"ds_read_b64_tr_b16 a[16:17], v177 offset:28672\n"
"v_max_f32 v120, 0, v120\n"
"v_mfma_f32_16x16x32_bf16 v[144:147], a[52:55], v[4:7], v[144:147]\n"
"s_waitcnt lgkmcnt(7)\n"
"ds_read_b64_tr_b16 a[18:19], v177 offset:30720\n"
"v_max_f32 v121, 0, v121\n"
"v_mfma_f32_16x16x32_bf16 v[144:147], a[56:59], v[8:11], v[144:147]\n"
"ds_read_b64_tr_b16 a[20:21], v177 offset:32768\n"
"v_max_f32 v122, 0, v122\n"
"v_mfma_f32_16x16x32_bf16 v[144:147], a[0:3], v[12:15], v[144:147]\n"
"s_waitcnt lgkmcnt(5)\n"
"ds_read_b64_tr_b16 a[22:23], v177 offset:34816\n"
"v_max_f32 v123, 0, v123\n"
"v_mfma_f32_16x16x32_bf16 v[144:147], a[4:7], v[16:19], v[144:147]\n"
"ds_read_b64_tr_b16 a[24:25], v177 offset:36864\n"
"v_max_f32 v124, 0, v124\n"
"v_mfma_f32_16x16x32_bf16 v[144:147], a[8:11], v[20:23], v[144:147]\n"
"ds_read_b64_tr_b16 a[26:27], v177 offset:38912\n"
"v_max_f32 v125, 0, v125\n"
"v_mfma_f32_16x16x32_bf16 v[148:151], a[48:51], v[56:59], 0\n"
"ds_read_b64_tr_b16 a[28:29], v177 offset:40960\n"
"v_max_f32 v126, 0, v126\n"
"v_mfma_f32_16x16x32_bf16 v[148:151], a[52:55], v[60:63], v[148:151]\n"
"ds_read_b64_tr_b16 a[30:31], v177 offset:43008\n"
"v_max_f32 v127, 0, v127\n"
"v_mfma_f32_16x16x32_bf16 v[148:151], a[56:59], v[64:67], v[148:151]\n"
"ds_read_b64_tr_b16 a[32:33], v177 offset:45056\n"
"v_max_f32 v128, 0, v128\n"
"v_mfma_f32_16x16x32_bf16 v[148:151], a[0:3], v[68:71], v[148:151]\n"
"ds_read_b64_tr_b16 a[34:35], v177 offset:47104\n"
"v_max_f32 v129, 0, v129\n"
"v_mfma_f32_16x16x32_bf16 v[148:151], a[4:7], v[72:75], v[148:151]\n"
"ds_read_b64_tr_b16 a[36:37], v178 offset:24576\n"
"v_max_f32 v130, 0, v130\n"
"v_mfma_f32_16x16x32_bf16 v[148:151], a[8:11], v[76:79], v[148:151]\n"
"s_waitcnt lgkmcnt(9)\n"
"ds_read_b64_tr_b16 a[38:39], v178 offset:26624\n"
"v_max_f32 v131, 0, v131\n"
"v_mfma_f32_16x16x32_bf16 v[152:155], a[12:15], v[0:3], 0\n"
"ds_read_b64_tr_b16 a[40:41], v178 offset:28672\n"
"v_max_f32 v132, 0, v132\n"
"v_mfma_f32_16x16x32_bf16 v[152:155], a[16:19], v[4:7], v[152:155]\n"
"s_waitcnt lgkmcnt(7)\n"
"ds_read_b64_tr_b16 a[42:43], v178 offset:30720\n"
"v_max_f32 v133, 0, v133\n"
"v_mfma_f32_16x16x32_bf16 v[152:155], a[20:23], v[8:11], v[152:155]\n"
"ds_read_b64_tr_b16 a[44:45], v178 offset:32768\n"
"v_max_f32 v134, 0, v134\n"
"v_mfma_f32_16x16x32_bf16 v[152:155], a[24:27], v[12:15], v[152:155]\n"
"s_waitcnt lgkmcnt(5)\n"
"ds_read_b64_tr_b16 a[46:47], v178 offset:34816\n"
"v_max_f32 v135, 0, v135\n"
"v_mfma_f32_16x16x32_bf16 v[152:155], a[28:31], v[16:19], v[152:155]\n"
"ds_read_b64_tr_b16 a[48:49], v178 offset:36864\n"
"v_max_f32 v136, 0, v136\n"
"v_mfma_f32_16x16x32_bf16 v[152:155], a[32:35], v[20:23], v[152:155]\n"
"ds_read_b64_tr_b16 a[50:51], v178 offset:38912\n"
"v_max_f32 v137, 0, v137\n"
"v_mfma_f32_16x16x32_bf16 v[156:159], a[12:15], v[56:59], 0\n"
"ds_read_b64_tr_b16 a[52:53], v178 offset:40960\n"
"v_max_f32 v138, 0, v138\n"
"v_mfma_f32_16x16x32_bf16 v[156:159], a[16:19], v[60:63], v[156:159]\n"
"ds_read_b64_tr_b16 a[54:55], v178 offset:43008\n"
"v_max_f32 v139, 0, v139\n"
"v_mfma_f32_16x16x32_bf16 v[156:159], a[20:23], v[64:67], v[156:159]\n"
"ds_read_b64_tr_b16 a[56:57], v178 offset:45056\n"
"v_add_u32 v176, 49152, v176\n"
"v_mfma_f32_16x16x32_bf16 v[156:159], a[24:27], v[68:71], v[156:159]\n"
"ds_read_b64_tr_b16 a[58:59], v178 offset:47104\n"
"v_max_f32 v140, 0, v140\n"
"v_mfma_f32_16x16x32_bf16 v[156:159], a[28:31], v[72:75], v[156:159]\n"
"ds_read_b64_tr_b16 a[0:1], v179 offset:24576\n"
"v_max_f32 v141, 0, v141\n"
"v_mfma_f32_16x16x32_bf16 v[156:159], a[32:35], v[76:79], v[156:159]\n"
"s_waitcnt lgkmcnt(9)\n"
"ds_read_b64_tr_b16 a[2:3], v179 offset:26624\n"
"v_max_f32 v142, 0, v142\n"
"v_mfma_f32_16x16x32_bf16 v[160:163], a[36:39], v[0:3], 0\n"
"s_nop 0\n"
"ds_read_b64_tr_b16 a[4:5], v179 offset:28672\n"
"ds_read_b64_tr_b16 a[6:7], v179 offset:30720\n"
"v_mfma_f32_16x16x32_bf16 v[160:163], a[40:43], v[4:7], v[160:163]\n"
"s_waitcnt lgkmcnt(8)\n"
"ds_read_b64_tr_b16 a[8:9], v179 offset:32768\n"
"ds_read_b64_tr_b16 a[10:11], v179 offset:34816\n"
"v_mfma_f32_16x16x32_bf16 v[160:163], a[44:47], v[8:11], v[160:163]\n"
"s_nop 0\n"
"ds_read_b64_tr_b16 a[12:13], v179 offset:36864\n"
"ds_read_b64_tr_b16 a[14:15], v179 offset:38912\n"
"v_mfma_f32_16x16x32_bf16 v[160:163], a[48:51], v[12:15], v[160:163]\n"
"s_waitcnt lgkmcnt(8)\n"
"ds_read_b64_tr_b16 a[16:17], v179 offset:40960\n"
"ds_read_b64_tr_b16 a[18:19], v179 offset:43008\n"
"v_mfma_f32_16x16x32_bf16 v[160:163], a[52:55], v[16:19], v[160:163]\n"
"s_nop 0\n"
"ds_read_b64_tr_b16 a[20:21], v179 offset:45056\n"
"ds_read_b64_tr_b16 a[22:23], v179 offset:47104\n"
"v_mfma_f32_16x16x32_bf16 v[160:163], a[56:59], v[20:23], v[160:163]\n"
"v_add_u32 v177, 49152, v177\n"
"v_max_f32 v143, 0, v143\n"
"v_mfma_f32_16x16x32_bf16 v[164:167], a[36:39], v[56:59], 0\n"
"v_mfma_f32_16x16x32_bf16 v[164:167], a[40:43], v[60:63], v[164:167]\n"
"v_mfma_f32_16x16x32_bf16 v[164:167], a[44:47], v[64:67], v[164:167]\n"
"v_mfma_f32_16x16x32_bf16 v[164:167], a[48:51], v[68:71], v[164:167]\n"
"s_waitcnt vmcnt(7) lgkmcnt(0)\n"
"v_mfma_f32_16x16x32_bf16 v[164:167], a[52:55], v[72:75], v[164:167]\n"
"s_barrier\n"
"ds_read_b64_tr_b16 a[24:25], v176 offset:0\n"
"ds_read_b64_tr_b16 a[26:27], v176 offset:2048\n"
"v_mfma_f32_16x16x32_bf16 v[164:167], a[56:59], v[76:79], v[164:167]\n"
"s_nop 0\n"
"ds_read_b64_tr_b16 a[28:29], v176 offset:4096\n"
"ds_read_b64_tr_b16 a[30:31], v176 offset:6144\n"
"v_mfma_f32_16x16x32_bf16 v[168:171], a[0:3], v[0:3], 0\n"
"ds_read_b64_tr_b16 a[32:33], v176 offset:8192\n"
"v_mfma_f32_16x16x32_bf16 v[168:171], a[4:7], v[4:7], v[168:171]\n"
"ds_read_b64_tr_b16 a[34:35], v176 offset:10240\n"
"v_mfma_f32_16x16x32_bf16 v[168:171], a[8:11], v[8:11], v[168:171]\n"
"ds_read_b64_tr_b16 a[36:37], v176 offset:12288\n"
"v_mfma_f32_16x16x32_bf16 v[168:171], a[12:15], v[12:15], v[168:171]\n"
"ds_read_b64_tr_b16 a[38:39], v176 offset:14336\n"
"v_mfma_f32_16x16x32_bf16 v[168:171], a[16:19], v[16:19], v[168:171]\n"
"ds_read_b64_tr_b16 a[40:41], v176 offset:16384\n"
"v_mfma_f32_16x16x32_bf16 v[168:171], a[20:23], v[20:23], v[168:171]\n"
"ds_read_b64_tr_b16 a[42:43], v176 offset:18432\n"
"v_mfma_f32_16x16x32_bf16 v[172:175], a[0:3], v[56:59], 0\n"
"s_waitcnt vmcnt(5)\n"
"v_cmp_ne_u64 vcc, s[64:65], v[244:245]\n"
"s_nop 1\n"
"s_cbranch_vccnz 50f\n"
"51:\n"
"v_mfma_f32_16x16x32_bf16 v[172:175], a[4:7], v[60:63], v[172:175]\n"
"ds_read_b64_tr_b16 a[44:45], v176 offset:20480\n"
"v_add_u32 v178, 49152, v178\n"
"v_mfma_f32_16x16x32_bf16 v[172:175], a[8:11], v[64:67], v[172:175]\n"
"s_nop 0\n"
"ds_read_b64_tr_b16 a[46:47], v176 offset:22528\n"
"ds_read_b64_tr_b16 a[48:49], v177 offset:0\n"
"v_mfma_f32_16x16x32_bf16 v[172:175], a[12:15], v[68:71], v[172:175]\n"
"ds_read_b64_tr_b16 a[50:51], v177 offset:2048\n"
"v_mfma_f32_16x16x32_bf16 v[172:175], a[16:19], v[72:75], v[172:175]\n"
"ds_read_b64_tr_b16 a[52:53], v177 offset:4096\n"
"v_mfma_f32_16x16x32_bf16 v[172:175], a[20:23], v[76:79], v[172:175]\n"
"s_waitcnt lgkmcnt(11)\n"
"ds_read_b64_tr_b16 a[54:55], v177 offset:6144\n"
"v_mfma_f32_16x16x32_bf16 v[144:147], a[24:27], v[24:27], v[144:147]\n"
"ds_read_b64_tr_b16 a[56:57], v177 offset:8192\n"
"v_mfma_f32_16x16x32_bf16 v[144:147], a[28:31], v[28:31], v[144:147]\n"
"s_waitcnt lgkmcnt(9)\n"
"s_waitcnt vmcnt(1)\n"
"s_barrier\n"
"buffer_store_dwordx2 v[198:199], v184, s[44:47], s82 offen sc1\n"
"buffer_store_dwordx2 v[198:199], v184, s[44:47], s81 offen sc1\n"
"v_mfma_f32_16x16x32_bf16 v[144:147], a[32:35], v[32:35], v[144:147]\n"
"s_mov_b32 s81, 0\n"
"ds_read_b64_tr_b16 a[58:59], v177 offset:10240\n"
"ds_read_b64_tr_b16 a[0:1], v177 offset:12288\n"
"v_mfma_f32_16x16x32_bf16 v[144:147], a[36:39], v[36:39], v[144:147]\n"
"s_waitcnt lgkmcnt(7)\n"
"ds_read_b64_tr_b16 a[2:3], v177 offset:14336\n"
"v_mfma_f32_16x16x32_bf16 v[144:147], a[40:43], v[40:43], v[144:147]\n"
"ds_read_b64_tr_b16 a[4:5], v177 offset:16384\n"
"v_mfma_f32_16x16x32_bf16 v[144:147], a[44:47], v[44:47], v[144:147]\n"
"ds_read_b64_tr_b16 a[6:7], v177 offset:18432\n"
"v_mfma_f32_16x16x32_bf16 v[148:151], a[24:27], v[80:83], v[148:151]\n"
"ds_read_b64_tr_b16 a[8:9], v177 offset:20480\n"
"v_add_u32 v179, 49152, v179\n"
"v_mfma_f32_16x16x32_bf16 v[148:151], a[28:31], v[84:87], v[148:151]\n"
"s_nop 0\n"
"buffer_load_dword v203, v246, s[44:47], s80 offen sc1\n"
"v_mfma_f32_16x16x32_bf16 v[148:151], a[32:35], v[88:91], v[148:151]\n"
"s_nop 0\n"
"buffer_load_dwordx4 v188, s[44:47], s56 offen sc1 lds\n"
"v_mfma_f32_16x16x32_bf16 v[148:151], a[36:39], v[92:95], v[148:151]\n"
"s_addk_i32 m0, 4096\n"
"ds_read_b64_tr_b16 a[10:11], v177 offset:22528\n"
"s_addk_i32 s56, 4096\n"
"v_mfma_f32_16x16x32_bf16 v[148:151], a[40:43], v[96:99], v[148:151]\n"
"s_nop 0\n"
"ds_read_b64_tr_b16 a[12:13], v178 offset:0\n"
"ds_read_b64_tr_b16 a[14:15], v178 offset:2048\n"
"v_mfma_f32_16x16x32_bf16 v[148:151], a[44:47], v[100:103], v[148:151]\n"
"s_waitcnt lgkmcnt(10)\n"
"buffer_load_dwordx4 v188, s[44:47], s56 offen sc1 lds\n"
"v_mfma_f32_16x16x32_bf16 v[152:155], a[48:51], v[24:27], v[152:155]\n"
"s_addk_i32 m0, 4096\n"
"ds_read_b64_tr_b16 a[16:17], v178 offset:4096\n"
"s_addk_i32 s56, 4096\n"
"v_mfma_f32_16x16x32_bf16 v[152:155], a[52:55], v[28:31], v[152:155]\n"
"s_waitcnt lgkmcnt(7)\n"
"ds_read_b64_tr_b16 a[18:19], v178 offset:6144\n"
"v_mfma_f32_16x16x32_bf16 v[152:155], a[56:59], v[32:35], v[152:155]\n"
"ds_read_b64_tr_b16 a[20:21], v178 offset:8192\n"
"v_mfma_f32_16x16x32_bf16 v[152:155], a[0:3], v[36:39], v[152:155]\n"
"s_waitcnt lgkmcnt(5)\n"
"ds_read_b64_tr_b16 a[22:23], v178 offset:10240\n"
"v_mfma_f32_16x16x32_bf16 v[152:155], a[4:7], v[40:43], v[152:155]\n"
"ds_read_b64_tr_b16 a[24:25], v178 offset:12288\n"
"v_mfma_f32_16x16x32_bf16 v[152:155], a[8:11], v[44:47], v[152:155]\n"
"ds_read_b64_tr_b16 a[26:27], v178 offset:14336\n"
"v_mfma_f32_16x16x32_bf16 v[156:159], a[48:51], v[80:83], v[156:159]\n"
"ds_read_b64_tr_b16 a[28:29], v178 offset:16384\n"
"v_mfma_f32_16x16x32_bf16 v[156:159], a[52:55], v[84:87], v[156:159]\n"
"ds_read_b64_tr_b16 a[30:31], v178 offset:18432\n"
"v_mfma_f32_16x16x32_bf16 v[156:159], a[56:59], v[88:91], v[156:159]\n"
"ds_read_b64_tr_b16 a[32:33], v178 offset:20480\n"
"v_mfma_f32_16x16x32_bf16 v[156:159], a[0:3], v[92:95], v[156:159]\n"
"ds_read_b64_tr_b16 a[34:35], v178 offset:22528\n"
"v_mfma_f32_16x16x32_bf16 v[156:159], a[4:7], v[96:99], v[156:159]\n"
"s_nop 0\n"
"ds_read_b64_tr_b16 a[36:37], v179 offset:0\n"
"ds_read_b64_tr_b16 a[38:39], v179 offset:2048\n"
"v_mfma_f32_16x16x32_bf16 v[156:159], a[8:11], v[100:103], v[156:159]\n"
"102:\n"
"s_cbranch_execz 106f\n"
"s_waitcnt lgkmcnt(10)\n"
"buffer_load_dwordx4 v188, s[44:47], s56 offen sc1 lds\n"
"v_mfma_f32_16x16x32_bf16 v[160:163], a[12:15], v[24:27], v[160:163]\n"
"s_addk_i32 m0, 4096\n"
"ds_read_b64_tr_b16 a[40:41], v179 offset:4096\n"
"ds_read_b64_tr_b16 a[42:43], v179 offset:6144\n"
"v_mfma_f32_16x16x32_bf16 v[160:163], a[16:19], v[28:31], v[160:163]\n"
"s_waitcnt lgkmcnt(8)\n"
"ds_read_b64_tr_b16 a[44:45], v179 offset:8192\n"
"ds_read_b64_tr_b16 a[46:47], v179 offset:10240\n"
"v_mfma_f32_16x16x32_bf16 v[160:163], a[20:23], v[32:35], v[160:163]\n"
"s_getreg_b32 s82, hwreg(HW_REG_IB_STS, 0, 4)\n"
"ds_read_b64_tr_b16 a[48:49], v179 offset:12288\n"
"ds_read_b64_tr_b16 a[50:51], v179 offset:14336\n"
"v_mfma_f32_16x16x32_bf16 v[160:163], a[24:27], v[36:39], v[160:163]\n"
"s_waitcnt lgkmcnt(8)\n"
"s_lshl4_add_u32 s81, s81, s82\n"
"ds_read_b64_tr_b16 a[52:53], v179 offset:16384\n"
"ds_read_b64_tr_b16 a[54:55], v179 offset:18432\n"
"v_mfma_f32_16x16x32_bf16 v[160:163], a[28:31], v[40:43], v[160:163]\n"
"s_getreg_b32 s82, hwreg(HW_REG_IB_STS, 0, 4)\n"
"ds_read_b64_tr_b16 a[56:57], v179 offset:20480\n"
"ds_read_b64_tr_b16 a[58:59], v179 offset:22528\n"
"v_mfma_f32_16x16x32_bf16 v[160:163], a[32:35], v[44:47], v[160:163]\n"
"s_lshl4_add_u32 s81, s81, s82\n"
"s_addk_i32 s56, 4096\n"
"v_mfma_f32_16x16x32_bf16 v[164:167], a[12:15], v[80:83], v[164:167]\n"
"s_getreg_b32 s82, hwreg(HW_REG_IB_STS, 0, 4)\n"
"buffer_load_dwordx4 v188, s[44:47], s56 offen sc1 lds\n"
"v_mfma_f32_16x16x32_bf16 v[164:167], a[16:19], v[84:87], v[164:167]\n"
"s_lshl4_add_u32 s81, s81, s82\n"
"s_addk_i32 m0, 4096\n"
"s_addk_i32 s56, 4096\n"
"v_mfma_f32_16x16x32_bf16 v[164:167], a[20:23], v[88:91], v[164:167]\n"
"s_getreg_b32 s82, hwreg(HW_REG_IB_STS, 0, 4)\n"
"buffer_load_dwordx4 v188, s[44:47], s56 offen sc1 lds\n"
"v_mfma_f32_16x16x32_bf16 v[164:167], a[24:27], v[92:95], v[164:167]\n"
"s_waitcnt vmcnt(8) lgkmcnt(0)\n"
"s_lshl4_add_u32 s81, s81, s82\n"
"s_addk_i32 m0, 4096\n"
"v_mfma_f32_16x16x32_bf16 v[164:167], a[28:31], v[96:99], v[164:167]\n"
"s_barrier\n"
"s_getreg_b32 s82, hwreg(HW_REG_IB_STS, 0, 4)\n"
"ds_read_b64_tr_b16 a[0:1], v176 offset:24576\n"
"v_mfma_f32_16x16x32_bf16 v[164:167], a[32:35], v[100:103], v[164:167]\n"
"s_lshl4_add_u32 s81, s81, s82\n"
"ds_read_b64_tr_b16 a[2:3], v176 offset:26624\n"
"ds_read_b64_tr_b16 a[4:5], v176 offset:28672\n"
"v_mfma_f32_16x16x32_bf16 v[168:171], a[36:39], v[24:27], v[168:171]\n"
"s_getreg_b32 s82, hwreg(HW_REG_IB_STS, 0, 4)\n"
"s_lshl4_add_u32 s81, s81, s82\n"
"s_addk_i32 s56, 4096\n"
"v_mfma_f32_16x16x32_bf16 v[168:171], a[40:43], v[28:31], v[168:171]\n"
"s_nop 0\n"
"ds_read_b64_tr_b16 a[6:7], v176 offset:30720\n"
"ds_read_b64_tr_b16 a[8:9], v176 offset:32768\n"
"v_mfma_f32_16x16x32_bf16 v[168:171], a[44:47], v[32:35], v[168:171]\n"
"s_getreg_b32 s82, hwreg(HW_REG_IB_STS, 0, 4)\n"
"buffer_load_dwordx4 v188, s[44:47], s56 offen sc1 lds\n"
"v_mfma_f32_16x16x32_bf16 v[168:171], a[48:51], v[36:39], v[168:171]\n"
"s_lshl4_add_u32 s81, s81, s82\n"
"ds_read_b64_tr_b16 a[10:11], v176 offset:34816\n"
"ds_read_b64_tr_b16 a[12:13], v176 offset:36864\n"
"v_mfma_f32_16x16x32_bf16 v[168:171], a[52:55], v[40:43], v[168:171]\n"
"s_getreg_b32 s82, hwreg(HW_REG_IB_STS, 0, 4)\n"
"s_lshl4_add_u32 s81, s81, s82\n"
"s_xor_b32 s81, s81, 0x055567778\n"
"v_mfma_f32_16x16x32_bf16 v[168:171], a[56:59], v[44:47], v[168:171]\n"
"s_addk_i32 m0, 4096\n"
"ds_read_b64_tr_b16 a[14:15], v176 offset:38912\n"
"s_lshr_b32 s82, s81, 1\n"
"v_mfma_f32_16x16x32_bf16 v[172:175], a[36:39], v[80:83], v[172:175]\n"
"s_add_u32 s56, s6, 24576\n"
"ds_read_b64_tr_b16 a[16:17], v176 offset:40960\n"
"s_or_b32 s81, s81, s82\n"
"v_mfma_f32_16x16x32_bf16 v[172:175], a[40:43], v[84:87], v[172:175]\n"
"s_lshr_b32 s82, s81, 2\n"
"ds_read_b64_tr_b16 a[18:19], v176 offset:43008\n"
"s_nor_b32 s81, s81, s82\n"
"v_mfma_f32_16x16x32_bf16 v[172:175], a[44:47], v[88:91], v[172:175]\n"
"s_and_b32 s81, s81, 0x11111111\n"
"ds_read_b64_tr_b16 a[20:21], v176 offset:45056\n"
"s_bcnt1_i32_b32 s81, s81\n"
"v_mfma_f32_16x16x32_bf16 v[172:175], a[48:51], v[92:95], v[172:175]\n"
"s_lshl_b32 s82, s54, 2\n"
"ds_read_b64_tr_b16 a[22:23], v176 offset:47104\n"
"s_add_u32 s82, s82, 163824\n"
"v_mfma_f32_16x16x32_bf16 v[172:175], a[52:55], v[96:99], v[172:175]\n"
"ds_read_b64_tr_b16 a[24:25], v177 offset:24576\n"
"v_mov_b32 v184, s82\n"
"v_mfma_f32_16x16x32_bf16 v[172:175], a[56:59], v[100:103], v[172:175]\n"
"s_waitcnt lgkmcnt(9)\n"
"ds_read_b64_tr_b16 a[26:27], v177 offset:26624\n"
"ds_read_b64_tr_b16 a[28:29], v177 offset:28672\n"
"v_mfma_f32_16x16x32_bf16 v[144:147], a[0:3], v[48:51], v[144:147]\n"
"s_nop 0\n"
"v_mov_b32 v199, s81\n"
"ds_write_b32 v184, v199\n"
"v_mfma_f32_16x16x32_bf16 v[144:147], a[4:7], v[52:55], v[144:147]\n"
"s_waitcnt lgkmcnt(8)\n"
"ds_read_b64_tr_b16 a[30:31], v177 offset:30720\n"
"v_mov_b32 v184, 163824\n"
"v_mfma_f32_16x16x32_bf16 v[144:147], a[8:11], a[172:175], v[144:147]\n"
"s_nop 0\n"
"ds_read_b64_tr_b16 a[32:33], v177 offset:32768\n"
"ds_read_b64_tr_b16 a[34:35], v177 offset:34816\n"
"v_mfma_f32_16x16x32_bf16 v[144:147], a[12:15], a[176:179], v[144:147]\n"
"s_waitcnt lgkmcnt(7)\n"
"s_cmp_eq_u32 s57, 0\n"
"s_cselect_b32 s16, s24, s28\n"
"s_cselect_b32 s17, s25, s29\n"
"v_mfma_f32_16x16x32_bf16 v[144:147], a[16:19], a[180:183], v[144:147]\n"
"s_mov_b32 s18, s26\n"
"ds_read_b64_tr_b16 a[36:37], v177 offset:36864\n"
"ds_read_b64_tr_b16 a[38:39], v177 offset:38912\n"
"v_mfma_f32_16x16x32_bf16 v[144:147], a[20:23], a[184:187], v[144:147]\n"
"s_nop 0\n"
"ds_read_b64_tr_b16 a[40:41], v177 offset:40960\n"
"ds_read_b64_tr_b16 a[42:43], v177 offset:43008\n"
"v_mfma_f32_16x16x32_bf16 v[148:151], a[0:3], v[104:107], v[148:151]\n"
"ds_read_b64_tr_b16 a[44:45], v177 offset:45056\n"
"v_mfma_f32_16x16x32_bf16 v[148:151], a[4:7], v[108:111], v[148:151]\n"
"ds_read_b64_tr_b16 a[46:47], v177 offset:47104\n"
"v_mfma_f32_16x16x32_bf16 v[148:151], a[8:11], a[188:191], v[148:151]\n"
"v_mul_f32 v144, v112, v144\n"
"v_mul_f32 v145, v113, v145\n"
"v_mfma_f32_16x16x32_bf16 v[148:151], a[12:15], a[192:195], v[148:151]\n"
"v_mul_f32 v146, v114, v146\n"
"v_mul_f32 v147, v115, v147\n"
"v_mfma_f32_16x16x32_bf16 v[148:151], a[16:19], a[196:199], v[148:151]\n"
"s_nop 0\n"
"ds_read_b64_tr_b16 a[48:49], v178 offset:24576\n"
"ds_read_b64_tr_b16 a[50:51], v178 offset:26624\n"
"v_mfma_f32_16x16x32_bf16 v[148:151], a[20:23], a[200:203], v[148:151]\n"
"s_waitcnt lgkmcnt(10)\n"
"buffer_load_dword v203, v196, s[16:19], 0 offen offset:512\n"
"v_mfma_f32_16x16x32_bf16 v[152:155], a[24:27], v[48:51], v[152:155]\n"
"ds_read_b64_tr_b16 a[52:53], v178 offset:28672\n"
"v_cvt_pk_bf16_f32 v144, v144, v145\n"
"v_mfma_f32_16x16x32_bf16 v[152:155], a[28:31], v[52:55], v[152:155]\n"
"s_waitcnt lgkmcnt(7)\n"
"ds_read_b64_tr_b16 a[54:55], v178 offset:30720\n"
"v_mul_f32 v148, v116, v148\n"
"v_mfma_f32_16x16x32_bf16 v[152:155], a[32:35], a[172:175], v[152:155]\n"
"ds_read_b64_tr_b16 a[56:57], v178 offset:32768\n"
"v_mul_f32 v149, v117, v149\n"
"v_mfma_f32_16x16x32_bf16 v[152:155], a[36:39], a[176:179], v[152:155]\n"
"s_waitcnt lgkmcnt(5)\n"
"ds_read_b64_tr_b16 a[58:59], v178 offset:34816\n"
"ds_read_b64_tr_b16 a[0:1], v178 offset:36864\n"
"v_mfma_f32_16x16x32_bf16 v[152:155], a[40:43], a[180:183], v[152:155]\n"
"s_nop 0\n"
"buffer_load_dwordx4 v188, s[44:47], s56 offen sc1 lds\n"
"v_mfma_f32_16x16x32_bf16 v[152:155], a[44:47], a[184:187], v[152:155]\n"
"s_addk_i32 m0, 4096\n"
"ds_read_b64_tr_b16 a[2:3], v178 offset:38912\n"
"v_mul_f32 v150, v118, v150\n"
"v_mfma_f32_16x16x32_bf16 v[156:159], a[24:27], v[104:107], v[156:159]\n"
"s_addk_i32 s56, 4096\n"
"ds_read_b64_tr_b16 a[4:5], v178 offset:40960\n"
"v_mul_f32 v151, v119, v151\n"
"v_mfma_f32_16x16x32_bf16 v[156:159], a[28:31], v[108:111], v[156:159]\n"
"ds_read_b64_tr_b16 a[6:7], v178 offset:43008\n"
"v_mul_f32 v152, v120, v152\n"
"v_mfma_f32_16x16x32_bf16 v[156:159], a[32:35], a[188:191], v[156:159]\n"
"s_nop 0\n"
"ds_read_b64_tr_b16 a[8:9], v178 offset:45056\n"
"ds_read_b64_tr_b16 a[10:11], v178 offset:47104\n"
"v_mfma_f32_16x16x32_bf16 v[156:159], a[36:39], a[192:195], v[156:159]\n"
"s_nop 0\n"
"buffer_load_dwordx4 v188, s[44:47], s56 offen sc1 lds\n"
"v_mfma_f32_16x16x32_bf16 v[156:159], a[40:43], a[196:199], v[156:159]\n"
"s_addk_i32 m0, 4096\n"
"ds_read_b64_tr_b16 a[12:13], v179 offset:24576\n"
"v_mul_f32 v153, v121, v153\n"
"v_mfma_f32_16x16x32_bf16 v[156:159], a[44:47], a[200:203], v[156:159]\n"
"s_waitcnt lgkmcnt(9)\n"
"ds_read_b64_tr_b16 a[14:15], v179 offset:26624\n"
"v_mul_f32 v154, v122, v154\n"
"v_mfma_f32_16x16x32_bf16 v[160:163], a[48:51], v[48:51], v[160:163]\n"
"s_addk_i32 s56, 4096\n"
"ds_read_b64_tr_b16 a[16:17], v179 offset:28672\n"
"ds_read_b64_tr_b16 a[18:19], v179 offset:30720\n"
"v_mfma_f32_16x16x32_bf16 v[160:163], a[52:55], v[52:55], v[160:163]\n"
"s_waitcnt lgkmcnt(8)\n"
"ds_read_b64_tr_b16 a[20:21], v179 offset:32768\n"
"ds_read_b64_tr_b16 a[22:23], v179 offset:34816\n"
"v_mfma_f32_16x16x32_bf16 v[160:163], a[56:59], a[172:175], v[160:163]\n"
"s_nop 0\n"
"ds_read_b64_tr_b16 a[24:25], v179 offset:36864\n"
"ds_read_b64_tr_b16 a[26:27], v179 offset:38912\n"
"v_mfma_f32_16x16x32_bf16 v[160:163], a[0:3], a[176:179], v[160:163]\n"
"s_waitcnt lgkmcnt(8)\n"
"ds_read_b64_tr_b16 a[28:29], v179 offset:40960\n"
"ds_read_b64_tr_b16 a[30:31], v179 offset:43008\n"
"v_mfma_f32_16x16x32_bf16 v[160:163], a[4:7], a[180:183], v[160:163]\n"
"s_nop 0\n"
"ds_read_b64_tr_b16 a[32:33], v179 offset:45056\n"
"ds_read_b64_tr_b16 a[34:35], v179 offset:47104\n"
"v_mfma_f32_16x16x32_bf16 v[160:163], a[8:11], a[184:187], v[160:163]\n"
"v_mul_f32 v155, v123, v155\n"
"v_mul_f32 v156, v124, v156\n"
"v_mfma_f32_16x16x32_bf16 v[164:167], a[48:51], v[104:107], v[164:167]\n"
"v_mul_f32 v157, v125, v157\n"
"v_mul_f32 v158, v126, v158\n"
"v_mfma_f32_16x16x32_bf16 v[164:167], a[52:55], v[108:111], v[164:167]\n"
"s_nop 0\n"
"buffer_load_dwordx4 v188, s[44:47], s56 offen sc1 lds\n"
"v_mfma_f32_16x16x32_bf16 v[164:167], a[56:59], a[188:191], v[164:167]\n"
"s_addk_i32 m0, 4096\n"
"v_mul_f32 v159, v127, v159\n"
"v_mul_f32 v160, v128, v160\n"
"v_mfma_f32_16x16x32_bf16 v[164:167], a[0:3], a[192:195], v[164:167]\n"
"s_waitcnt vmcnt(4) lgkmcnt(0)\n"
"v_mul_f32 v161, v129, v161\n"
"v_mul_f32 v162, v130, v162\n"
"v_mfma_f32_16x16x32_bf16 v[164:167], a[4:7], a[196:199], v[164:167]\n"
"s_barrier\n"
"ds_read_b64_tr_b16 a[36:37], v189 offset:0\n"
"ds_read_b64_tr_b16 a[38:39], v189 offset:4096\n"
"v_mfma_f32_16x16x32_bf16 v[164:167], a[8:11], a[200:203], v[164:167]\n"
"s_addk_i32 s56, 4096\n"
"buffer_load_dwordx4 v188, s[44:47], s56 offen sc1 lds\n"
"v_mfma_f32_16x16x32_bf16 v[168:171], a[12:15], v[48:51], v[168:171]\n"
"s_addk_i32 m0, 4096\n"
"ds_read_b64_tr_b16 a[40:41], v189 offset:8192\n"
"ds_read_b64_tr_b16 a[42:43], v189 offset:12288\n"
"v_mfma_f32_16x16x32_bf16 v[168:171], a[16:19], v[52:55], v[168:171]\n"
"s_addk_i32 s56, 4096\n"
"buffer_load_dwordx4 v188, s[44:47], s56 offen sc1 lds\n"
"v_mfma_f32_16x16x32_bf16 v[168:171], a[20:23], a[172:175], v[168:171]\n"
"s_addk_i32 m0, 4096\n"
"ds_read_b64_tr_b16 a[44:45], v189 offset:16384\n"
"ds_read_b32 v184, v184\n"
"v_mfma_f32_16x16x32_bf16 v[168:171], a[24:27], a[176:179], v[168:171]\n"
"s_addk_i32 s56, 4096\n"
"ds_read_b64_tr_b16 a[46:47], v189 offset:20480\n"
"ds_read_b64_tr_b16 a[48:49], v190 offset:0\n"
"v_mfma_f32_16x16x32_bf16 v[168:171], a[28:31], a[180:183], v[168:171]\n"
"s_nop 0\n"
"buffer_load_dwordx4 v188, s[44:47], s56 offen sc1 lds\n"
"v_mfma_f32_16x16x32_bf16 v[168:171], a[32:35], a[184:187], v[168:171]\n"
"s_addk_i32 m0, 4096\n"
"ds_read_b64_tr_b16 a[50:51], v190 offset:4096\n"
"v_mul_f32 v163, v131, v163\n"
"v_mfma_f32_16x16x32_bf16 v[172:175], a[12:15], v[104:107], v[172:175]\n"
"s_add_u32 s56, s6, 49152\n"
"ds_read_b64_tr_b16 a[52:53], v190 offset:8192\n"
"v_mul_f32 v164, v132, v164\n"
"v_mfma_f32_16x16x32_bf16 v[172:175], a[16:19], v[108:111], v[172:175]\n"
"ds_read_b64_tr_b16 a[54:55], v190 offset:12288\n"
"v_mul_f32 v165, v133, v165\n"
"v_mfma_f32_16x16x32_bf16 v[172:175], a[20:23], a[188:191], v[172:175]\n"
"ds_read_b64_tr_b16 a[56:57], v190 offset:16384\n"
"v_mul_f32 v166, v134, v166\n"
"v_mfma_f32_16x16x32_bf16 v[172:175], a[24:27], a[192:195], v[172:175]\n"
"s_nop 0\n"
"ds_read_b64_tr_b16 a[58:59], v190 offset:20480\n"
"ds_read_b64_tr_b16 a[0:1], v191 offset:0\n"
"v_mfma_f32_16x16x32_bf16 v[172:175], a[28:31], a[196:199], v[172:175]\n"
"s_nop 0\n"
"buffer_load_dword v203, v196, s[28:31], s99 offen offset:192\n"
"v_mfma_f32_16x16x32_bf16 v[172:175], a[32:35], a[200:203], v[172:175]\n"
"s_waitcnt lgkmcnt(10)\n"
"ds_read_b64_tr_b16 a[2:3], v191 offset:4096\n"
"v_mul_f32 v167, v135, v167\n"
"v_mfma_f32_16x16x32_bf16 v[232:235], a[36:39], v[0:3], 0\n"
"ds_read_b64_tr_b16 a[4:5], v191 offset:8192\n"
"v_mul_f32 v168, v136, v168\n"
"v_mfma_f32_16x16x32_bf16 v[232:235], a[40:43], v[4:7], v[232:235]\n"
"s_waitcnt lgkmcnt(9)\n"
"ds_read_b64_tr_b16 a[6:7], v191 offset:12288\n"
"ds_read_b64_tr_b16 a[8:9], v191 offset:16384\n"
"v_mfma_f32_16x16x32_bf16 v[232:235], a[44:47], v[8:11], v[232:235]\n"
"s_nop 0\n"
"buffer_load_dwordx4 v188, s[44:47], s56 offen sc1 lds\n"
"v_mfma_f32_16x16x32_bf16 v[236:239], a[36:39], v[56:59], 0\n"
"s_addk_i32 m0, 4096\n"
"ds_read_b64_tr_b16 a[10:11], v191 offset:20480\n"
"ds_read_b64_tr_b16 a[12:13], v192 offset:0\n"
"v_mfma_f32_16x16x32_bf16 v[236:239], a[40:43], v[60:63], v[236:239]\n"
"s_addk_i32 s56, 4096\n"
"v_cmp_gt_u32 vcc, 2, v184\n"
"s_nop 1\n"
"s_cmp_eq_u64 vcc, 0\n"
"s_cselect_b32 s81, 0, 1\n"
"v_mfma_f32_16x16x32_bf16 v[236:239], a[44:47], v[64:67], v[236:239]\n"
"s_waitcnt lgkmcnt(9)\n"
"ds_read_b64_tr_b16 a[14:15], v192 offset:4096\n"
"ds_read_b64_tr_b16 a[16:17], v192 offset:8192\n"
"v_mfma_f32_16x16x32_bf16 v[240:243], a[48:51], v[0:3], 0\n"
"s_xor_b32 s81, s81, 0\n"
"buffer_load_dwordx4 v188, s[44:47], s56 offen sc1 lds\n"
"v_mfma_f32_16x16x32_bf16 v[240:243], a[52:55], v[4:7], v[240:243]\n"
"s_waitcnt lgkmcnt(9)\n"
"ds_read_b64_tr_b16 a[18:19], v192 offset:12288\n"
"s_lshl_b32 s82, s81, 13\n"
"v_mfma_f32_16x16x32_bf16 v[240:243], a[56:59], v[8:11], v[240:243]\n"
"s_addk_i32 m0, 4096\n"
"ds_read_b64_tr_b16 a[20:21], v192 offset:16384\n"
"s_add_u32 s77, s77, s82\n"
"v_mfma_f32_16x16x32_bf16 v[244:247], a[48:51], v[56:59], 0\n"
"s_addk_i32 s56, 4096\n"
"ds_read_b64_tr_b16 a[22:23], v192 offset:20480\n"
"s_add_u32 s83, s77, 37632\n"
"v_mfma_f32_16x16x32_bf16 v[244:247], a[52:55], v[60:63], v[244:247]\n"
"s_add_u32 s81, s81, s65\n"
"ds_read_b64_tr_b16 a[24:25], v193 offset:0\n"
"s_lshl_b32 s82, s52, 4\n"
"v_mfma_f32_16x16x32_bf16 v[244:247], a[56:59], v[64:67], v[244:247]\n"
"s_waitcnt lgkmcnt(9)\n"
"ds_read_b64_tr_b16 a[26:27], v193 offset:4096\n"
"s_add_u32 s82, s82, s53\n"
"v_mfma_f32_16x16x32_bf16 v[248:251], a[0:3], v[0:3], 0\n"
"s_lshl_b32 s82, s82, 7\n"
"ds_read_b64_tr_b16 a[28:29], v193 offset:8192\n"
"s_add_u32 s82, s82, 167936\n"
"v_mfma_f32_16x16x32_bf16 v[248:251], a[4:7], v[4:7], v[248:251]\n"
"s_waitcnt lgkmcnt(9)\n"
"ds_read_b64_tr_b16 a[30:31], v193 offset:12288\n"
"v_accvgpr_read_b32 v184, a251\n"
"v_mfma_f32_16x16x32_bf16 v[248:251], a[8:11], v[8:11], v[248:251]\n"
"ds_read_b64_tr_b16 a[32:33], v193 offset:16384\n"
"v_mul_f32 v169, v137, v169\n"
"v_mfma_f32_16x16x32_bf16 v[252:255], a[0:3], v[56:59], 0\n"
"ds_read_b64_tr_b16 a[34:35], v193 offset:20480\n"
"v_mul_f32 v170, v138, v170\n"
"v_mfma_f32_16x16x32_bf16 v[252:255], a[4:7], v[60:63], v[252:255]\n"
"s_nop 0\n"
"ds_read_b64_tr_b16 a[36:37], v194 offset:0\n"
"ds_read_b64_tr_b16 a[38:39], v194 offset:4096\n"
"v_mfma_f32_16x16x32_bf16 v[252:255], a[8:11], v[64:67], v[252:255]\n"
"s_waitcnt lgkmcnt(10)\n"
"v_mov_b32 v199, s81\n"
"buffer_store_dwordx2 v[198:199], v197, s[44:47], s82 offen sc1\n"
"v_mfma_f32_16x16x32_bf16 v[112:115], a[12:15], v[0:3], 0\n"
"s_nop 0\n"
"ds_read_b64_tr_b16 a[40:41], v194 offset:8192\n"
"ds_read_b64_tr_b16 a[42:43], v194 offset:12288\n"
"v_mfma_f32_16x16x32_bf16 v[112:115], a[16:19], v[4:7], v[112:115]\n"
"s_waitcnt lgkmcnt(10)\n"
"ds_read_b64_tr_b16 a[44:45], v194 offset:16384\n"
"ds_read_b64_tr_b16 a[46:47], v194 offset:20480\n"
"v_mfma_f32_16x16x32_bf16 v[112:115], a[20:23], v[8:11], v[112:115]\n"
"s_nop 0\n"
"ds_read_b64_tr_b16 a[48:49], v195 offset:0\n"
"ds_read_b64_tr_b16 a[50:51], v195 offset:4096\n"
"v_mfma_f32_16x16x32_bf16 v[116:119], a[12:15], v[56:59], 0\n"
"s_nop 0\n"
"ds_read_b64_tr_b16 a[52:53], v195 offset:8192\n"
"ds_read_b64_tr_b16 a[54:55], v195 offset:12288\n"
"v_mfma_f32_16x16x32_bf16 v[116:119], a[16:19], v[60:63], v[116:119]\n"
"s_nop 0\n"
"ds_read_b64_tr_b16 a[56:57], v195 offset:16384\n"
"ds_read_b64_tr_b16 a[58:59], v195 offset:20480\n"
"v_mfma_f32_16x16x32_bf16 v[116:119], a[20:23], v[64:67], v[116:119]\n"
"s_waitcnt lgkmcnt(14)\n"
"buffer_load_dwordx4 v188, s[44:47], s56 offen sc1 lds\n"
"v_mfma_f32_16x16x32_bf16 v[120:123], a[24:27], v[0:3], 0\n"
"s_addk_i32 m0, 4096\n"
"v_mul_f32 v171, v139, v171\n"
"v_mul_f32 v172, v140, v172\n"
"v_mfma_f32_16x16x32_bf16 v[120:123], a[28:31], v[4:7], v[120:123]\n"
"s_waitcnt lgkmcnt(12)\n"
"v_mul_f32 v173, v141, v173\n"
"v_mul_f32 v174, v142, v174\n"
"v_mfma_f32_16x16x32_bf16 v[120:123], a[32:35], v[8:11], v[120:123]\n"
"s_addk_i32 s56, 4096\n"
"v_mul_f32 v175, v143, v175\n"
"v_cvt_pk_bf16_f32 v145, v146, v147\n"
"v_mfma_f32_16x16x32_bf16 v[124:127], a[24:27], v[56:59], 0\n"
"s_waitcnt vmcnt(5) lgkmcnt(0)\n"
"buffer_load_dwordx4 v188, s[44:47], s56 offen sc1 lds\n"
"v_mfma_f32_16x16x32_bf16 v[124:127], a[28:31], v[60:63], v[124:127]\n"
"s_barrier\n"
"ds_read_b64_tr_b16 a[0:1], v189 offset:24576\n"
"v_accvgpr_write_b32 a60, v144\n"
"v_mfma_f32_16x16x32_bf16 v[124:127], a[32:35], v[64:67], v[124:127]\n"
"s_addk_i32 m0, 4096\n"
"ds_read_b64_tr_b16 a[2:3], v189 offset:28672\n"
"v_accvgpr_write_b32 a61, v145\n"
"v_mfma_f32_16x16x32_bf16 v[128:131], a[36:39], v[0:3], 0\n"
"s_addk_i32 s56, 4096\n"
"ds_read_b64_tr_b16 a[4:5], v189 offset:32768\n"
"ds_read_b64_tr_b16 a[6:7], v189 offset:36864\n"
"v_mfma_f32_16x16x32_bf16 v[128:131], a[40:43], v[4:7], v[128:131]\n"
"s_nop 0\n"
"buffer_load_dwordx4 v188, s[44:47], s56 offen sc1 lds\n"
"v_mfma_f32_16x16x32_bf16 v[128:131], a[44:47], v[8:11], v[128:131]\n"
"s_addk_i32 m0, 4096\n"
"ds_read_b64_tr_b16 a[8:9], v189 offset:40960\n"
"v_cvt_pk_bf16_f32 v148, v148, v149\n"
"v_mfma_f32_16x16x32_bf16 v[132:135], a[36:39], v[56:59], 0\n"
"s_addk_i32 s56, 4096\n"
"ds_read_b64_tr_b16 a[10:11], v189 offset:45056\n"
"ds_read_b64_tr_b16 a[12:13], v190 offset:24576\n"
"v_mfma_f32_16x16x32_bf16 v[132:135], a[40:43], v[60:63], v[132:135]\n"
"s_nop 0\n"
"buffer_load_dwordx4 v188, s[44:47], s56 offen sc1 lds\n"
"v_mfma_f32_16x16x32_bf16 v[132:135], a[44:47], v[64:67], v[132:135]\n"
"s_addk_i32 m0, 4096\n"
"ds_read_b64_tr_b16 a[14:15], v190 offset:28672\n"
"v_cvt_pk_bf16_f32 v149, v150, v151\n"
"v_mfma_f32_16x16x32_bf16 v[136:139], a[48:51], v[0:3], 0\n"
"s_add_u32 s56, s6, 73728\n"
"ds_read_b64_tr_b16 a[16:17], v190 offset:32768\n"
"ds_read_b64_tr_b16 a[18:19], v190 offset:36864\n"
"v_mfma_f32_16x16x32_bf16 v[136:139], a[52:55], v[4:7], v[136:139]\n"
"s_nop 0\n"
"buffer_load_dwordx4 v188, s[44:47], s56 offen sc1 lds\n"
"v_mfma_f32_16x16x32_bf16 v[136:139], a[56:59], v[8:11], v[136:139]\n"
"s_addk_i32 m0, 4096\n"
"ds_read_b64_tr_b16 a[20:21], v190 offset:40960\n"
"v_accvgpr_write_b32 a102, v148\n"
"v_mfma_f32_16x16x32_bf16 v[140:143], a[48:51], v[56:59], 0\n"
"s_addk_i32 s56, 4096\n"
"ds_read_b64_tr_b16 a[22:23], v190 offset:45056\n"
"v_accvgpr_write_b32 a103, v149\n"
"v_mfma_f32_16x16x32_bf16 v[140:143], a[52:55], v[60:63], v[140:143]\n"
"ds_read_b64_tr_b16 a[24:25], v191 offset:24576\n"
"v_cvt_pk_bf16_f32 v152, v152, v153\n"
"v_mfma_f32_16x16x32_bf16 v[140:143], a[56:59], v[64:67], v[140:143]\n"
"s_waitcnt lgkmcnt(9)\n"
"ds_read_b64_tr_b16 a[26:27], v191 offset:28672\n"
"v_cvt_pk_bf16_f32 v153, v154, v155\n"
"v_mfma_f32_16x16x32_bf16 v[232:235], a[0:3], v[12:15], v[232:235]\n"
"ds_read_b64_tr_b16 a[28:29], v191 offset:32768\n"
"v_accvgpr_write_b32 a62, v152\n"
"v_mfma_f32_16x16x32_bf16 v[232:235], a[4:7], v[16:19], v[232:235]\n"
"s_waitcnt lgkmcnt(9)\n"
"ds_read_b64_tr_b16 a[30:31], v191 offset:36864\n"
"ds_read_b64_tr_b16 a[32:33], v191 offset:40960\n"
"v_mfma_f32_16x16x32_bf16 v[232:235], a[8:11], v[20:23], v[232:235]\n"
"s_nop 0\n"
"buffer_load_dwordx4 v188, s[44:47], s56 offen sc1 lds\n"
"v_mfma_f32_16x16x32_bf16 v[236:239], a[0:3], v[68:71], v[236:239]\n"
"s_addk_i32 m0, 4096\n"
"ds_read_b64_tr_b16 a[34:35], v191 offset:45056\n"
"v_accvgpr_write_b32 a63, v153\n"
"v_mfma_f32_16x16x32_bf16 v[236:239], a[4:7], v[72:75], v[236:239]\n"
"s_addk_i32 s56, 4096\n"
"ds_read_b64_tr_b16 a[36:37], v192 offset:24576\n"
"v_cvt_pk_bf16_f32 v156, v156, v157\n"
"v_mfma_f32_16x16x32_bf16 v[236:239], a[8:11], v[76:79], v[236:239]\n"
"s_waitcnt lgkmcnt(9)\n"
"ds_read_b64_tr_b16 a[38:39], v192 offset:28672\n"
"v_cvt_pk_bf16_f32 v157, v158, v159\n"
"v_mfma_f32_16x16x32_bf16 v[240:243], a[12:15], v[12:15], v[240:243]\n"
"s_nop 0\n"
"ds_read_b64_tr_b16 a[40:41], v192 offset:32768\n"
"ds_read_b64_tr_b16 a[42:43], v192 offset:36864\n"
"v_mfma_f32_16x16x32_bf16 v[240:243], a[16:19], v[16:19], v[240:243]\n"
"s_waitcnt lgkmcnt(10)\n"
"buffer_load_dwordx4 v188, s[44:47], s56 offen sc1 lds\n"
"v_mfma_f32_16x16x32_bf16 v[240:243], a[20:23], v[20:23], v[240:243]\n"
"s_addk_i32 m0, 4096\n"
"ds_read_b64_tr_b16 a[44:45], v192 offset:40960\n"
"v_accvgpr_write_b32 a104, v156\n"
"v_mfma_f32_16x16x32_bf16 v[244:247], a[12:15], v[68:71], v[244:247]\n"
"s_addk_i32 s56, 4096\n"
"ds_read_b64_tr_b16 a[46:47], v192 offset:45056\n"
"v_accvgpr_write_b32 a105, v157\n"
"v_mfma_f32_16x16x32_bf16 v[244:247], a[16:19], v[72:75], v[244:247]\n"
"ds_read_b64_tr_b16 a[48:49], v193 offset:24576\n"
"v_cvt_pk_bf16_f32 v160, v160, v161\n"
"v_mfma_f32_16x16x32_bf16 v[244:247], a[20:23], v[76:79], v[244:247]\n"
"s_waitcnt lgkmcnt(9)\n"
"ds_read_b64_tr_b16 a[50:51], v193 offset:28672\n"
"ds_read_b64_tr_b16 a[52:53], v193 offset:32768\n"
"v_mfma_f32_16x16x32_bf16 v[248:251], a[24:27], v[12:15], v[248:251]\n"
"s_nop 0\n"
"buffer_load_dwordx4 v188, s[44:47], s56 offen sc1 lds\n"
"v_mfma_f32_16x16x32_bf16 v[248:251], a[28:31], v[16:19], v[248:251]\n"
"s_waitcnt lgkmcnt(9)\n"
"ds_read_b64_tr_b16 a[54:55], v193 offset:36864\n"
"v_cvt_pk_bf16_f32 v161, v162, v163\n"
"v_mfma_f32_16x16x32_bf16 v[248:251], a[32:35], v[20:23], v[248:251]\n"
"s_addk_i32 m0, 4096\n"
"ds_read_b64_tr_b16 a[56:57], v193 offset:40960\n"
"v_add_u32 v189, 49152, v189\n"
"v_mfma_f32_16x16x32_bf16 v[252:255], a[24:27], v[68:71], v[252:255]\n"
"s_addk_i32 s56, 4096\n"
"ds_read_b64_tr_b16 a[58:59], v193 offset:45056\n"
"v_accvgpr_write_b32 a64, v160\n"
"v_mfma_f32_16x16x32_bf16 v[252:255], a[28:31], v[72:75], v[252:255]\n"
"s_nop 0\n"
"ds_read_b64_tr_b16 a[0:1], v194 offset:24576\n"
"ds_read_b64_tr_b16 a[2:3], v194 offset:28672\n"
"v_mfma_f32_16x16x32_bf16 v[252:255], a[32:35], v[76:79], v[252:255]\n"
"s_waitcnt lgkmcnt(10)\n"
"buffer_load_dwordx4 v188, s[44:47], s56 offen sc1 lds\n"
"v_mfma_f32_16x16x32_bf16 v[112:115], a[36:39], v[12:15], v[112:115]\n"
"s_addk_i32 m0, 4096\n"
"ds_read_b64_tr_b16 a[4:5], v194 offset:32768\n"
"ds_read_b64_tr_b16 a[6:7], v194 offset:36864\n"
"v_mfma_f32_16x16x32_bf16 v[112:115], a[40:43], v[16:19], v[112:115]\n"
"s_waitcnt lgkmcnt(10)\n"
"ds_read_b64_tr_b16 a[8:9], v194 offset:40960\n"
"ds_read_b64_tr_b16 a[10:11], v194 offset:45056\n"
"v_mfma_f32_16x16x32_bf16 v[112:115], a[44:47], v[20:23], v[112:115]\n"
"s_addk_i32 s56, 4096\n"
"ds_read_b64_tr_b16 a[12:13], v195 offset:24576\n"
"ds_read_b64_tr_b16 a[14:15], v195 offset:28672\n"
"v_mfma_f32_16x16x32_bf16 v[116:119], a[36:39], v[68:71], v[116:119]\n"
"s_nop 0\n"
"ds_read_b64_tr_b16 a[16:17], v195 offset:32768\n"
"ds_read_b64_tr_b16 a[18:19], v195 offset:36864\n"
"v_mfma_f32_16x16x32_bf16 v[116:119], a[40:43], v[72:75], v[116:119]\n"
"s_nop 0\n"
"ds_read_b64_tr_b16 a[20:21], v195 offset:40960\n"
"ds_read_b64_tr_b16 a[22:23], v195 offset:45056\n"
"v_mfma_f32_16x16x32_bf16 v[116:119], a[44:47], v[76:79], v[116:119]\n"
"s_waitcnt lgkmcnt(14)\n"
"buffer_load_dwordx4 v188, s[44:47], s56 offen sc1 lds\n"
"v_mfma_f32_16x16x32_bf16 v[120:123], a[48:51], v[12:15], v[120:123]\n"
"s_addk_i32 m0, 4096\n"
"v_add_u32 v190, 49152, v190\n"
"v_add_u32 v191, 49152, v191\n"
"v_mfma_f32_16x16x32_bf16 v[120:123], a[52:55], v[16:19], v[120:123]\n"
"s_waitcnt lgkmcnt(12)\n"
"v_accvgpr_write_b32 a65, v161\n"
"v_cvt_pk_bf16_f32 v164, v164, v165\n"
"v_mfma_f32_16x16x32_bf16 v[120:123], a[56:59], v[20:23], v[120:123]\n"
"s_add_u32 s56, s6, 98304\n"
"buffer_load_dwordx4 v188, s[44:47], s56 offen sc1 lds\n"
"v_mfma_f32_16x16x32_bf16 v[124:127], a[48:51], v[68:71], v[124:127]\n"
"s_waitcnt vmcnt(7) lgkmcnt(0)\n"
"v_cvt_pk_bf16_f32 v165, v166, v167\n"
"v_accvgpr_write_b32 a106, v164\n"
"v_mfma_f32_16x16x32_bf16 v[124:127], a[52:55], v[72:75], v[124:127]\n"
"s_barrier\n"
"ds_read_b64_tr_b16 a[24:25], v189 offset:0\n"
"v_accvgpr_write_b32 a107, v165\n"
"v_mfma_f32_16x16x32_bf16 v[124:127], a[56:59], v[76:79], v[124:127]\n"
"s_addk_i32 m0, 4096\n"
"ds_read_b64_tr_b16 a[26:27], v189 offset:4096\n"
"v_cvt_pk_bf16_f32 v168, v168, v169\n"
"v_mfma_f32_16x16x32_bf16 v[128:131], a[0:3], v[12:15], v[128:131]\n"
"s_addk_i32 s56, 4096\n"
"ds_read_b64_tr_b16 a[28:29], v189 offset:8192\n"
"ds_read_b64_tr_b16 a[30:31], v189 offset:12288\n"
"v_mfma_f32_16x16x32_bf16 v[128:131], a[4:7], v[16:19], v[128:131]\n"
"s_nop 0\n"
"buffer_load_dwordx4 v188, s[44:47], s56 offen sc1 lds\n"
"v_mfma_f32_16x16x32_bf16 v[128:131], a[8:11], v[20:23], v[128:131]\n"
"s_addk_i32 m0, 4096\n"
"ds_read_b64_tr_b16 a[32:33], v189 offset:16384\n"
"v_add_u32 v192, 49152, v192\n"
"v_mfma_f32_16x16x32_bf16 v[132:135], a[0:3], v[68:71], v[132:135]\n"
"s_addk_i32 s56, 4096\n"
"ds_read_b64_tr_b16 a[34:35], v189 offset:20480\n"
"ds_read_b64_tr_b16 a[36:37], v190 offset:0\n"
"v_mfma_f32_16x16x32_bf16 v[132:135], a[4:7], v[72:75], v[132:135]\n"
"s_nop 0\n"
"buffer_load_dwordx4 v188, s[44:47], s56 offen sc1 lds\n"
"v_mfma_f32_16x16x32_bf16 v[132:135], a[8:11], v[76:79], v[132:135]\n"
"s_addk_i32 m0, 4096\n"
"ds_read_b64_tr_b16 a[38:39], v190 offset:4096\n"
"v_cvt_pk_bf16_f32 v169, v170, v171\n"
"v_mfma_f32_16x16x32_bf16 v[136:139], a[12:15], v[12:15], v[136:139]\n"
"s_addk_i32 s56, 4096\n"
"ds_read_b64_tr_b16 a[40:41], v190 offset:8192\n"
"ds_read_b64_tr_b16 a[42:43], v190 offset:12288\n"
"v_mfma_f32_16x16x32_bf16 v[136:139], a[16:19], v[16:19], v[136:139]\n"
"s_nop 0\n"
"buffer_load_dwordx4 v188, s[44:47], s56 offen sc1 lds\n"
"v_mfma_f32_16x16x32_bf16 v[136:139], a[20:23], v[20:23], v[136:139]\n"
"s_addk_i32 m0, 4096\n"
"ds_read_b64_tr_b16 a[44:45], v190 offset:16384\n"
"v_add_u32 v193, 49152, v193\n"
"v_mfma_f32_16x16x32_bf16 v[140:143], a[12:15], v[68:71], v[140:143]\n"
"s_addk_i32 s56, 4096\n"
"ds_read_b64_tr_b16 a[46:47], v190 offset:20480\n"
"ds_read_b64_tr_b16 a[48:49], v191 offset:0\n"
"v_mfma_f32_16x16x32_bf16 v[140:143], a[16:19], v[72:75], v[140:143]\n"
"s_nop 0\n"
"buffer_load_dwordx4 v188, s[44:47], s56 offen sc1 lds\n"
"v_mfma_f32_16x16x32_bf16 v[140:143], a[20:23], v[76:79], v[140:143]\n"
"s_waitcnt lgkmcnt(9)\n"
"ds_read_b64_tr_b16 a[50:51], v191 offset:4096\n"
"v_accvgpr_write_b32 a66, v168\n"
"v_mfma_f32_16x16x32_bf16 v[232:235], a[24:27], v[24:27], v[232:235]\n"
"s_addk_i32 m0, 4096\n"
"ds_read_b64_tr_b16 a[52:53], v191 offset:8192\n"
"v_accvgpr_write_b32 a67, v169\n"
"v_mfma_f32_16x16x32_bf16 v[232:235], a[28:31], v[28:31], v[232:235]\n"
"s_waitcnt lgkmcnt(9)\n"
"ds_read_b64_tr_b16 a[54:55], v191 offset:12288\n"
"v_cvt_pk_bf16_f32 v172, v172, v173\n"
"v_mfma_f32_16x16x32_bf16 v[232:235], a[32:35], v[32:35], v[232:235]\n"
"s_addk_i32 s56, 4096\n"
"ds_read_b64_tr_b16 a[56:57], v191 offset:16384\n"
"ds_read_b64_tr_b16 a[58:59], v191 offset:20480\n"
"v_mfma_f32_16x16x32_bf16 v[236:239], a[24:27], v[80:83], v[236:239]\n"
"s_nop 0\n"
"buffer_load_dwordx4 v188, s[44:47], s56 offen sc1 lds\n"
"v_mfma_f32_16x16x32_bf16 v[236:239], a[28:31], v[84:87], v[236:239]\n"
"s_addk_i32 m0, 4096\n"
"ds_read_b64_tr_b16 a[0:1], v192 offset:0\n"
"v_add_u32 v194, 49152, v194\n"
"v_mfma_f32_16x16x32_bf16 v[236:239], a[32:35], v[88:91], v[236:239]\n"
"s_waitcnt lgkmcnt(9)\n"
"ds_read_b64_tr_b16 a[2:3], v192 offset:4096\n"
"v_cvt_pk_bf16_f32 v173, v174, v175\n"
"v_mfma_f32_16x16x32_bf16 v[240:243], a[36:39], v[24:27], v[240:243]\n"
"s_add_u32 s56, s6, 122880\n"
"ds_read_b64_tr_b16 a[4:5], v192 offset:8192\n"
"ds_read_b64_tr_b16 a[6:7], v192 offset:12288\n"
"v_mfma_f32_16x16x32_bf16 v[240:243], a[40:43], v[28:31], v[240:243]\n"
"103:\n"
"s_cbranch_execz 107f\n"
"s_waitcnt lgkmcnt(10)\n"
"buffer_load_dwordx4 v188, s[44:47], s56 offen sc1 lds\n"
"v_mfma_f32_16x16x32_bf16 v[240:243], a[44:47], v[32:35], v[240:243]\n"
"s_addk_i32 m0, 4096\n"
"ds_read_b64_tr_b16 a[8:9], v192 offset:16384\n"
"v_add_u32 v195, 49152, v195\n"
"v_mfma_f32_16x16x32_bf16 v[244:247], a[36:39], v[80:83], v[244:247]\n"
"s_addk_i32 s56, 4096\n"
"ds_read_b64_tr_b16 a[10:11], v192 offset:20480\n"
"ds_read_b64_tr_b16 a[12:13], v193 offset:0\n"
"v_mfma_f32_16x16x32_bf16 v[244:247], a[40:43], v[84:87], v[244:247]\n"
"s_nop 0\n"
"buffer_load_dwordx4 v188, s[44:47], s56 offen sc1 lds\n"
"v_mfma_f32_16x16x32_bf16 v[244:247], a[44:47], v[88:91], v[244:247]\n"
"s_waitcnt lgkmcnt(9)\n"
"ds_read_b64_tr_b16 a[14:15], v193 offset:4096\n"
"v_accvgpr_write_b32 a108, v172\n"
"v_mfma_f32_16x16x32_bf16 v[248:251], a[48:51], v[24:27], v[248:251]\n"
"s_addk_i32 m0, 4096\n"
"ds_read_b64_tr_b16 a[16:17], v193 offset:8192\n"
"v_accvgpr_write_b32 a109, v173\n"
"v_mfma_f32_16x16x32_bf16 v[248:251], a[52:55], v[28:31], v[248:251]\n"
"s_waitcnt lgkmcnt(9)\n"
"ds_read_b64_tr_b16 a[18:19], v193 offset:12288\n"
"s_addk_i32 s56, 4096\n"
"v_mfma_f32_16x16x32_bf16 v[248:251], a[56:59], v[32:35], v[248:251]\n"
"s_nop 0\n"
"ds_read_b64_tr_b16 a[20:21], v193 offset:16384\n"
"ds_read_b64_tr_b16 a[22:23], v193 offset:20480\n"
"v_mfma_f32_16x16x32_bf16 v[252:255], a[48:51], v[80:83], v[252:255]\n"
"s_nop 0\n"
"buffer_load_dwordx4 v188, s[44:47], s56 offen sc1 lds\n"
"v_mfma_f32_16x16x32_bf16 v[252:255], a[52:55], v[84:87], v[252:255]\n"
"s_addk_i32 m0, 4096\n"
"ds_read_b64_tr_b16 a[24:25], v194 offset:0\n"
"s_addk_i32 s56, 4096\n"
"v_mfma_f32_16x16x32_bf16 v[252:255], a[56:59], v[88:91], v[252:255]\n"
"s_waitcnt lgkmcnt(9)\n"
"ds_read_b64_tr_b16 a[26:27], v194 offset:4096\n"
"v_mfma_f32_16x16x32_bf16 v[112:115], a[0:3], v[24:27], v[112:115]\n"
"s_nop 0\n"
"ds_read_b64_tr_b16 a[28:29], v194 offset:8192\n"
"ds_read_b64_tr_b16 a[30:31], v194 offset:12288\n"
"v_mfma_f32_16x16x32_bf16 v[112:115], a[4:7], v[28:31], v[112:115]\n"
"s_waitcnt lgkmcnt(10)\n"
"ds_read_b64_tr_b16 a[32:33], v194 offset:16384\n"
"ds_read_b64_tr_b16 a[34:35], v194 offset:20480\n"
"v_mfma_f32_16x16x32_bf16 v[112:115], a[8:11], v[32:35], v[112:115]\n"
"s_nop 0\n"
"ds_read_b64_tr_b16 a[36:37], v195 offset:0\n"
"ds_read_b64_tr_b16 a[38:39], v195 offset:4096\n"
"v_mfma_f32_16x16x32_bf16 v[116:119], a[0:3], v[80:83], v[116:119]\n"
"s_nop 0\n"
"ds_read_b64_tr_b16 a[40:41], v195 offset:8192\n"
"ds_read_b64_tr_b16 a[42:43], v195 offset:12288\n"
"v_mfma_f32_16x16x32_bf16 v[116:119], a[4:7], v[84:87], v[116:119]\n"
"s_nop 0\n"
"ds_read_b64_tr_b16 a[44:45], v195 offset:16384\n"
"ds_read_b64_tr_b16 a[46:47], v195 offset:20480\n"
"v_mfma_f32_16x16x32_bf16 v[116:119], a[8:11], v[88:91], v[116:119]\n"
"s_waitcnt lgkmcnt(14)\n"
"buffer_load_dwordx4 v188, s[44:47], s56 offen sc1 lds\n"
"v_mfma_f32_16x16x32_bf16 v[120:123], a[12:15], v[24:27], v[120:123]\n"
"s_addk_i32 m0, 4096\n"
"s_addk_i32 s56, 4096\n"
"v_mfma_f32_16x16x32_bf16 v[120:123], a[16:19], v[28:31], v[120:123]\n"
"s_waitcnt lgkmcnt(12)\n"
"buffer_load_dwordx4 v188, s[44:47], s56 offen sc1 lds\n"
"v_mfma_f32_16x16x32_bf16 v[120:123], a[20:23], v[32:35], v[120:123]\n"
"s_addk_i32 m0, 4096\n"
"s_addk_i32 s56, 4096\n"
"v_mfma_f32_16x16x32_bf16 v[124:127], a[12:15], v[80:83], v[124:127]\n"
"s_waitcnt vmcnt(11) lgkmcnt(0)\n"
"v_mfma_f32_16x16x32_bf16 v[124:127], a[16:19], v[84:87], v[124:127]\n"
"s_barrier\n"
"ds_read_b64_tr_b16 a[48:49], v189 offset:24576\n"
"ds_read_b64_tr_b16 a[50:51], v189 offset:28672\n"
"v_mfma_f32_16x16x32_bf16 v[124:127], a[20:23], v[88:91], v[124:127]\n"
"s_nop 0\n"
"buffer_load_dwordx4 v188, s[44:47], s56 offen sc1 lds\n"
"v_mfma_f32_16x16x32_bf16 v[128:131], a[24:27], v[24:27], v[128:131]\n"
"s_add_u32 m0, s55, 0\n"
"ds_read_b64_tr_b16 a[52:53], v189 offset:32768\n"
"v_lshlrev_b32 v181, 7, v187\n"
"v_mfma_f32_16x16x32_bf16 v[128:131], a[28:31], v[28:31], v[128:131]\n"
"s_mov_b32 s56, 128\n"
"ds_read_b64_tr_b16 a[54:55], v189 offset:36864\n"
"v_min_u32 v181, 2048, v181\n"
"v_mfma_f32_16x16x32_bf16 v[128:131], a[32:35], v[32:35], v[128:131]\n"
"s_add_u32 s79, s63, 0\n"
"ds_read_b64_tr_b16 a[56:57], v189 offset:40960\n"
"ds_read_b64_tr_b16 a[58:59], v189 offset:45056\n"
"v_mfma_f32_16x16x32_bf16 v[132:135], a[24:27], v[80:83], v[132:135]\n"
"s_mul_i32 s79, s79, 2176\n"
"buffer_load_dwordx4 v180, s[24:27], s56 offen lds\n"
"v_mfma_f32_16x16x32_bf16 v[132:135], a[28:31], v[84:87], v[132:135]\n"
"s_addk_i32 m0, 4096\n"
"ds_read_b64_tr_b16 a[0:1], v190 offset:24576\n"
"s_min_u32 s79, s79, 329600\n"
"v_mfma_f32_16x16x32_bf16 v[132:135], a[32:35], v[88:91], v[132:135]\n"
"s_add_u32 s56, s56, s62\n"
"ds_read_b64_tr_b16 a[2:3], v190 offset:28672\n"
"s_lshr_b32 s80, s53, 0\n"
"v_mfma_f32_16x16x32_bf16 v[136:139], a[36:39], v[24:27], v[136:139]\n"
"s_mul_i32 s80, s80, 331776\n"
"ds_read_b64_tr_b16 a[4:5], v190 offset:32768\n"
"s_add_u32 s79, s79, s80\n"
"v_mfma_f32_16x16x32_bf16 v[136:139], a[40:43], v[28:31], v[136:139]\n"
"s_nop 0\n"
"ds_read_b64_tr_b16 a[6:7], v190 offset:36864\n"
"ds_read_b64_tr_b16 a[8:9], v190 offset:40960\n"
"v_mfma_f32_16x16x32_bf16 v[136:139], a[44:47], v[32:35], v[136:139]\n"
"s_nop 0\n"
"buffer_load_dword v203, v181, s[36:39], s79 offen\n"
"v_mfma_f32_16x16x32_bf16 v[140:143], a[36:39], v[80:83], v[140:143]\n"
"s_nop 0\n"
"ds_read_b64_tr_b16 a[10:11], v190 offset:45056\n"
"ds_read_b64_tr_b16 a[12:13], v191 offset:24576\n"
"v_mfma_f32_16x16x32_bf16 v[140:143], a[40:43], v[84:87], v[140:143]\n"
"s_nop 0\n"
"ds_read_b64_tr_b16 a[14:15], v191 offset:28672\n"
"ds_read_b64_tr_b16 a[16:17], v191 offset:32768\n"
"v_mfma_f32_16x16x32_bf16 v[140:143], a[44:47], v[88:91], v[140:143]\n"
"s_waitcnt lgkmcnt(11)\n"
"ds_read_b64_tr_b16 a[18:19], v191 offset:36864\n"
"v_mfma_f32_16x16x32_bf16 v[232:235], a[48:51], v[36:39], v[232:235]\n"
"ds_read_b64_tr_b16 a[20:21], v191 offset:40960\n"
"v_mfma_f32_16x16x32_bf16 v[232:235], a[52:55], v[40:43], v[232:235]\n"
"s_waitcnt lgkmcnt(11)\n"
"ds_read_b64_tr_b16 a[22:23], v191 offset:45056\n"
"v_mfma_f32_16x16x32_bf16 v[232:235], a[56:59], v[44:47], v[232:235]\n"
"ds_read_b64_tr_b16 a[24:25], v192 offset:24576\n"
"v_mfma_f32_16x16x32_bf16 v[236:239], a[48:51], v[92:95], v[236:239]\n"
"ds_read_b64_tr_b16 a[26:27], v192 offset:28672\n"
"v_mfma_f32_16x16x32_bf16 v[236:239], a[52:55], v[96:99], v[236:239]\n"
"ds_read_b64_tr_b16 a[28:29], v192 offset:32768\n"
"v_mfma_f32_16x16x32_bf16 v[236:239], a[56:59], v[100:103], v[236:239]\n"
"s_waitcnt lgkmcnt(11)\n"
"ds_read_b64_tr_b16 a[30:31], v192 offset:36864\n"
"v_mfma_f32_16x16x32_bf16 v[240:243], a[0:3], v[36:39], v[240:243]\n"
"s_nop 0\n"
"buffer_load_dword v203, v196, s[24:27], s99 offen offset:448\n"
"v_mfma_f32_16x16x32_bf16 v[240:243], a[4:7], v[40:43], v[240:243]\n"
"s_waitcnt lgkmcnt(10)\n"
"ds_read_b64_tr_b16 a[32:33], v192 offset:40960\n"
"ds_read_b64_tr_b16 a[34:35], v192 offset:45056\n"
"v_mfma_f32_16x16x32_bf16 v[240:243], a[8:11], v[44:47], v[240:243]\n"
"ds_read_b64_tr_b16 a[36:37], v193 offset:24576\n"
"v_mfma_f32_16x16x32_bf16 v[244:247], a[0:3], v[92:95], v[244:247]\n"
"ds_read_b64_tr_b16 a[38:39], v193 offset:28672\n"
"v_mfma_f32_16x16x32_bf16 v[244:247], a[4:7], v[96:99], v[244:247]\n"
"ds_read_b64_tr_b16 a[40:41], v193 offset:32768\n"
"v_mfma_f32_16x16x32_bf16 v[244:247], a[8:11], v[100:103], v[244:247]\n"
"s_waitcnt lgkmcnt(11)\n"
"ds_read_b64_tr_b16 a[42:43], v193 offset:36864\n"
"v_mfma_f32_16x16x32_bf16 v[248:251], a[12:15], v[36:39], v[248:251]\n"
"ds_read_b64_tr_b16 a[44:45], v193 offset:40960\n"
"v_add_u32 v189, 49152, v189\n"
"v_mfma_f32_16x16x32_bf16 v[248:251], a[16:19], v[40:43], v[248:251]\n"
"s_waitcnt lgkmcnt(11)\n"
"ds_read_b64_tr_b16 a[46:47], v193 offset:45056\n"
"v_mfma_f32_16x16x32_bf16 v[248:251], a[20:23], v[44:47], v[248:251]\n"
"ds_read_b64_tr_b16 a[48:49], v194 offset:24576\n"
"v_mfma_f32_16x16x32_bf16 v[252:255], a[12:15], v[92:95], v[252:255]\n"
"ds_read_b64_tr_b16 a[50:51], v194 offset:28672\n"
"v_mfma_f32_16x16x32_bf16 v[252:255], a[16:19], v[96:99], v[252:255]\n"
"s_nop 0\n"
"ds_read_b64_tr_b16 a[52:53], v194 offset:32768\n"
"ds_read_b64_tr_b16 a[54:55], v194 offset:36864\n"
"v_mfma_f32_16x16x32_bf16 v[252:255], a[20:23], v[100:103], v[252:255]\n"
"s_waitcnt lgkmcnt(12)\n"
"buffer_load_dword v203, v196, s[28:31], s99 offen offset:448\n"
"v_mfma_f32_16x16x32_bf16 v[112:115], a[24:27], v[36:39], v[112:115]\n"
"s_nop 0\n"
"buffer_load_dwordx4 v180, s[24:27], s56 offen lds\n"
"v_mfma_f32_16x16x32_bf16 v[112:115], a[28:31], v[40:43], v[112:115]\n"
"s_waitcnt lgkmcnt(10)\n"
"ds_read_b64_tr_b16 a[56:57], v194 offset:40960\n"
"ds_read_b64_tr_b16 a[58:59], v194 offset:45056\n"
"v_mfma_f32_16x16x32_bf16 v[112:115], a[32:35], v[44:47], v[112:115]\n"
"s_addk_i32 m0, 4096\n"
"ds_read_b64_tr_b16 a[0:1], v195 offset:24576\n"
"ds_read_b64_tr_b16 a[2:3], v195 offset:28672\n"
"v_mfma_f32_16x16x32_bf16 v[116:119], a[24:27], v[92:95], v[116:119]\n"
"s_add_u32 s56, s56, s62\n"
"ds_read_b64_tr_b16 a[4:5], v195 offset:32768\n"
"ds_read_b64_tr_b16 a[6:7], v195 offset:36864\n"
"v_mfma_f32_16x16x32_bf16 v[116:119], a[28:31], v[96:99], v[116:119]\n"
"s_nop 0\n"
"ds_read_b64_tr_b16 a[8:9], v195 offset:40960\n"
"ds_read_b64_tr_b16 a[10:11], v195 offset:45056\n"
"v_mfma_f32_16x16x32_bf16 v[116:119], a[32:35], v[100:103], v[116:119]\n"
"s_waitcnt lgkmcnt(14)\n"
"v_add_u32 v190, 49152, v190\n"
"v_add_u32 v191, 49152, v191\n"
"v_mfma_f32_16x16x32_bf16 v[120:123], a[36:39], v[36:39], v[120:123]\n"
"v_mfma_f32_16x16x32_bf16 v[120:123], a[40:43], v[40:43], v[120:123]\n"
"s_waitcnt lgkmcnt(12)\n"
"v_mfma_f32_16x16x32_bf16 v[120:123], a[44:47], v[44:47], v[120:123]\n"
"s_nop 0\n"
"buffer_load_dwordx4 v180, s[24:27], s56 offen lds\n"
"v_mfma_f32_16x16x32_bf16 v[124:127], a[36:39], v[92:95], v[124:127]\n"
"s_waitcnt vmcnt(12) lgkmcnt(0)\n"
"s_addk_i32 m0, 4096\n"
"s_add_u32 s56, s56, s62\n"
"v_mfma_f32_16x16x32_bf16 v[124:127], a[40:43], v[96:99], v[124:127]\n"
"s_barrier\n"
"ds_read_b64_tr_b16 a[12:13], v189 offset:0\n"
"v_mfma_f32_16x16x32_bf16 v[124:127], a[44:47], v[100:103], v[124:127]\n"
"ds_read_b64_tr_b16 a[14:15], v189 offset:4096\n"
"v_mfma_f32_16x16x32_bf16 v[128:131], a[48:51], v[36:39], v[128:131]\n"
"ds_read_b64_tr_b16 a[16:17], v189 offset:8192\n"
"v_mfma_f32_16x16x32_bf16 v[128:131], a[52:55], v[40:43], v[128:131]\n"
"ds_read_b64_tr_b16 a[18:19], v189 offset:12288\n"
"v_mfma_f32_16x16x32_bf16 v[128:131], a[56:59], v[44:47], v[128:131]\n"
"ds_read_b64_tr_b16 a[20:21], v189 offset:16384\n"
"v_add_u32 v192, 49152, v192\n"
"v_mfma_f32_16x16x32_bf16 v[132:135], a[48:51], v[92:95], v[132:135]\n"
"ds_read_b64_tr_b16 a[22:23], v189 offset:20480\n"
"v_mfma_f32_16x16x32_bf16 v[132:135], a[52:55], v[96:99], v[132:135]\n"
"ds_read_b64_tr_b16 a[24:25], v190 offset:0\n"
"v_mfma_f32_16x16x32_bf16 v[132:135], a[56:59], v[100:103], v[132:135]\n"
"s_nop 0\n"
"ds_read_b64_tr_b16 a[26:27], v190 offset:4096\n"
"ds_read_b64_tr_b16 a[28:29], v190 offset:8192\n"
"v_mfma_f32_16x16x32_bf16 v[136:139], a[0:3], v[36:39], v[136:139]\n"
"s_nop 0\n"
"buffer_load_dwordx4 v180, s[24:27], s56 offen lds\n"
"v_mfma_f32_16x16x32_bf16 v[136:139], a[4:7], v[40:43], v[136:139]\n"
"s_addk_i32 m0, 4096\n"
"ds_read_b64_tr_b16 a[30:31], v190 offset:12288\n"
"s_add_u32 s56, s56, s62\n"
"v_mfma_f32_16x16x32_bf16 v[136:139], a[8:11], v[44:47], v[136:139]\n"
"ds_read_b64_tr_b16 a[32:33], v190 offset:16384\n"
"v_add_u32 v193, 49152, v193\n"
"v_mfma_f32_16x16x32_bf16 v[140:143], a[0:3], v[92:95], v[140:143]\n"
"ds_read_b64_tr_b16 a[34:35], v190 offset:20480\n"
"v_mfma_f32_16x16x32_bf16 v[140:143], a[4:7], v[96:99], v[140:143]\n"
"ds_read_b64_tr_b16 a[36:37], v191 offset:0\n"
"v_mfma_f32_16x16x32_bf16 v[140:143], a[8:11], v[100:103], v[140:143]\n"
"s_waitcnt lgkmcnt(9)\n"
"ds_read_b64_tr_b16 a[38:39], v191 offset:4096\n"
"v_mfma_f32_16x16x32_bf16 v[232:235], a[12:15], v[48:51], v[232:235]\n"
"ds_read_b64_tr_b16 a[40:41], v191 offset:8192\n"
"v_mfma_f32_16x16x32_bf16 v[232:235], a[16:19], v[52:55], v[232:235]\n"
"s_waitcnt lgkmcnt(9)\n"
"ds_read_b64_tr_b16 a[42:43], v191 offset:12288\n"
"v_mfma_f32_16x16x32_bf16 v[232:235], a[20:23], a[172:175], v[232:235]\n"
"ds_read_b64_tr_b16 a[44:45], v191 offset:16384\n"
"v_add_u32 v194, 49152, v194\n"
"v_mfma_f32_16x16x32_bf16 v[236:239], a[12:15], v[104:107], v[236:239]\n"
"ds_read_b64_tr_b16 a[46:47], v191 offset:20480\n"
"v_mfma_f32_16x16x32_bf16 v[236:239], a[16:19], v[108:111], v[236:239]\n"
"ds_read_b64_tr_b16 a[48:49], v192 offset:0\n"
"v_mfma_f32_16x16x32_bf16 v[236:239], a[20:23], a[188:191], v[236:239]\n"
"s_waitcnt lgkmcnt(9)\n"
"ds_read_b64_tr_b16 a[50:51], v192 offset:4096\n"
"v_mfma_f32_16x16x32_bf16 v[240:243], a[24:27], v[48:51], v[240:243]\n"
"ds_read_b64_tr_b16 a[52:53], v192 offset:8192\n"
"v_mfma_f32_16x16x32_bf16 v[240:243], a[28:31], v[52:55], v[240:243]\n"
"s_waitcnt lgkmcnt(9)\n"
"ds_read_b64_tr_b16 a[54:55], v192 offset:12288\n"
"v_mfma_f32_16x16x32_bf16 v[240:243], a[32:35], a[172:175], v[240:243]\n"
"ds_read_b64_tr_b16 a[56:57], v192 offset:16384\n"
"v_add_u32 v195, 49152, v195\n"
"v_mfma_f32_16x16x32_bf16 v[244:247], a[24:27], v[104:107], v[244:247]\n"
"ds_read_b64_tr_b16 a[58:59], v192 offset:20480\n"
"v_mfma_f32_16x16x32_bf16 v[244:247], a[28:31], v[108:111], v[244:247]\n"
"ds_read_b64_tr_b16 a[0:1], v193 offset:0\n"
"v_mfma_f32_16x16x32_bf16 v[244:247], a[32:35], a[188:191], v[244:247]\n"
"s_waitcnt lgkmcnt(9)\n"
"ds_read_b64_tr_b16 a[2:3], v193 offset:4096\n"
"v_mfma_f32_16x16x32_bf16 v[248:251], a[36:39], v[48:51], v[248:251]\n"
"ds_read_b64_tr_b16 a[4:5], v193 offset:8192\n"
"v_mfma_f32_16x16x32_bf16 v[248:251], a[40:43], v[52:55], v[248:251]\n"
"s_waitcnt lgkmcnt(9)\n"
"ds_read_b64_tr_b16 a[6:7], v193 offset:12288\n"
"v_mfma_f32_16x16x32_bf16 v[248:251], a[44:47], a[172:175], v[248:251]\n"
"ds_read_b64_tr_b16 a[8:9], v193 offset:16384\n"
"v_mfma_f32_16x16x32_bf16 v[252:255], a[36:39], v[104:107], v[252:255]\n"
"ds_read_b64_tr_b16 a[10:11], v193 offset:20480\n"
"v_mfma_f32_16x16x32_bf16 v[252:255], a[40:43], v[108:111], v[252:255]\n"
"ds_read_b64_tr_b16 a[12:13], v194 offset:0\n"
"v_mfma_f32_16x16x32_bf16 v[252:255], a[44:47], a[188:191], v[252:255]\n"
"s_waitcnt lgkmcnt(9)\n"
"ds_read_b64_tr_b16 a[14:15], v194 offset:4096\n"
"v_mfma_f32_16x16x32_bf16 v[112:115], a[48:51], v[48:51], v[112:115]\n"
"s_nop 0\n"
"ds_read_b64_tr_b16 a[16:17], v194 offset:8192\n"
"ds_read_b64_tr_b16 a[18:19], v194 offset:12288\n"
"v_mfma_f32_16x16x32_bf16 v[112:115], a[52:55], v[52:55], v[112:115]\n"
"s_waitcnt lgkmcnt(10)\n"
"ds_read_b64_tr_b16 a[20:21], v194 offset:16384\n"
"ds_read_b64_tr_b16 a[22:23], v194 offset:20480\n"
"v_mfma_f32_16x16x32_bf16 v[112:115], a[56:59], a[172:175], v[112:115]\n"
"s_nop 0\n"
"ds_read_b64_tr_b16 a[24:25], v195 offset:0\n"
"ds_read_b64_tr_b16 a[26:27], v195 offset:4096\n"
"v_mfma_f32_16x16x32_bf16 v[116:119], a[48:51], v[104:107], v[116:119]\n"
"s_nop 0\n"
"ds_read_b64_tr_b16 a[28:29], v195 offset:8192\n"
"ds_read_b64_tr_b16 a[30:31], v195 offset:12288\n"
"v_mfma_f32_16x16x32_bf16 v[116:119], a[52:55], v[108:111], v[116:119]\n"
"s_nop 0\n"
"ds_read_b64_tr_b16 a[32:33], v195 offset:16384\n"
"ds_read_b64_tr_b16 a[34:35], v195 offset:20480\n"
"v_mfma_f32_16x16x32_bf16 v[116:119], a[56:59], a[188:191], v[116:119]\n"
"s_waitcnt lgkmcnt(14)\n"
"v_mfma_f32_16x16x32_bf16 v[120:123], a[0:3], v[48:51], v[120:123]\n"
"v_mfma_f32_16x16x32_bf16 v[120:123], a[4:7], v[52:55], v[120:123]\n"
"s_waitcnt lgkmcnt(12)\n"
"v_mfma_f32_16x16x32_bf16 v[120:123], a[8:11], a[172:175], v[120:123]\n"
"v_mfma_f32_16x16x32_bf16 v[124:127], a[0:3], v[104:107], v[124:127]\n"
"s_waitcnt vmcnt(7) lgkmcnt(0)\n"
"v_mfma_f32_16x16x32_bf16 v[124:127], a[4:7], v[108:111], v[124:127]\n"
"s_barrier\n"
"ds_read_b64_tr_b16 a[36:37], v189 offset:24576\n"
"v_mfma_f32_16x16x32_bf16 v[124:127], a[8:11], a[188:191], v[124:127]\n"
"ds_read_b64_tr_b16 a[38:39], v189 offset:28672\n"
"v_mfma_f32_16x16x32_bf16 v[128:131], a[12:15], v[48:51], v[128:131]\n"
"s_nop 0\n"
"ds_read_b64_tr_b16 a[40:41], v189 offset:32768\n"
"ds_read_b64_tr_b16 a[42:43], v189 offset:36864\n"
"v_mfma_f32_16x16x32_bf16 v[128:131], a[16:19], v[52:55], v[128:131]\n"
"s_nop 0\n"
"buffer_load_dwordx4 v180, s[24:27], s56 offen lds\n"
"v_mfma_f32_16x16x32_bf16 v[128:131], a[20:23], a[172:175], v[128:131]\n"
"s_addk_i32 m0, 4096\n"
"ds_read_b64_tr_b16 a[44:45], v189 offset:40960\n"
"s_add_u32 s56, s56, s62\n"
"v_mfma_f32_16x16x32_bf16 v[132:135], a[12:15], v[104:107], v[132:135]\n"
"ds_read_b64_tr_b16 a[46:47], v189 offset:45056\n"
"v_mfma_f32_16x16x32_bf16 v[132:135], a[16:19], v[108:111], v[132:135]\n"
"ds_read_b64_tr_b16 a[48:49], v190 offset:24576\n"
"v_mfma_f32_16x16x32_bf16 v[132:135], a[20:23], a[188:191], v[132:135]\n"
"s_nop 0\n"
"ds_read_b64_tr_b16 a[50:51], v190 offset:28672\n"
"ds_read_b64_tr_b16 a[52:53], v190 offset:32768\n"
"v_mfma_f32_16x16x32_bf16 v[136:139], a[24:27], v[48:51], v[136:139]\n"
"s_nop 0\n"
"buffer_load_dwordx4 v180, s[24:27], s56 offen lds\n"
"v_mfma_f32_16x16x32_bf16 v[136:139], a[28:31], v[52:55], v[136:139]\n"
"s_addk_i32 m0, 4096\n"
"ds_read_b64_tr_b16 a[54:55], v190 offset:36864\n"
"s_mov_b32 s56, 1769600\n"
"v_mfma_f32_16x16x32_bf16 v[136:139], a[32:35], a[172:175], v[136:139]\n"
"ds_read_b64_tr_b16 a[56:57], v190 offset:40960\n"
"v_mfma_f32_16x16x32_bf16 v[140:143], a[24:27], v[104:107], v[140:143]\n"
"ds_read_b64_tr_b16 a[58:59], v190 offset:45056\n"
"v_mfma_f32_16x16x32_bf16 v[140:143], a[28:31], v[108:111], v[140:143]\n"
"s_nop 0\n"
"ds_read_b64_tr_b16 a[0:1], v191 offset:24576\n"
"ds_read_b64_tr_b16 a[2:3], v191 offset:28672\n"
"v_mfma_f32_16x16x32_bf16 v[140:143], a[32:35], a[188:191], v[140:143]\n"
"s_waitcnt lgkmcnt(10)\n"
"buffer_load_dwordx4 v180, s[24:27], s56 offen lds\n"
"v_mfma_f32_16x16x32_bf16 v[232:235], a[36:39], a[176:179], v[232:235]\n"
"s_addk_i32 m0, 4096\n"
"ds_read_b64_tr_b16 a[4:5], v191 offset:32768\n"
"s_add_u32 s56, s56, s62\n"
"v_mfma_f32_16x16x32_bf16 v[232:235], a[40:43], a[180:183], v[232:235]\n"
"s_waitcnt lgkmcnt(9)\n"
"ds_read_b64_tr_b16 a[6:7], v191 offset:36864\n"
"v_mfma_f32_16x16x32_bf16 v[232:235], a[44:47], a[184:187], v[232:235]\n"
"ds_read_b64_tr_b16 a[8:9], v191 offset:40960\n"
"v_mfma_f32_16x16x32_bf16 v[236:239], a[36:39], a[192:195], v[236:239]\n"
"s_nop 0\n"
"ds_read_b64_tr_b16 a[10:11], v191 offset:45056\n"
"ds_read_b64_tr_b16 a[12:13], v192 offset:24576\n"
"v_mfma_f32_16x16x32_bf16 v[236:239], a[40:43], a[196:199], v[236:239]\n"
"s_nop 0\n"
"buffer_load_dwordx4 v180, s[24:27], s56 offen lds\n"
"v_mfma_f32_16x16x32_bf16 v[236:239], a[44:47], a[200:203], v[236:239]\n"
"s_waitcnt lgkmcnt(9)\n"
"ds_read_b64_tr_b16 a[14:15], v192 offset:28672\n"
"v_cvt_pk_bf16_f32 v204, v232, v233\n"
"v_mfma_f32_16x16x32_bf16 v[240:243], a[48:51], a[176:179], v[240:243]\n"
"s_addk_i32 m0, 4096\n"
"ds_read_b64_tr_b16 a[16:17], v192 offset:32768\n"
"v_cvt_pk_bf16_f32 v205, v234, v235\n"
"v_mfma_f32_16x16x32_bf16 v[240:243], a[52:55], a[180:183], v[240:243]\n"
"s_waitcnt lgkmcnt(9)\n"
"ds_read_b64_tr_b16 a[18:19], v192 offset:36864\n"
"v_cvt_pk_bf16_f32 v218, v236, v237\n"
"v_mfma_f32_16x16x32_bf16 v[240:243], a[56:59], a[184:187], v[240:243]\n"
"s_add_u32 s56, s56, s62\n"
"ds_read_b64_tr_b16 a[20:21], v192 offset:40960\n"
"ds_read_b64_tr_b16 a[22:23], v192 offset:45056\n"
"v_mfma_f32_16x16x32_bf16 v[244:247], a[48:51], a[192:195], v[244:247]\n"
"s_nop 0\n"
"buffer_load_dwordx4 v180, s[24:27], s56 offen lds\n"
"v_mfma_f32_16x16x32_bf16 v[244:247], a[52:55], a[196:199], v[244:247]\n"
"s_addk_i32 m0, 4096\n"
"ds_read_b64_tr_b16 a[24:25], v193 offset:24576\n"
"v_cvt_pk_bf16_f32 v219, v238, v239\n"
"v_mfma_f32_16x16x32_bf16 v[244:247], a[56:59], a[200:203], v[244:247]\n"
"s_waitcnt lgkmcnt(9)\n"
"ds_read_b64_tr_b16 a[26:27], v193 offset:28672\n"
"v_cvt_pk_bf16_f32 v206, v240, v241\n"
"v_mfma_f32_16x16x32_bf16 v[248:251], a[0:3], a[176:179], v[248:251]\n"
"s_add_u32 s56, s56, s62\n"
"ds_read_b64_tr_b16 a[28:29], v193 offset:32768\n"
"v_cvt_pk_bf16_f32 v207, v242, v243\n"
"v_mfma_f32_16x16x32_bf16 v[248:251], a[4:7], a[180:183], v[248:251]\n"
"s_waitcnt lgkmcnt(9)\n"
"ds_read_b64_tr_b16 a[30:31], v193 offset:36864\n"
"ds_read_b64_tr_b16 a[32:33], v193 offset:40960\n"
"v_mfma_f32_16x16x32_bf16 v[248:251], a[8:11], a[184:187], v[248:251]\n"
"s_nop 0\n"
"buffer_load_dwordx4 v180, s[24:27], s56 offen lds\n"
"v_mfma_f32_16x16x32_bf16 v[252:255], a[0:3], a[192:195], v[252:255]\n"
"s_addk_i32 m0, 4096\n"
"ds_read_b64_tr_b16 a[34:35], v193 offset:45056\n"
"v_add_u32 v176, 4294868992, v176\n"
"v_mfma_f32_16x16x32_bf16 v[252:255], a[4:7], a[196:199], v[252:255]\n"
"s_add_u32 s56, s56, s62\n"
"ds_read_b64_tr_b16 a[36:37], v194 offset:24576\n"
"v_cvt_pk_bf16_f32 v220, v244, v245\n"
"v_mfma_f32_16x16x32_bf16 v[252:255], a[8:11], a[200:203], v[252:255]\n"
"s_waitcnt lgkmcnt(9)\n"
"ds_read_b64_tr_b16 a[38:39], v194 offset:28672\n"
"ds_read_b64_tr_b16 a[40:41], v194 offset:32768\n"
"v_mfma_f32_16x16x32_bf16 v[112:115], a[12:15], a[176:179], v[112:115]\n"
"s_nop 0\n"
"ds_read_b64_tr_b16 a[42:43], v194 offset:36864\n"
"ds_read_b64_tr_b16 a[44:45], v194 offset:40960\n"
"v_mfma_f32_16x16x32_bf16 v[112:115], a[16:19], a[180:183], v[112:115]\n"
"s_waitcnt lgkmcnt(11)\n"
"ds_read_b64_tr_b16 a[46:47], v194 offset:45056\n"
"ds_read_b64_tr_b16 a[48:49], v195 offset:24576\n"
"v_mfma_f32_16x16x32_bf16 v[112:115], a[20:23], a[184:187], v[112:115]\n"
"s_nop 0\n"
"ds_read_b64_tr_b16 a[50:51], v195 offset:28672\n"
"ds_read_b64_tr_b16 a[52:53], v195 offset:32768\n"
"v_mfma_f32_16x16x32_bf16 v[116:119], a[12:15], a[192:195], v[116:119]\n"
"s_nop 0\n"
"ds_read_b64_tr_b16 a[54:55], v195 offset:36864\n"
"ds_read_b64_tr_b16 a[56:57], v195 offset:40960\n"
"v_mfma_f32_16x16x32_bf16 v[116:119], a[16:19], a[196:199], v[116:119]\n"
"ds_read_b64_tr_b16 a[58:59], v195 offset:45056\n"
"v_add_u32 v177, 4294868992, v177\n"
"v_mfma_f32_16x16x32_bf16 v[116:119], a[20:23], a[200:203], v[116:119]\n"
"s_waitcnt lgkmcnt(14)\n"
"buffer_load_dwordx4 v180, s[24:27], s56 offen lds\n"
"v_mfma_f32_16x16x32_bf16 v[120:123], a[24:27], a[176:179], v[120:123]\n"
"s_addk_i32 m0, 4096\n"
"v_cvt_pk_bf16_f32 v221, v246, v247\n"
"v_cvt_pk_bf16_f32 v208, v248, v249\n"
"v_mfma_f32_16x16x32_bf16 v[120:123], a[28:31], a[180:183], v[120:123]\n"
"s_waitcnt lgkmcnt(12)\n"
"v_cvt_pk_bf16_f32 v209, v250, v251\n"
"v_cvt_pk_bf16_f32 v222, v252, v253\n"
"v_mfma_f32_16x16x32_bf16 v[120:123], a[32:35], a[184:187], v[120:123]\n"
"s_add_u32 s56, s56, s62\n"
"buffer_load_dwordx4 v180, s[24:27], s56 offen lds\n"
"v_mfma_f32_16x16x32_bf16 v[124:127], a[24:27], a[192:195], v[124:127]\n"
"s_waitcnt vmcnt(6) lgkmcnt(0)\n"
"v_cvt_pk_bf16_f32 v223, v254, v255\n"
"v_cvt_pk_bf16_f32 v210, v112, v113\n"
"v_mfma_f32_16x16x32_bf16 v[124:127], a[28:31], a[196:199], v[124:127]\n"
"s_barrier\n"
"ds_read_b64_tr_b16 a[0:1], v176 offset:0\n"
"v_lshlrev_b32 v181, 7, v187\n"
"v_mfma_f32_16x16x32_bf16 v[124:127], a[32:35], a[200:203], v[124:127]\n"
"s_addk_i32 m0, 4096\n"
"ds_read_b64_tr_b16 a[2:3], v176 offset:2048\n"
"v_min_u32 v181, 2048, v181\n"
"v_mfma_f32_16x16x32_bf16 v[128:131], a[36:39], a[176:179], v[128:131]\n"
"s_mov_b32 s56, 3539072\n"
"ds_read_b64_tr_b16 a[4:5], v176 offset:4096\n"
"ds_read_b64_tr_b16 a[6:7], v176 offset:6144\n"
"v_mfma_f32_16x16x32_bf16 v[128:131], a[40:43], a[180:183], v[128:131]\n"
"s_add_u32 s79, s63, 96\n"
"buffer_load_dwordx4 v180, s[24:27], s56 offen lds\n"
"v_mfma_f32_16x16x32_bf16 v[128:131], a[44:47], a[184:187], v[128:131]\n"
"s_addk_i32 m0, 4096\n"
"ds_read_b64_tr_b16 a[8:9], v176 offset:8192\n"
"s_mul_i32 s79, s79, 2176\n"
"v_mfma_f32_16x16x32_bf16 v[132:135], a[36:39], a[192:195], v[132:135]\n"
"s_add_u32 s56, s56, s62\n"
"ds_read_b64_tr_b16 a[10:11], v176 offset:10240\n"
"ds_read_b64_tr_b16 a[12:13], v176 offset:12288\n"
"v_mfma_f32_16x16x32_bf16 v[132:135], a[40:43], a[196:199], v[132:135]\n"
"s_min_u32 s79, s79, 329600\n"
"buffer_load_dwordx4 v180, s[24:27], s56 offen lds\n"
"v_mfma_f32_16x16x32_bf16 v[132:135], a[44:47], a[200:203], v[132:135]\n"
"s_addk_i32 m0, 4096\n"
"ds_read_b64_tr_b16 a[14:15], v176 offset:14336\n"
"s_lshr_b32 s80, s53, 0\n"
"v_mfma_f32_16x16x32_bf16 v[136:139], a[48:51], a[176:179], v[136:139]\n"
"s_add_u32 s56, s56, s62\n"
"ds_read_b64_tr_b16 a[16:17], v176 offset:16384\n"
"ds_read_b64_tr_b16 a[18:19], v176 offset:18432\n"
"v_mfma_f32_16x16x32_bf16 v[136:139], a[52:55], a[180:183], v[136:139]\n"
"s_mul_i32 s80, s80, 331776\n"
"s_add_u32 s79, s79, s80\n"
"ds_read_b64_tr_b16 a[20:21], v176 offset:20480\n"
"v_mfma_f32_16x16x32_bf16 v[136:139], a[56:59], a[184:187], v[136:139]\n"
"s_nop 0\n"
"buffer_load_dword v203, v181, s[36:39], s79 offen\n"
"v_mfma_f32_16x16x32_bf16 v[140:143], a[48:51], a[192:195], v[140:143]\n"
"s_nop 0\n"
"ds_read_b64_tr_b16 a[22:23], v176 offset:22528\n"
"ds_read_b64_tr_b16 a[24:25], v177 offset:0\n"
"v_mfma_f32_16x16x32_bf16 v[140:143], a[52:55], a[196:199], v[140:143]\n"
"s_nop 0\n"
"buffer_load_dwordx4 v180, s[24:27], s56 offen lds\n"
"v_mfma_f32_16x16x32_bf16 v[140:143], a[56:59], a[200:203], v[140:143]\n"
"s_waitcnt lgkmcnt(9)\n"
"ds_read_b64_tr_b16 a[26:27], v177 offset:2048\n"
"v_add_u32 v178, 4294868992, v178\n"
"v_cvt_pk_bf16_f32 v211, v114, v115\n"
"v_mfma_f32_16x16x32_bf16 v[112:115], a[0:3], v[0:3], 0\n"
"s_addk_i32 m0, 4096\n"
"ds_read_b64_tr_b16 a[28:29], v177 offset:4096\n"
"v_lshlrev_b32 v181, 7, v187\n"
"v_mfma_f32_16x16x32_bf16 v[112:115], a[4:7], v[4:7], v[112:115]\n"
"s_waitcnt lgkmcnt(7)\n"
"ds_read_b64_tr_b16 a[30:31], v177 offset:6144\n"
"v_min_u32 v181, 2048, v181\n"
"v_mfma_f32_16x16x32_bf16 v[112:115], a[8:11], v[8:11], v[112:115]\n"
"s_add_u32 s56, s56, s62\n"
"ds_read_b64_tr_b16 a[32:33], v177 offset:8192\n"
"ds_read_b64_tr_b16 a[34:35], v177 offset:10240\n"
"v_mfma_f32_16x16x32_bf16 v[112:115], a[12:15], v[12:15], v[112:115]\n"
"s_waitcnt lgkmcnt(6)\n"
"buffer_load_dwordx4 v180, s[24:27], s56 offen lds\n"
"v_mfma_f32_16x16x32_bf16 v[112:115], a[16:19], v[16:19], v[112:115]\n"
"s_addk_i32 m0, 4096\n"
"ds_read_b64_tr_b16 a[36:37], v177 offset:12288\n"
"s_add_u32 s79, s63, 32\n"
"v_mfma_f32_16x16x32_bf16 v[112:115], a[20:23], v[20:23], v[112:115]\n"
"s_add_u32 s56, s56, s62\n"
"ds_read_b64_tr_b16 a[38:39], v177 offset:14336\n"
"s_mul_i32 s79, s79, 2176\n"
"v_cvt_pk_bf16_f32 v224, v116, v117\n"
"v_cvt_pk_bf16_f32 v225, v118, v119\n"
"v_mfma_f32_16x16x32_bf16 v[116:119], a[0:3], v[56:59], 0\n"
"s_min_u32 s79, s79, 329600\n"
"ds_read_b64_tr_b16 a[40:41], v177 offset:16384\n"
"s_lshr_b32 s80, s53, 0\n"
"v_mfma_f32_16x16x32_bf16 v[116:119], a[4:7], v[60:63], v[116:119]\n"
"s_mul_i32 s80, s80, 331776\n"
"ds_read_b64_tr_b16 a[42:43], v177 offset:18432\n"
"s_add_u32 s79, s79, s80\n"
"v_mfma_f32_16x16x32_bf16 v[116:119], a[8:11], v[64:67], v[116:119]\n"
"ds_read_b64_tr_b16 a[44:45], v177 offset:20480\n"
"v_add_u32 v179, 4294868992, v179\n"
"v_mfma_f32_16x16x32_bf16 v[116:119], a[12:15], v[68:71], v[116:119]\n"
"s_nop 0\n"
"ds_read_b64_tr_b16 a[46:47], v177 offset:22528\n"
"ds_read_b64_tr_b16 a[48:49], v178 offset:0\n"
"v_mfma_f32_16x16x32_bf16 v[116:119], a[16:19], v[72:75], v[116:119]\n"
"s_nop 0\n"
"buffer_load_dword v203, v181, s[36:39], s79 offen\n"
"v_mfma_f32_16x16x32_bf16 v[116:119], a[20:23], v[76:79], v[116:119]\n"
"s_waitcnt lgkmcnt(9)\n"
"ds_read_b64_tr_b16 a[50:51], v178 offset:2048\n"
"v_cvt_pk_bf16_f32 v212, v120, v121\n"
"v_cvt_pk_bf16_f32 v213, v122, v123\n"
"v_mfma_f32_16x16x32_bf16 v[120:123], a[24:27], v[0:3], 0\n"
"ds_read_b64_tr_b16 a[52:53], v178 offset:4096\n"
"v_cvt_pk_bf16_f32 v226, v124, v125\n"
"v_mfma_f32_16x16x32_bf16 v[120:123], a[28:31], v[4:7], v[120:123]\n"
"s_waitcnt lgkmcnt(7)\n"
"ds_read_b64_tr_b16 a[54:55], v178 offset:6144\n"
"v_cvt_pk_bf16_f32 v227, v126, v127\n"
"v_mfma_f32_16x16x32_bf16 v[120:123], a[32:35], v[8:11], v[120:123]\n"
"ds_read_b64_tr_b16 a[56:57], v178 offset:8192\n"
"v_cvt_pk_bf16_f32 v214, v128, v129\n"
"v_mfma_f32_16x16x32_bf16 v[120:123], a[36:39], v[12:15], v[120:123]\n"
"s_waitcnt lgkmcnt(5)\n"
"ds_read_b64_tr_b16 a[58:59], v178 offset:10240\n"
"v_cvt_pk_bf16_f32 v215, v130, v131\n"
"v_mfma_f32_16x16x32_bf16 v[120:123], a[40:43], v[16:19], v[120:123]\n"
"ds_read_b64_tr_b16 a[0:1], v178 offset:12288\n"
"v_cvt_pk_bf16_f32 v228, v132, v133\n"
"v_mfma_f32_16x16x32_bf16 v[120:123], a[44:47], v[20:23], v[120:123]\n"
"ds_read_b64_tr_b16 a[2:3], v178 offset:14336\n"
"v_cvt_pk_bf16_f32 v229, v134, v135\n"
"v_mfma_f32_16x16x32_bf16 v[124:127], a[24:27], v[56:59], 0\n"
"s_nop 0\n"
"ds_read_b64_tr_b16 a[4:5], v178 offset:16384\n"
"ds_read_b64_tr_b16 a[6:7], v178 offset:18432\n"
"v_mfma_f32_16x16x32_bf16 v[124:127], a[28:31], v[60:63], v[124:127]\n"
"104:\n"
"s_cbranch_execz 108f\n"
"s_nop 0\n"
"buffer_load_dword v203, v196, s[24:27], s99 offen offset:320\n"
"v_mfma_f32_16x16x32_bf16 v[124:127], a[32:35], v[64:67], v[124:127]\n"
"s_nop 0\n"
"ds_read_b64_tr_b16 a[8:9], v178 offset:20480\n"
"ds_read_b64_tr_b16 a[10:11], v178 offset:22528\n"
"v_mfma_f32_16x16x32_bf16 v[124:127], a[36:39], v[68:71], v[124:127]\n"
"s_nop 0\n"
"buffer_load_dwordx4 v180, s[24:27], s56 offen lds\n"
"v_mfma_f32_16x16x32_bf16 v[124:127], a[40:43], v[72:75], v[124:127]\n"
"s_addk_i32 m0, 4096\n"
"ds_read_b64_tr_b16 a[12:13], v179 offset:0\n"
"v_cvt_pk_bf16_f32 v216, v136, v137\n"
"v_mfma_f32_16x16x32_bf16 v[124:127], a[44:47], v[76:79], v[124:127]\n"
"s_waitcnt lgkmcnt(9)\n"
"ds_read_b64_tr_b16 a[14:15], v179 offset:2048\n"
"v_cvt_pk_bf16_f32 v217, v138, v139\n"
"v_mfma_f32_16x16x32_bf16 v[128:131], a[48:51], v[0:3], 0\n"
"s_add_u32 s56, s56, s62\n"
"ds_read_b64_tr_b16 a[16:17], v179 offset:4096\n"
"ds_read_b64_tr_b16 a[18:19], v179 offset:6144\n"
"v_mfma_f32_16x16x32_bf16 v[128:131], a[52:55], v[4:7], v[128:131]\n"
"s_waitcnt lgkmcnt(8)\n"
"ds_read_b64_tr_b16 a[20:21], v179 offset:8192\n"
"ds_read_b64_tr_b16 a[22:23], v179 offset:10240\n"
"v_mfma_f32_16x16x32_bf16 v[128:131], a[56:59], v[8:11], v[128:131]\n"
"s_nop 0\n"
"ds_read_b64_tr_b16 a[24:25], v179 offset:12288\n"
"ds_read_b64_tr_b16 a[26:27], v179 offset:14336\n"
"v_mfma_f32_16x16x32_bf16 v[128:131], a[0:3], v[12:15], v[128:131]\n"
"s_waitcnt lgkmcnt(8)\n"
"ds_read_b64_tr_b16 a[28:29], v179 offset:16384\n"
"ds_read_b64_tr_b16 a[30:31], v179 offset:18432\n"
"v_mfma_f32_16x16x32_bf16 v[128:131], a[4:7], v[16:19], v[128:131]\n"
"s_nop 0\n"
"ds_read_b64_tr_b16 a[32:33], v179 offset:20480\n"
"ds_read_b64_tr_b16 a[34:35], v179 offset:22528\n"
"v_mfma_f32_16x16x32_bf16 v[128:131], a[8:11], v[20:23], v[128:131]\n"
"s_nop 0\n"
"buffer_load_dwordx4 v180, s[24:27], s56 offen lds\n"
"v_mfma_f32_16x16x32_bf16 v[132:135], a[48:51], v[56:59], 0\n"
"s_addk_i32 m0, 4096\n"
"v_cvt_pk_bf16_f32 v230, v140, v141\n"
"v_cvt_pk_bf16_f32 v231, v142, v143\n"
"v_mfma_f32_16x16x32_bf16 v[132:135], a[52:55], v[60:63], v[132:135]\n"
"s_mov_b32 s56, 128\n"
"v_accvgpr_read_b32 v189, a234\n"
"v_accvgpr_read_b32 v190, a235\n"
"v_mfma_f32_16x16x32_bf16 v[132:135], a[56:59], v[64:67], v[132:135]\n"
"v_accvgpr_read_b32 v191, a236\n"
"v_accvgpr_read_b32 v192, a237\n"
"v_mfma_f32_16x16x32_bf16 v[132:135], a[0:3], v[68:71], v[132:135]\n"
"s_waitcnt vmcnt(9) lgkmcnt(0)\n"
"v_accvgpr_read_b32 v193, a238\n"
"v_accvgpr_read_b32 v194, a239\n"
"v_mfma_f32_16x16x32_bf16 v[132:135], a[4:7], v[72:75], v[132:135]\n"
"s_barrier\n"
"ds_read_b64_tr_b16 a[36:37], v176 offset:24576\n"
"v_accvgpr_read_b32 v195, a240\n"
"v_mfma_f32_16x16x32_bf16 v[132:135], a[8:11], v[76:79], v[132:135]\n"
"s_nop 0\n"
"ds_read_b64_tr_b16 a[38:39], v176 offset:26624\n"
"ds_read_b64_tr_b16 a[40:41], v176 offset:28672\n"
"v_mfma_f32_16x16x32_bf16 v[232:235], a[204:207], v[210:213], 0\n"
"s_nop 0\n"
"ds_read_b64_tr_b16 a[42:43], v176 offset:30720\n"
"ds_read_b64_tr_b16 a[44:45], v176 offset:32768\n"
"v_mfma_f32_16x16x32_bf16 v[236:239], a[210:213], v[210:213], 0\n"
"s_nop 0\n"
"ds_read_b64_tr_b16 a[46:47], v176 offset:34816\n"
"ds_read_b64_tr_b16 a[48:49], v176 offset:36864\n"
"v_mfma_f32_16x16x32_bf16 v[240:243], a[216:219], v[210:213], 0\n"
"s_nop 0\n"
"ds_read_b64_tr_b16 a[50:51], v176 offset:38912\n"
"ds_read_b64_tr_b16 a[52:53], v176 offset:40960\n"
"v_mfma_f32_16x16x32_bf16 v[244:247], a[222:225], v[210:213], 0\n"
"ds_read_b64_tr_b16 a[54:55], v176 offset:43008\n"
"v_mfma_f32_16x16x32_bf16 v[248:251], a[228:231], v[210:213], 0\n"
"v_mfma_f32_16x16x16_bf16 v[232:235], a[208:209], v[214:215], v[232:235]\n"
"v_mfma_f32_16x16x32_bf16 v[136:139], a[12:15], v[0:3], 0\n"
"ds_read_b64_tr_b16 a[56:57], v176 offset:45056\n"
"v_mfma_f32_16x16x32_bf16 v[136:139], a[16:19], v[4:7], v[136:139]\n"
"ds_read_b64_tr_b16 a[58:59], v176 offset:47104\n"
"v_mfma_f32_16x16x32_bf16 v[136:139], a[20:23], v[8:11], v[136:139]\n"
"ds_read_b64_tr_b16 a[0:1], v177 offset:24576\n"
"v_mfma_f32_16x16x32_bf16 v[136:139], a[24:27], v[12:15], v[136:139]\n"
"ds_read_b64_tr_b16 a[2:3], v177 offset:26624\n"
"v_mfma_f32_16x16x32_bf16 v[136:139], a[28:31], v[16:19], v[136:139]\n"
"ds_read_b64_tr_b16 a[4:5], v177 offset:28672\n"
"v_mfma_f32_16x16x32_bf16 v[136:139], a[32:35], v[20:23], v[136:139]\n"
"ds_read_b64_tr_b16 a[6:7], v177 offset:30720\n"
"v_mfma_f32_16x16x16_bf16 v[236:239], a[214:215], v[214:215], v[236:239]\n"
"v_mfma_f32_16x16x16_bf16 v[240:243], a[220:221], v[214:215], v[240:243]\n"
"v_mfma_f32_16x16x16_bf16 v[244:247], a[226:227], v[214:215], v[244:247]\n"
"v_mfma_f32_16x16x16_bf16 v[248:251], a[232:233], v[214:215], v[248:251]\n"
"v_mul_f32 v254, 0x3fb8aa3b, v185\n"
"v_max3_f32 v252, v232, v233, v234\n"
"v_mfma_f32_16x16x32_bf16 v[140:143], a[12:15], v[56:59], 0\n"
"ds_read_b64_tr_b16 a[8:9], v177 offset:32768\n"
"v_mfma_f32_16x16x32_bf16 v[140:143], a[16:19], v[60:63], v[140:143]\n"
"v_max3_f32 v252, v252, v235, v236\n"
"v_max3_f32 v252, v252, v237, v238\n"
"v_mfma_f32_16x16x32_bf16 v[140:143], a[20:23], v[64:67], v[140:143]\n"
"v_max3_f32 v252, v252, v239, v240\n"
"v_max3_f32 v252, v252, v241, v242\n"
"v_mfma_f32_16x16x32_bf16 v[140:143], a[24:27], v[68:71], v[140:143]\n"
"v_max3_f32 v252, v252, v243, v244\n"
"v_max3_f32 v252, v252, v245, v246\n"
"v_mfma_f32_16x16x32_bf16 v[140:143], a[28:31], v[72:75], v[140:143]\n"
"v_max3_f32 v252, v252, v247, v248\n"
"v_max_f32 v252, v252, v249\n"
"v_mfma_f32_16x16x32_bf16 v[140:143], a[32:35], v[76:79], v[140:143]\n"
"s_waitcnt lgkmcnt(13)\n"
"v_mul_f32_e64 v250, v254, -v252\n"
"v_mfma_f32_16x16x32_bf16 v[112:115], a[36:39], v[24:27], v[112:115]\n"
"s_nop 0\n"
"v_pk_fma_f32 v[232:233], v[232:233], v[254:255], v[250:251] op_sel_hi:[1,0,0]\n"
"v_pk_fma_f32 v[234:235], v[234:235], v[254:255], v[250:251] op_sel_hi:[1,0,0]\n"
"v_pk_fma_f32 v[236:237], v[236:237], v[254:255], v[250:251] op_sel_hi:[1,0,0]\n"
"v_pk_fma_f32 v[238:239], v[238:239], v[254:255], v[250:251] op_sel_hi:[1,0,0]\n"
"v_pk_fma_f32 v[240:241], v[240:241], v[254:255], v[250:251] op_sel_hi:[1,0,0]\n"
"v_pk_fma_f32 v[242:243], v[242:243], v[254:255], v[250:251] op_sel_hi:[1,0,0]\n"
"v_pk_fma_f32 v[244:245], v[244:245], v[254:255], v[250:251] op_sel_hi:[1,0,0]\n"
"v_pk_fma_f32 v[246:247], v[246:247], v[254:255], v[250:251] op_sel_hi:[1,0,0]\n"
"v_pk_fma_f32 v[248:249], v[248:249], v[254:255], v[250:251] op_sel_hi:[1,0,0]\n"
"v_exp_f32 v232, v232\n"
"v_exp_f32 v233, v233\n"
"v_exp_f32 v234, v234\n"
"v_exp_f32 v235, v235\n"
"v_exp_f32 v236, v236\n"
"v_exp_f32 v237, v237\n"
"v_exp_f32 v238, v238\n"
"v_exp_f32 v239, v239\n"
"v_exp_f32 v240, v240\n"
"v_exp_f32 v241, v241\n"
"v_exp_f32 v242, v242\n"
"v_exp_f32 v243, v243\n"
"v_exp_f32 v244, v244\n"
"v_exp_f32 v245, v245\n"
"v_exp_f32 v246, v246\n"
"v_exp_f32 v247, v247\n"
"v_exp_f32 v248, v248\n"
"v_exp_f32 v249, v249\n"
"v_pk_add_f32 v[252:253], v[232:233], v[234:235]\n"
"v_pk_add_f32 v[250:251], v[236:237], v[238:239]\n"
"v_pk_add_f32 v[250:251], v[250:251], v[240:241]\n"
"v_pk_add_f32 v[252:253], v[252:253], v[242:243]\n"
"v_pk_add_f32 v[250:251], v[250:251], v[244:245]\n"
"v_pk_add_f32 v[252:253], v[252:253], v[246:247]\n"
"v_pk_add_f32 v[250:251], v[250:251], v[248:249]\n"
"v_pk_add_f32 v[252:253], v[252:253], v[250:251]\n"
"v_add_f32 v252, v252, v253\n"
"v_rcp_f32 v252, v252\n"
"s_nop 0\n"
"v_mul_f32 v252, v252, v185\n"
"v_pk_mul_f32 v[232:233], v[232:233], v[252:253] op_sel_hi:[1,0]\n"
"v_pk_mul_f32 v[234:235], v[234:235], v[252:253] op_sel_hi:[1,0]\n"
"v_pk_mul_f32 v[236:237], v[236:237], v[252:253] op_sel_hi:[1,0]\n"
"v_pk_mul_f32 v[238:239], v[238:239], v[252:253] op_sel_hi:[1,0]\n"
"v_pk_mul_f32 v[240:241], v[240:241], v[252:253] op_sel_hi:[1,0]\n"
"v_pk_mul_f32 v[242:243], v[242:243], v[252:253] op_sel_hi:[1,0]\n"
"v_pk_mul_f32 v[244:245], v[244:245], v[252:253] op_sel_hi:[1,0]\n"
"v_pk_mul_f32 v[246:247], v[246:247], v[252:253] op_sel_hi:[1,0]\n"
"v_pk_mul_f32 v[248:249], v[248:249], v[252:253] op_sel_hi:[1,0]\n"
"v_mfma_f32_16x16x32_bf16 v[112:115], a[40:43], v[28:31], v[112:115]\n"
"s_waitcnt lgkmcnt(9)\n"
"buffer_load_dwordx4 v180, s[28:31], s56 offen lds\n"
"v_mfma_f32_16x16x32_bf16 v[112:115], a[44:47], v[32:35], v[112:115]\n"
"s_addk_i32 m0, 4096\n"
"v_cvt_pk_bf16_f32 v232, v232, v233\n"
"v_cvt_pk_bf16_f32 v233, v234, v235\n"
"v_mfma_f32_16x16x32_bf16 v[112:115], a[48:51], v[36:39], v[112:115]\n"
"s_waitcnt lgkmcnt(5)\n"
"ds_read_b64_tr_b16 a[10:11], v177 offset:34816\n"
"v_cvt_pk_bf16_f32 v234, v236, v237\n"
"v_mfma_f32_16x16x32_bf16 v[112:115], a[52:55], v[40:43], v[112:115]\n"
"s_add_u32 s56, s56, s62\n"
"ds_read_b64_tr_b16 a[12:13], v177 offset:36864\n"
"ds_read_b64_tr_b16 a[14:15], v177 offset:38912\n"
"v_mfma_f32_16x16x32_bf16 v[112:115], a[56:59], v[44:47], v[112:115]\n"
"s_nop 0\n"
"buffer_load_dwordx4 v180, s[28:31], s56 offen lds\n"
"v_mfma_f32_16x16x32_bf16 v[116:119], a[36:39], v[80:83], v[116:119]\n"
"s_addk_i32 m0, 4096\n"
"ds_read_b64_tr_b16 a[16:17], v177 offset:40960\n"
"v_cvt_pk_bf16_f32 v235, v238, v239\n"
"v_mfma_f32_16x16x32_bf16 v[116:119], a[40:43], v[84:87], v[116:119]\n"
"s_add_u32 s56, s56, s62\n"
"ds_read_b64_tr_b16 a[18:19], v177 offset:43008\n"
"ds_read_b64_tr_b16 a[20:21], v177 offset:45056\n"
"v_mfma_f32_16x16x32_bf16 v[116:119], a[44:47], v[88:91], v[116:119]\n"
"s_nop 0\n"
"buffer_load_dwordx4 v180, s[28:31], s56 offen lds\n"
"v_mfma_f32_16x16x32_bf16 v[116:119], a[48:51], v[92:95], v[116:119]\n"
"s_addk_i32 m0, 4096\n"
"ds_read_b64_tr_b16 a[22:23], v177 offset:47104\n"
"v_cvt_pk_bf16_f32 v236, v240, v241\n"
"v_mfma_f32_16x16x32_bf16 v[116:119], a[52:55], v[96:99], v[116:119]\n"
"s_add_u32 s56, s56, s62\n"
"ds_read_b64_tr_b16 a[24:25], v178 offset:24576\n"
"v_cvt_pk_bf16_f32 v237, v242, v243\n"
"v_mfma_f32_16x16x32_bf16 v[116:119], a[56:59], v[100:103], v[116:119]\n"
"s_waitcnt lgkmcnt(9)\n"
"ds_read_b64_tr_b16 a[26:27], v178 offset:26624\n"
"ds_read_b64_tr_b16 a[28:29], v178 offset:28672\n"
"v_mfma_f32_16x16x32_bf16 v[120:123], a[0:3], v[24:27], v[120:123]\n"
"s_nop 0\n"
"buffer_load_dwordx4 v180, s[28:31], s56 offen lds\n"
"v_mfma_f32_16x16x32_bf16 v[120:123], a[4:7], v[28:31], v[120:123]\n"
"s_waitcnt lgkmcnt(7)\n"
"ds_read_b64_tr_b16 a[30:31], v178 offset:30720\n"
"v_cvt_pk_bf16_f32 v238, v244, v245\n"
"v_mfma_f32_16x16x32_bf16 v[120:123], a[8:11], v[32:35], v[120:123]\n"
"s_addk_i32 m0, 4096\n"
"ds_read_b64_tr_b16 a[32:33], v178 offset:32768\n"
"v_cvt_pk_bf16_f32 v239, v246, v247\n"
"v_mfma_f32_16x16x32_bf16 v[120:123], a[12:15], v[36:39], v[120:123]\n"
"s_waitcnt lgkmcnt(5)\n"
"ds_read_b64_tr_b16 a[34:35], v178 offset:34816\n"
"v_cvt_pk_bf16_f32 v240, v248, v249\n"
"v_mfma_f32_16x16x32_bf16 v[120:123], a[16:19], v[40:43], v[120:123]\n"
"s_add_u32 s56, s56, s62\n"
"ds_read_b64_tr_b16 a[36:37], v178 offset:36864\n"
"ds_read_b64_tr_b16 a[38:39], v178 offset:38912\n"
"v_mfma_f32_16x16x32_bf16 v[120:123], a[20:23], v[44:47], v[120:123]\n"
"s_nop 0\n"
"buffer_load_dwordx4 v180, s[28:31], s56 offen lds\n"
"v_mfma_f32_16x16x32_bf16 v[124:127], a[0:3], v[80:83], v[124:127]\n"
"s_addk_i32 m0, 4096\n"
"ds_read_b64_tr_b16 a[40:41], v178 offset:40960\n"
"v_mov_b32 v241, 0\n"
"v_mfma_f32_16x16x32_bf16 v[124:127], a[4:7], v[84:87], v[124:127]\n"
"s_add_u32 s56, s56, s62\n"
"ds_read_b64_tr_b16 a[42:43], v178 offset:43008\n"
"ds_read_b64_tr_b16 a[44:45], v178 offset:45056\n"
"v_mfma_f32_16x16x32_bf16 v[124:127], a[8:11], v[88:91], v[124:127]\n"
"s_nop 0\n"
"buffer_load_dwordx4 v180, s[28:31], s56 offen lds\n"
"v_mfma_f32_16x16x32_bf16 v[124:127], a[12:15], v[92:95], v[124:127]\n"
"s_addk_i32 m0, 4096\n"
"ds_read_b64_tr_b16 a[46:47], v178 offset:47104\n"
"v_add_u32 v176, 49152, v176\n"
"v_mfma_f32_16x16x32_bf16 v[124:127], a[16:19], v[96:99], v[124:127]\n"
"s_mov_b32 s56, 1769600\n"
"ds_read_b64_tr_b16 a[48:49], v179 offset:24576\n"
"v_accvgpr_read_b32 v252, a249\n"
"v_mfma_f32_16x16x32_bf16 v[124:127], a[20:23], v[100:103], v[124:127]\n"
"s_waitcnt lgkmcnt(9)\n"
"ds_read_b64_tr_b16 a[50:51], v179 offset:26624\n"
"ds_read_b64_tr_b16 a[52:53], v179 offset:28672\n"
"v_mfma_f32_16x16x32_bf16 v[128:131], a[24:27], v[24:27], v[128:131]\n"
"s_nop 0\n"
"ds_read_b64_tr_b16 a[54:55], v179 offset:30720\n"
"ds_read_b64_tr_b16 a[56:57], v179 offset:32768\n"
"v_mfma_f32_16x16x32_bf16 v[128:131], a[28:31], v[28:31], v[128:131]\n"
"s_waitcnt lgkmcnt(9)\n"
"ds_read_b64_tr_b16 a[58:59], v179 offset:34816\n"
"ds_read_b64_tr_b16 a[0:1], v179 offset:36864\n"
"v_mfma_f32_16x16x32_bf16 v[128:131], a[32:35], v[32:35], v[128:131]\n"
"s_nop 0\n"
"ds_read_b64_tr_b16 a[2:3], v179 offset:38912\n"
"ds_read_b64_tr_b16 a[4:5], v179 offset:40960\n"
"v_mfma_f32_16x16x32_bf16 v[128:131], a[36:39], v[36:39], v[128:131]\n"
"s_waitcnt lgkmcnt(9)\n"
"ds_read_b64_tr_b16 a[6:7], v179 offset:43008\n"
"ds_read_b64_tr_b16 a[8:9], v179 offset:45056\n"
"v_mfma_f32_16x16x32_bf16 v[128:131], a[40:43], v[40:43], v[128:131]\n"
"ds_read_b64_tr_b16 a[10:11], v179 offset:47104\n"
"v_add_u32 v177, 49152, v177\n"
"v_mfma_f32_16x16x32_bf16 v[128:131], a[44:47], v[44:47], v[128:131]\n"
"s_nop 0\n"
"v_cmp_gt_u32 vcc, 16, v187\n"
"s_nop 1\n"
"v_cndmask_b32 v214, v214, v252, vcc\n"
"v_cndmask_b32 v215, v215, v252, vcc\n"
"ds_write_b64 v189, v[232:233] offset:0\n"
"ds_write_b64 v189, v[234:235] offset:8\n"
"ds_write_b64 v189, v[236:237] offset:16\n"
"ds_write_b64 v189, v[238:239] offset:24\n"
"ds_write_b64 v189, v[240:241] offset:32\n"
"ds_write_b32 v191, v214\n"
"ds_write_b32 v192, v215 offset:8\n"
"ds_write_b64 v190, v[208:209] offset:152\n"
"ds_write_b64 v190, v[216:217] offset:192\n"
"v_mfma_f32_16x16x32_bf16 v[232:235], a[204:207], v[224:227], 0\n"
"v_mfma_f32_16x16x32_bf16 v[236:239], a[210:213], v[224:227], 0\n"
"v_mfma_f32_16x16x32_bf16 v[240:243], a[216:219], v[224:227], 0\n"
"v_mfma_f32_16x16x32_bf16 v[244:247], a[222:225], v[224:227], 0\n"
"v_mfma_f32_16x16x32_bf16 v[248:251], a[228:231], v[224:227], 0\n"
"v_mfma_f32_16x16x32_bf16 v[132:135], a[24:27], v[80:83], v[132:135]\n"
"v_mfma_f32_16x16x32_bf16 v[132:135], a[28:31], v[84:87], v[132:135]\n"
"v_mfma_f32_16x16x32_bf16 v[132:135], a[32:35], v[88:91], v[132:135]\n"
"v_mfma_f32_16x16x32_bf16 v[132:135], a[36:39], v[92:95], v[132:135]\n"
"s_waitcnt vmcnt(6) lgkmcnt(9)\n"
"buffer_load_dwordx4 v180, s[28:31], s56 offen lds\n"
"v_mfma_f32_16x16x32_bf16 v[132:135], a[40:43], v[96:99], v[132:135]\n"
"s_barrier\n"
"ds_read_b64_tr_b16 a[12:13], v176 offset:0\n"
"s_addk_i32 m0, 4096\n"
"v_mfma_f32_16x16x32_bf16 v[132:135], a[44:47], v[100:103], v[132:135]\n"
"s_add_u32 s56, s56, s62\n"
"ds_read_b64_tr_b16 a[14:15], v176 offset:2048\n"
"ds_read_b64_tr_b16 a[16:17], v176 offset:4096\n"
"v_mfma_f32_16x16x16_bf16 v[232:235], a[208:209], v[228:229], v[232:235]\n"
"s_nop 0\n"
"buffer_load_dwordx4 v180, s[28:31], s56 offen lds\n"
"v_mfma_f32_16x16x16_bf16 v[236:239], a[214:215], v[228:229], v[236:239]\n"
"s_addk_i32 m0, 4096\n"
"s_add_u32 s56, s56, s62\n"
"ds_read_b64_tr_b16 a[18:19], v176 offset:6144\n"
"v_mfma_f32_16x16x16_bf16 v[240:243], a[220:221], v[228:229], v[240:243]\n"
"s_nop 0\n"
"buffer_load_dwordx4 v180, s[28:31], s56 offen lds\n"
"v_mfma_f32_16x16x16_bf16 v[244:247], a[226:227], v[228:229], v[244:247]\n"
"s_addk_i32 m0, 4096\n"
"s_add_u32 s56, s56, s62\n"
"ds_read_b64_tr_b16 a[20:21], v176 offset:8192\n"
"v_mfma_f32_16x16x16_bf16 v[248:251], a[232:233], v[228:229], v[248:251]\n"
"s_nop 0\n"
"buffer_load_dwordx4 v180, s[28:31], s56 offen lds\n"
"v_mfma_f32_16x16x32_bf16 v[136:139], a[48:51], v[24:27], v[136:139]\n"
"s_addk_i32 m0, 4096\n"
"v_mul_f32 v254, 0x3fb8aa3b, v186\n"
"v_max3_f32 v252, v232, v233, v234\n"
"v_mfma_f32_16x16x32_bf16 v[136:139], a[52:55], v[28:31], v[136:139]\n"
"s_add_u32 s56, s56, s62\n"
"buffer_load_dwordx4 v180, s[28:31], s56 offen lds\n"
"v_mfma_f32_16x16x32_bf16 v[136:139], a[56:59], v[32:35], v[136:139]\n"
"s_addk_i32 m0, 4096\n"
"v_max3_f32 v252, v252, v235, v236\n"
"v_max3_f32 v252, v252, v237, v238\n"
"v_mfma_f32_16x16x32_bf16 v[136:139], a[0:3], v[36:39], v[136:139]\n"
"s_add_u32 s56, s56, s62\n"
"ds_read_b64_tr_b16 a[22:23], v176 offset:10240\n"
"v_max3_f32 v252, v252, v239, v240\n"
"v_mfma_f32_16x16x32_bf16 v[136:139], a[4:7], v[40:43], v[136:139]\n"
"s_nop 0\n"
"ds_read_b64_tr_b16 a[24:25], v176 offset:12288\n"
"ds_read_b64_tr_b16 a[26:27], v176 offset:14336\n"
"v_mfma_f32_16x16x32_bf16 v[136:139], a[8:11], v[44:47], v[136:139]\n"
"s_nop 0\n"
"buffer_load_dwordx4 v180, s[28:31], s56 offen lds\n"
"v_mfma_f32_16x16x32_bf16 v[140:143], a[48:51], v[80:83], v[140:143]\n"
"s_addk_i32 m0, 4096\n"
"ds_read_b64_tr_b16 a[28:29], v176 offset:16384\n"
"v_max3_f32 v252, v252, v241, v242\n"
"v_mfma_f32_16x16x32_bf16 v[140:143], a[52:55], v[84:87], v[140:143]\n"
"s_mov_b32 s56, 3539072\n"
"ds_read_b64_tr_b16 a[30:31], v176 offset:18432\n"
"v_max3_f32 v252, v252, v243, v244\n"
"v_mfma_f32_16x16x32_bf16 v[140:143], a[56:59], v[88:91], v[140:143]\n"
"ds_read_b64_tr_b16 a[32:33], v176 offset:20480\n"
"v_add_u32 v178, 49152, v178\n"
"v_mfma_f32_16x16x32_bf16 v[140:143], a[0:3], v[92:95], v[140:143]\n"
"s_nop 0\n"
"ds_read_b64_tr_b16 a[34:35], v176 offset:22528\n"
"ds_read_b64_tr_b16 a[36:37], v177 offset:0\n"
"v_mfma_f32_16x16x32_bf16 v[140:143], a[4:7], v[96:99], v[140:143]\n"
"s_nop 0\n"
"buffer_load_dwordx4 v180, s[28:31], s56 offen lds\n"
"v_mfma_f32_16x16x32_bf16 v[140:143], a[8:11], v[100:103], v[140:143]\n"
"s_waitcnt lgkmcnt(9)\n"
"ds_read_b64_tr_b16 a[38:39], v177 offset:2048\n"
"v_max3_f32 v252, v252, v245, v246\n"
"v_mfma_f32_16x16x32_bf16 v[112:115], a[12:15], v[48:51], v[112:115]\n"
"s_addk_i32 m0, 4096\n"
"ds_read_b64_tr_b16 a[40:41], v177 offset:4096\n"
"v_max3_f32 v252, v252, v247, v248\n"
"v_mfma_f32_16x16x32_bf16 v[112:115], a[16:19], v[52:55], v[112:115]\n"
"s_waitcnt lgkmcnt(7)\n"
"ds_read_b64_tr_b16 a[42:43], v177 offset:6144\n"
"v_max_f32 v252, v252, v249\n"
"v_mfma_f32_16x16x32_bf16 v[112:115], a[20:23], a[172:175], v[112:115]\n"
"s_add_u32 s56, s56, s62\n"
"ds_read_b64_tr_b16 a[44:45], v177 offset:8192\n"
"ds_read_b64_tr_b16 a[46:47], v177 offset:10240\n"
"v_mfma_f32_16x16x32_bf16 v[112:115], a[24:27], a[176:179], v[112:115]\n"
"s_waitcnt lgkmcnt(6)\n"
"buffer_load_dwordx4 v180, s[28:31], s56 offen lds\n"
"v_mfma_f32_16x16x32_bf16 v[112:115], a[28:31], a[180:183], v[112:115]\n"
"s_addk_i32 m0, 4096\n"
"ds_read_b64_tr_b16 a[48:49], v177 offset:12288\n"
"v_mul_f32_e64 v250, v254, -v252\n"
"v_mfma_f32_16x16x32_bf16 v[112:115], a[32:35], a[184:187], v[112:115]\n"
"s_add_u32 s56, s56, s62\n"
"ds_read_b64_tr_b16 a[50:51], v177 offset:14336\n"
"ds_read_b64_tr_b16 a[52:53], v177 offset:16384\n"
"v_mfma_f32_16x16x32_bf16 v[116:119], a[12:15], v[104:107], v[116:119]\n"
"s_nop 0\n"
"buffer_load_dwordx4 v180, s[28:31], s56 offen lds\n"
"v_mfma_f32_16x16x32_bf16 v[116:119], a[16:19], v[108:111], v[116:119]\n"
"s_addk_i32 m0, 4096\n"
"ds_read_b64_tr_b16 a[54:55], v177 offset:18432\n"
"ds_read_b64_tr_b16 a[56:57], v177 offset:20480\n"
"v_mfma_f32_16x16x32_bf16 v[116:119], a[20:23], a[188:191], v[116:119]\n"
"s_add_u32 s56, s56, s62\n"
"v_pk_fma_f32 v[232:233], v[232:233], v[254:255], v[250:251] op_sel_hi:[1,0,0]\n"
"v_pk_fma_f32 v[234:235], v[234:235], v[254:255], v[250:251] op_sel_hi:[1,0,0]\n"
"v_pk_fma_f32 v[236:237], v[236:237], v[254:255], v[250:251] op_sel_hi:[1,0,0]\n"
"v_pk_fma_f32 v[238:239], v[238:239], v[254:255], v[250:251] op_sel_hi:[1,0,0]\n"
"v_pk_fma_f32 v[240:241], v[240:241], v[254:255], v[250:251] op_sel_hi:[1,0,0]\n"
"v_pk_fma_f32 v[242:243], v[242:243], v[254:255], v[250:251] op_sel_hi:[1,0,0]\n"
"v_pk_fma_f32 v[244:245], v[244:245], v[254:255], v[250:251] op_sel_hi:[1,0,0]\n"
"v_pk_fma_f32 v[246:247], v[246:247], v[254:255], v[250:251] op_sel_hi:[1,0,0]\n"
"v_pk_fma_f32 v[248:249], v[248:249], v[254:255], v[250:251] op_sel_hi:[1,0,0]\n"
"v_exp_f32 v232, v232\n"
"v_exp_f32 v233, v233\n"
"v_exp_f32 v234, v234\n"
"v_exp_f32 v235, v235\n"
"v_exp_f32 v236, v236\n"
"v_exp_f32 v237, v237\n"
"v_exp_f32 v238, v238\n"
"v_exp_f32 v239, v239\n"
"v_exp_f32 v240, v240\n"
"v_exp_f32 v241, v241\n"
"v_exp_f32 v242, v242\n"
"v_exp_f32 v243, v243\n"
"v_exp_f32 v244, v244\n"
"v_exp_f32 v245, v245\n"
"v_exp_f32 v246, v246\n"
"v_exp_f32 v247, v247\n"
"v_exp_f32 v248, v248\n"
"v_exp_f32 v249, v249\n"
"v_pk_add_f32 v[252:253], v[232:233], v[234:235]\n"
"v_pk_add_f32 v[250:251], v[236:237], v[238:239]\n"
"v_pk_add_f32 v[250:251], v[250:251], v[240:241]\n"
"v_pk_add_f32 v[252:253], v[252:253], v[242:243]\n"
"v_pk_add_f32 v[250:251], v[250:251], v[244:245]\n"
"v_pk_add_f32 v[252:253], v[252:253], v[246:247]\n"
"v_pk_add_f32 v[250:251], v[250:251], v[248:249]\n"
"v_pk_add_f32 v[252:253], v[252:253], v[250:251]\n"
"v_add_f32 v252, v252, v253\n"
"v_rcp_f32 v252, v252\n"
"s_nop 0\n"
"v_mul_f32 v252, v252, v186\n"
"v_pk_mul_f32 v[232:233], v[232:233], v[252:253] op_sel_hi:[1,0]\n"
"v_pk_mul_f32 v[234:235], v[234:235], v[252:253] op_sel_hi:[1,0]\n"
"v_pk_mul_f32 v[236:237], v[236:237], v[252:253] op_sel_hi:[1,0]\n"
"v_pk_mul_f32 v[238:239], v[238:239], v[252:253] op_sel_hi:[1,0]\n"
"v_pk_mul_f32 v[240:241], v[240:241], v[252:253] op_sel_hi:[1,0]\n"
"v_pk_mul_f32 v[242:243], v[242:243], v[252:253] op_sel_hi:[1,0]\n"
"v_pk_mul_f32 v[244:245], v[244:245], v[252:253] op_sel_hi:[1,0]\n"
"v_pk_mul_f32 v[246:247], v[246:247], v[252:253] op_sel_hi:[1,0]\n"
"v_pk_mul_f32 v[248:249], v[248:249], v[252:253] op_sel_hi:[1,0]\n"
"v_mfma_f32_16x16x32_bf16 v[116:119], a[24:27], a[192:195], v[116:119]\n"
"s_nop 0\n"
"ds_read_b64_tr_b16 a[58:59], v177 offset:22528\n"
"ds_read_b64_tr_b16 a[0:1], v178 offset:0\n"
"v_mfma_f32_16x16x32_bf16 v[116:119], a[28:31], a[196:199], v[116:119]\n"
"s_nop 0\n"
"buffer_load_dwordx4 v180, s[28:31], s56 offen lds\n"
"v_mfma_f32_16x16x32_bf16 v[116:119], a[32:35], a[200:203], v[116:119]\n"
"s_waitcnt lgkmcnt(9)\n"
"ds_read_b64_tr_b16 a[2:3], v178 offset:2048\n"
"v_add_u32 v179, 49152, v179\n"
"v_mfma_f32_16x16x32_bf16 v[120:123], a[36:39], v[48:51], v[120:123]\n"
"s_addk_i32 m0, 4096\n"
"ds_read_b64_tr_b16 a[4:5], v178 offset:4096\n"
"v_cvt_pk_bf16_f32 v232, v232, v233\n"
"v_mfma_f32_16x16x32_bf16 v[120:123], a[40:43], v[52:55], v[120:123]\n"
"s_waitcnt lgkmcnt(7)\n"
"ds_read_b64_tr_b16 a[6:7], v178 offset:6144\n"
"v_cvt_pk_bf16_f32 v233, v234, v235\n"
"v_mfma_f32_16x16x32_bf16 v[120:123], a[44:47], a[172:175], v[120:123]\n"
"s_add_u32 s56, s56, s62\n"
"ds_read_b64_tr_b16 a[8:9], v178 offset:8192\n"
"ds_read_b64_tr_b16 a[10:11], v178 offset:10240\n"
"v_mfma_f32_16x16x32_bf16 v[120:123], a[48:51], a[176:179], v[120:123]\n"
"s_waitcnt lgkmcnt(6)\n"
"buffer_load_dwordx4 v180, s[28:31], s56 offen lds\n"
"v_mfma_f32_16x16x32_bf16 v[120:123], a[52:55], a[180:183], v[120:123]\n"
"s_addk_i32 m0, 4096\n"
"ds_read_b64_tr_b16 a[12:13], v178 offset:12288\n"
"v_cvt_pk_bf16_f32 v234, v236, v237\n"
"v_mfma_f32_16x16x32_bf16 v[120:123], a[56:59], a[184:187], v[120:123]\n"
"s_add_u32 s56, s56, s62\n"
"ds_read_b64_tr_b16 a[14:15], v178 offset:14336\n"
"ds_read_b64_tr_b16 a[16:17], v178 offset:16384\n"
"v_mfma_f32_16x16x32_bf16 v[124:127], a[36:39], v[104:107], v[124:127]\n"
"s_nop 0\n"
"buffer_load_dwordx4 v180, s[28:31], s56 offen lds\n"
"v_mfma_f32_16x16x32_bf16 v[124:127], a[40:43], v[108:111], v[124:127]\n"
"s_add_u32 m0, s55, 0\n"
"ds_read_b64_tr_b16 a[18:19], v178 offset:18432\n"
"v_cvt_pk_bf16_f32 v235, v238, v239\n"
"v_mfma_f32_16x16x32_bf16 v[124:127], a[44:47], a[188:191], v[124:127]\n"
"s_mov_b32 s56, 0\n"
"ds_read_b64_tr_b16 a[20:21], v178 offset:20480\n"
"ds_read_b64_tr_b16 a[22:23], v178 offset:22528\n"
"v_mfma_f32_16x16x32_bf16 v[124:127], a[48:51], a[192:195], v[124:127]\n"
"s_nop 0\n"
"buffer_load_dwordx4 v182, s[48:51], s56 offen lds\n"
"v_mfma_f32_16x16x32_bf16 v[124:127], a[52:55], a[196:199], v[124:127]\n"
"s_addk_i32 m0, 4096\n"
"ds_read_b64_tr_b16 a[24:25], v179 offset:0\n"
"v_cvt_pk_bf16_f32 v236, v240, v241\n"
"v_mfma_f32_16x16x32_bf16 v[124:127], a[56:59], a[200:203], v[124:127]\n"
"s_waitcnt lgkmcnt(9)\n"
"ds_read_b64_tr_b16 a[26:27], v179 offset:2048\n"
"v_cvt_pk_bf16_f32 v237, v242, v243\n"
"v_mfma_f32_16x16x32_bf16 v[128:131], a[0:3], v[48:51], v[128:131]\n"
"s_add_u32 s56, s56, s62\n"
"ds_read_b64_tr_b16 a[28:29], v179 offset:4096\n"
"ds_read_b64_tr_b16 a[30:31], v179 offset:6144\n"
"v_mfma_f32_16x16x32_bf16 v[128:131], a[4:7], v[52:55], v[128:131]\n"
"s_waitcnt lgkmcnt(8)\n"
"ds_read_b64_tr_b16 a[32:33], v179 offset:8192\n"
"ds_read_b64_tr_b16 a[34:35], v179 offset:10240\n"
"v_mfma_f32_16x16x32_bf16 v[128:131], a[8:11], a[172:175], v[128:131]\n"
"s_nop 0\n"
"ds_read_b64_tr_b16 a[36:37], v179 offset:12288\n"
"ds_read_b64_tr_b16 a[38:39], v179 offset:14336\n"
"v_mfma_f32_16x16x32_bf16 v[128:131], a[12:15], a[176:179], v[128:131]\n"
"s_waitcnt lgkmcnt(8)\n"
"ds_read_b64_tr_b16 a[40:41], v179 offset:16384\n"
"ds_read_b64_tr_b16 a[42:43], v179 offset:18432\n"
"v_mfma_f32_16x16x32_bf16 v[128:131], a[16:19], a[180:183], v[128:131]\n"
"s_nop 0\n"
"ds_read_b64_tr_b16 a[44:45], v179 offset:20480\n"
"ds_read_b64_tr_b16 a[46:47], v179 offset:22528\n"
"v_mfma_f32_16x16x32_bf16 v[128:131], a[20:23], a[184:187], v[128:131]\n"
"s_nop 0\n"
"buffer_load_dwordx4 v182, s[48:51], s56 offen lds\n"
"v_mfma_f32_16x16x32_bf16 v[132:135], a[0:3], v[104:107], v[132:135]\n"
"s_addk_i32 m0, 4096\n"
"v_cvt_pk_bf16_f32 v238, v244, v245\n"
"v_cvt_pk_bf16_f32 v239, v246, v247\n"
"v_mfma_f32_16x16x32_bf16 v[132:135], a[4:7], v[108:111], v[132:135]\n"
"s_add_u32 s56, s56, s62\n"
"buffer_load_dwordx4 v182, s[48:51], s56 offen lds\n"
"v_mfma_f32_16x16x32_bf16 v[132:135], a[8:11], a[188:191], v[132:135]\n"
"s_addk_i32 m0, 4096\n"
"v_cvt_pk_bf16_f32 v240, v248, v249\n"
"v_mov_b32 v241, 0\n"
"v_mfma_f32_16x16x32_bf16 v[132:135], a[12:15], a[192:195], v[132:135]\n"
"s_waitcnt vmcnt(15) lgkmcnt(0)\n"
"v_max_f32 v112, 0, v112\n"
"v_max_f32 v113, 0, v113\n"
"v_mfma_f32_16x16x32_bf16 v[132:135], a[16:19], a[196:199], v[132:135]\n"
"s_barrier\n"
"ds_read_b64_tr_b16 a[48:49], v176 offset:24576\n"
"v_lshlrev_b32 v181, 7, v187\n"
"v_mfma_f32_16x16x32_bf16 v[132:135], a[20:23], a[200:203], v[132:135]\n"
"s_add_u32 s56, s56, s62\n"
"ds_read_b64_tr_b16 a[50:51], v176 offset:26624\n"
"ds_read_b64_tr_b16 a[52:53], v176 offset:28672\n"
"v_mfma_f32_16x16x32_bf16 v[136:139], a[24:27], v[48:51], v[136:139]\n"
"s_nop 0\n"
"buffer_load_dwordx4 v182, s[48:51], s56 offen lds\n"
"v_mfma_f32_16x16x32_bf16 v[136:139], a[28:31], v[52:55], v[136:139]\n"
"s_addk_i32 m0, 4096\n"
"ds_read_b64_tr_b16 a[54:55], v176 offset:30720\n"
"v_min_u32 v181, 2048, v181\n"
"v_mfma_f32_16x16x32_bf16 v[136:139], a[32:35], a[172:175], v[136:139]\n"
"s_add_u32 s56, s56, s62\n"
"ds_read_b64_tr_b16 a[56:57], v176 offset:32768\n"
"s_add_u32 s79, s63, 64\n"
"v_mfma_f32_16x16x32_bf16 v[136:139], a[36:39], a[176:179], v[136:139]\n"
"s_mul_i32 s79, s79, 2176\n"
"ds_read_b64_tr_b16 a[58:59], v176 offset:34816\n"
"s_min_u32 s79, s79, 329600\n"
"v_mfma_f32_16x16x32_bf16 v[136:139], a[40:43], a[180:183], v[136:139]\n"
"s_lshr_b32 s80, s53, 0\n"
"ds_read_b64_tr_b16 a[0:1], v176 offset:36864\n"
"ds_read_b64_tr_b16 a[2:3], v176 offset:38912\n"
"v_mfma_f32_16x16x32_bf16 v[136:139], a[44:47], a[184:187], v[136:139]\n"
"s_mul_i32 s80, s80, 331776\n"
"s_add_u32 s79, s79, s80\n"
"ds_read_b64_tr_b16 a[4:5], v176 offset:40960\n"
"v_mfma_f32_16x16x32_bf16 v[140:143], a[24:27], v[104:107], v[140:143]\n"
"s_nop 0\n"
"buffer_load_dword v203, v181, s[36:39], s79 offen\n"
"v_mfma_f32_16x16x32_bf16 v[140:143], a[28:31], v[108:111], v[140:143]\n"
"s_nop 0\n"
"ds_read_b64_tr_b16 a[6:7], v176 offset:43008\n"
"ds_read_b64_tr_b16 a[96:97], v194 offset:0\n"
"v_mfma_f32_16x16x32_bf16 v[140:143], a[32:35], a[188:191], v[140:143]\n"
"s_nop 0\n"
"ds_read_b64_tr_b16 a[8:9], v176 offset:45056\n"
"ds_read_b64_tr_b16 a[98:99], v194 offset:3968\n"
"v_mfma_f32_16x16x32_bf16 v[140:143], a[36:39], a[192:195], v[140:143]\n"
"s_nop 0\n"
"ds_read_b64_tr_b16 a[10:11], v176 offset:47104\n"
"ds_read_b64_tr_b16 a[144:145], v193 offset:0\n"
"v_mfma_f32_16x16x32_bf16 v[140:143], a[40:43], a[196:199], v[140:143]\n"
"s_nop 0\n"
"ds_read_b64_tr_b16 a[12:13], v177 offset:24576\n"
"ds_read_b64_tr_b16 a[146:147], v193 offset:3968\n"
"v_mfma_f32_16x16x32_bf16 v[140:143], a[44:47], a[200:203], v[140:143]\n"
"s_waitcnt lgkmcnt(13)\n"
"ds_read_b64_tr_b16 a[14:15], v177 offset:26624\n"
"ds_read_b64_tr_b16 a[148:149], v193 offset:8\n"
"v_mfma_f32_16x16x32_bf16 v[144:147], a[48:51], v[0:3], 0\n"
"s_nop 0\n"
"ds_read_b64_tr_b16 a[16:17], v177 offset:28672\n"
"ds_read_b64_tr_b16 a[18:19], v177 offset:30720\n"
"v_mfma_f32_16x16x32_bf16 v[144:147], a[52:55], v[4:7], v[144:147]\n"
"105:\n"
"s_cbranch_execz 109f\n"
"s_waitcnt lgkmcnt(13)\n"
"buffer_load_dwordx4 v182, s[48:51], s56 offen lds\n"
"v_mfma_f32_16x16x32_bf16 v[144:147], a[56:59], v[8:11], v[144:147]\n"
"s_addk_i32 m0, 4096\n"
"ds_read_b64_tr_b16 a[20:21], v177 offset:32768\n"
"ds_read_b64_tr_b16 a[150:151], v193 offset:3976\n"
"v_mfma_f32_16x16x32_bf16 v[144:147], a[0:3], v[12:15], v[144:147]\n"
"s_waitcnt lgkmcnt(9)\n"
"ds_read_b64_tr_b16 a[22:23], v177 offset:34816\n"
"ds_read_b64_tr_b16 a[152:153], v193 offset:16\n"
"v_mfma_f32_16x16x32_bf16 v[144:147], a[4:7], v[16:19], v[144:147]\n"
"s_add_u32 s56, s56, s62\n"
"ds_read_b64_tr_b16 a[24:25], v177 offset:36864\n"
"ds_read_b64_tr_b16 a[154:155], v193 offset:3984\n"
"v_mfma_f32_16x16x32_bf16 v[144:147], a[8:11], v[20:23], v[144:147]\n"
"s_nop 0\n"
"ds_read_b64_tr_b16 a[26:27], v177 offset:38912\n"
"ds_read_b64_tr_b16 a[156:157], v193 offset:24\n"
"v_mfma_f32_16x16x32_bf16 v[148:151], a[48:51], v[56:59], 0\n"
"s_nop 0\n"
"ds_read_b64_tr_b16 a[28:29], v177 offset:40960\n"
"ds_read_b64_tr_b16 a[158:159], v193 offset:3992\n"
"v_mfma_f32_16x16x32_bf16 v[148:151], a[52:55], v[60:63], v[148:151]\n"
"s_nop 0\n"
"ds_read_b64_tr_b16 a[30:31], v177 offset:43008\n"
"ds_read_b64_tr_b16 a[160:161], v193 offset:32\n"
"v_mfma_f32_16x16x32_bf16 v[148:151], a[56:59], v[64:67], v[148:151]\n"
"s_nop 0\n"
"ds_read_b64_tr_b16 a[32:33], v177 offset:45056\n"
"ds_read_b64_tr_b16 a[34:35], v177 offset:47104\n"
"v_mfma_f32_16x16x32_bf16 v[148:151], a[0:3], v[68:71], v[148:151]\n"
"s_nop 0\n"
"buffer_load_dwordx4 v182, s[48:51], s56 offen lds\n"
"v_mfma_f32_16x16x32_bf16 v[148:151], a[4:7], v[72:75], v[148:151]\n"
"s_addk_i32 m0, 4096\n"
"ds_read_b64_tr_b16 a[36:37], v178 offset:24576\n"
"ds_read_b64_tr_b16 a[162:163], v193 offset:4000\n"
"v_mfma_f32_16x16x32_bf16 v[148:151], a[8:11], v[76:79], v[148:151]\n"
"s_waitcnt lgkmcnt(15)\n"
"ds_read_b64_tr_b16 a[38:39], v178 offset:26624\n"
"v_max_f32 v114, 0, v114\n"
"v_mfma_f32_16x16x32_bf16 v[244:247], a[144:147], a[96:99], 0\n"
"s_add_u32 s56, s56, s62\n"
"v_max_f32 v115, 0, v115\n"
"v_max_f32 v116, 0, v116\n"
"v_mfma_f32_16x16x32_bf16 v[152:155], a[12:15], v[0:3], 0\n"
"ds_read_b64_tr_b16 a[40:41], v178 offset:28672\n"
"v_max_f32 v117, 0, v117\n"
"v_mfma_f32_16x16x32_bf16 v[152:155], a[16:19], v[4:7], v[152:155]\n"
"s_waitcnt lgkmcnt(11)\n"
"ds_read_b64_tr_b16 a[42:43], v178 offset:30720\n"
"v_max_f32 v118, 0, v118\n"
"v_mfma_f32_16x16x32_bf16 v[152:155], a[20:23], v[8:11], v[152:155]\n"
"ds_read_b64_tr_b16 a[44:45], v178 offset:32768\n"
"v_max_f32 v119, 0, v119\n"
"v_mfma_f32_16x16x32_bf16 v[152:155], a[24:27], v[12:15], v[152:155]\n"
"s_waitcnt lgkmcnt(6)\n"
"ds_read_b64_tr_b16 a[46:47], v178 offset:34816\n"
"v_max_f32 v120, 0, v120\n"
"v_mfma_f32_16x16x32_bf16 v[152:155], a[28:31], v[16:19], v[152:155]\n"
"s_nop 0\n"
"ds_read_b64_tr_b16 a[48:49], v178 offset:36864\n"
"ds_read_b64_tr_b16 a[50:51], v178 offset:38912\n"
"v_mfma_f32_16x16x32_bf16 v[152:155], a[32:35], v[20:23], v[152:155]\n"
"s_nop 0\n"
"buffer_load_dwordx4 v182, s[48:51], s56 offen lds\n"
"v_mfma_f32_16x16x32_bf16 v[248:251], a[148:151], a[96:99], 0\n"
"s_addk_i32 m0, 4096\n"
"v_max_f32 v121, 0, v121\n"
"v_max_f32 v122, 0, v122\n"
"v_mfma_f32_16x16x32_bf16 v[210:213], a[152:155], a[96:99], 0\n"
"s_add_u32 s56, s56, s62\n"
"v_max_f32 v123, 0, v123\n"
"v_max_f32 v124, 0, v124\n"
"v_mfma_f32_16x16x32_bf16 v[214:217], a[156:159], a[96:99], 0\n"
"v_max_f32 v125, 0, v125\n"
"v_max_f32 v126, 0, v126\n"
"v_mfma_f32_16x16x32_bf16 v[156:159], a[12:15], v[56:59], 0\n"
"ds_read_b64_tr_b16 a[52:53], v178 offset:40960\n"
"v_max_f32 v127, 0, v127\n"
"v_mfma_f32_16x16x32_bf16 v[156:159], a[16:19], v[60:63], v[156:159]\n"
"ds_read_b64_tr_b16 a[54:55], v178 offset:43008\n"
"v_max_f32 v128, 0, v128\n"
"v_mfma_f32_16x16x32_bf16 v[156:159], a[20:23], v[64:67], v[156:159]\n"
"ds_read_b64_tr_b16 a[56:57], v178 offset:45056\n"
"v_add_u32 v176, 49152, v176\n"
"v_mfma_f32_16x16x32_bf16 v[156:159], a[24:27], v[68:71], v[156:159]\n"
"ds_read_b64_tr_b16 a[58:59], v178 offset:47104\n"
"v_max_f32 v129, 0, v129\n"
"v_mfma_f32_16x16x32_bf16 v[156:159], a[28:31], v[72:75], v[156:159]\n"
"s_nop 0\n"
"ds_read_b64_tr_b16 a[0:1], v179 offset:24576\n"
"ds_read_b64_tr_b16 a[2:3], v179 offset:26624\n"
"v_mfma_f32_16x16x32_bf16 v[156:159], a[32:35], v[76:79], v[156:159]\n"
"s_waitcnt lgkmcnt(10)\n"
"buffer_load_dwordx4 v182, s[48:51], s56 offen lds\n"
"v_mfma_f32_16x16x32_bf16 v[224:227], a[160:163], a[96:99], 0\n"
"s_addk_i32 m0, 4096\n"
"ds_read_b64_tr_b16 a[96:97], v194 offset:7936\n"
"ds_read_b64_tr_b16 a[98:99], v194 offset:11904\n"
"v_mfma_f32_16x16x32_bf16 v[160:163], a[36:39], v[0:3], 0\n"
"s_add_u32 s56, s56, s62\n"
"ds_read_b64_tr_b16 a[4:5], v179 offset:28672\n"
"ds_read_b64_tr_b16 a[6:7], v179 offset:30720\n"
"v_mfma_f32_16x16x32_bf16 v[160:163], a[40:43], v[4:7], v[160:163]\n"
"s_waitcnt lgkmcnt(10)\n"
"ds_read_b64_tr_b16 a[8:9], v179 offset:32768\n"
"ds_read_b64_tr_b16 a[10:11], v179 offset:34816\n"
"v_mfma_f32_16x16x32_bf16 v[160:163], a[44:47], v[8:11], v[160:163]\n"
"s_nop 0\n"
"ds_read_b64_tr_b16 a[12:13], v179 offset:36864\n"
"ds_read_b64_tr_b16 a[14:15], v179 offset:38912\n"
"v_mfma_f32_16x16x32_bf16 v[160:163], a[48:51], v[12:15], v[160:163]\n"
"s_waitcnt lgkmcnt(10)\n"
"ds_read_b64_tr_b16 a[16:17], v179 offset:40960\n"
"ds_read_b64_tr_b16 a[18:19], v179 offset:43008\n"
"v_mfma_f32_16x16x32_bf16 v[160:163], a[52:55], v[16:19], v[160:163]\n"
"s_nop 0\n"
"ds_read_b64_tr_b16 a[20:21], v179 offset:45056\n"
"ds_read_b64_tr_b16 a[22:23], v179 offset:47104\n"
"v_mfma_f32_16x16x32_bf16 v[160:163], a[56:59], v[20:23], v[160:163]\n"
"s_nop 0\n"
"buffer_load_dwordx4 v182, s[48:51], s56 offen lds\n"
"v_mfma_f32_16x16x32_bf16 v[164:167], a[36:39], v[56:59], 0\n"
"s_addk_i32 m0, 4096\n"
"v_add_u32 v177, 49152, v177\n"
"v_max_f32 v130, 0, v130\n"
"v_mfma_f32_16x16x32_bf16 v[164:167], a[40:43], v[60:63], v[164:167]\n"
"s_add_u32 s56, s56, s62\n"
"ds_read_b64_tr_b16 a[144:145], v193 offset:7936\n"
"ds_read_b64_tr_b16 a[146:147], v193 offset:11904\n"
"v_mfma_f32_16x16x32_bf16 v[164:167], a[44:47], v[64:67], v[164:167]\n"
"s_nop 0\n"
"ds_read_b64_tr_b16 a[148:149], v193 offset:7944\n"
"ds_read_b64_tr_b16 a[150:151], v193 offset:11912\n"
"v_mfma_f32_16x16x32_bf16 v[164:167], a[48:51], v[68:71], v[164:167]\n"
"s_waitcnt vmcnt(16) lgkmcnt(4)\n"
"ds_read_b64_tr_b16 a[152:153], v193 offset:7952\n"
"ds_read_b64_tr_b16 a[154:155], v193 offset:11920\n"
"v_mfma_f32_16x16x32_bf16 v[164:167], a[52:55], v[72:75], v[164:167]\n"
"s_barrier\n"
"ds_read_b64_tr_b16 a[24:25], v176 offset:0\n"
"ds_read_b64_tr_b16 a[156:157], v193 offset:7960\n"
"v_mfma_f32_16x16x32_bf16 v[164:167], a[56:59], v[76:79], v[164:167]\n"
"s_nop 0\n"
"ds_read_b64_tr_b16 a[26:27], v176 offset:2048\n"
"ds_read_b64_tr_b16 a[158:159], v193 offset:11928\n"
"v_mfma_f32_16x16x32_bf16 v[168:171], a[0:3], v[0:3], 0\n"
"s_nop 0\n"
"ds_read_b64_tr_b16 a[28:29], v176 offset:4096\n"
"ds_read_b64_tr_b16 a[160:161], v193 offset:7968\n"
"v_mfma_f32_16x16x32_bf16 v[168:171], a[4:7], v[4:7], v[168:171]\n"
"s_nop 0\n"
"ds_read_b64_tr_b16 a[30:31], v176 offset:6144\n"
"ds_read_b64_tr_b16 a[162:163], v193 offset:11936\n"
"v_mfma_f32_16x16x32_bf16 v[168:171], a[8:11], v[8:11], v[168:171]\n"
"ds_read_b64_tr_b16 a[32:33], v176 offset:8192\n"
"v_max_f32 v131, 0, v131\n"
"v_mfma_f32_16x16x32_bf16 v[168:171], a[12:15], v[12:15], v[168:171]\n"
"ds_read_b64_tr_b16 a[34:35], v176 offset:10240\n"
"v_max_f32 v132, 0, v132\n"
"v_mfma_f32_16x16x32_bf16 v[168:171], a[16:19], v[16:19], v[168:171]\n"
"ds_read_b64_tr_b16 a[36:37], v176 offset:12288\n"
"v_max_f32 v133, 0, v133\n"
"v_mfma_f32_16x16x32_bf16 v[168:171], a[20:23], v[20:23], v[168:171]\n"
"ds_read_b64_tr_b16 a[38:39], v176 offset:14336\n"
"v_max_f32 v134, 0, v134\n"
"v_mfma_f32_16x16x32_bf16 v[172:175], a[0:3], v[56:59], 0\n"
"ds_read_b64_tr_b16 a[40:41], v176 offset:16384\n"
"v_max_f32 v135, 0, v135\n"
"v_mfma_f32_16x16x32_bf16 v[172:175], a[4:7], v[60:63], v[172:175]\n"
"ds_read_b64_tr_b16 a[42:43], v176 offset:18432\n"
"v_max_f32 v136, 0, v136\n"
"v_mfma_f32_16x16x32_bf16 v[172:175], a[8:11], v[64:67], v[172:175]\n"
"ds_read_b64_tr_b16 a[44:45], v176 offset:20480\n"
"v_add_u32 v178, 49152, v178\n"
"v_mfma_f32_16x16x32_bf16 v[172:175], a[12:15], v[68:71], v[172:175]\n"
"ds_read_b64_tr_b16 a[46:47], v176 offset:22528\n"
"v_max_f32 v137, 0, v137\n"
"v_mfma_f32_16x16x32_bf16 v[172:175], a[16:19], v[72:75], v[172:175]\n"
"ds_read_b64_tr_b16 a[48:49], v177 offset:0\n"
"v_max_f32 v138, 0, v138\n"
"v_mfma_f32_16x16x32_bf16 v[172:175], a[20:23], v[76:79], v[172:175]\n"
"s_waitcnt lgkmcnt(10)\n"
"ds_read_b64_tr_b16 a[50:51], v177 offset:2048\n"
"v_max_f32 v139, 0, v139\n"
"v_mfma_f32_16x16x32_bf16 v[244:247], a[144:147], a[96:99], v[244:247]\n"
"v_max_f32 v140, 0, v140\n"
"v_max_f32 v141, 0, v141\n"
"v_mfma_f32_16x16x32_bf16 v[248:251], a[148:151], a[96:99], v[248:251]\n"
"v_max_f32 v142, 0, v142\n"
"v_max_f32 v143, 0, v143\n"
"v_mfma_f32_16x16x32_bf16 v[210:213], a[152:155], a[96:99], v[210:213]\n"
"s_nop 0\n"
"ds_read_b64_tr_b16 a[52:53], v177 offset:4096\n"
"ds_read_b64_tr_b16 a[54:55], v177 offset:6144\n"
"v_mfma_f32_16x16x32_bf16 v[214:217], a[156:159], a[96:99], v[214:217]\n"
"s_nop 0\n"
"ds_read_b64_tr_b16 a[56:57], v177 offset:8192\n"
"ds_read_b64_tr_b16 a[58:59], v177 offset:10240\n"
"v_mfma_f32_16x16x32_bf16 v[144:147], a[24:27], v[24:27], v[144:147]\n"
"s_nop 0\n"
"ds_read_b64_tr_b16 a[0:1], v177 offset:12288\n"
"ds_read_b64_tr_b16 a[2:3], v177 offset:14336\n"
"v_mfma_f32_16x16x32_bf16 v[144:147], a[28:31], v[28:31], v[144:147]\n"
"s_waitcnt lgkmcnt(12)\n"
"ds_read_b64_tr_b16 a[4:5], v177 offset:16384\n"
"ds_read_b64_tr_b16 a[6:7], v177 offset:18432\n"
"v_mfma_f32_16x16x32_bf16 v[144:147], a[32:35], v[32:35], v[144:147]\n"
"ds_read_b64_tr_b16 a[8:9], v177 offset:20480\n"
"v_add_u32 v179, 49152, v179\n"
"v_mfma_f32_16x16x32_bf16 v[144:147], a[36:39], v[36:39], v[144:147]\n"
"s_waitcnt lgkmcnt(11)\n"
"ds_read_b64_tr_b16 a[10:11], v177 offset:22528\n"
"ds_read_b64_tr_b16 a[12:13], v178 offset:0\n"
"v_mfma_f32_16x16x32_bf16 v[144:147], a[40:43], v[40:43], v[144:147]\n"
"s_nop 0\n"
"ds_read_b64_tr_b16 a[14:15], v178 offset:2048\n"
"ds_read_b64_tr_b16 a[16:17], v178 offset:4096\n"
"v_mfma_f32_16x16x32_bf16 v[144:147], a[44:47], v[44:47], v[144:147]\n"
"ds_read_b64_tr_b16 a[18:19], v178 offset:6144\n"
"v_mfma_f32_16x16x32_bf16 v[224:227], a[160:163], a[96:99], v[224:227]\n"
"v_mfma_f32_16x16x32_bf16 v[148:151], a[24:27], v[80:83], v[148:151]\n"
"ds_read_b64_tr_b16 a[20:21], v178 offset:8192\n"
"v_mfma_f32_16x16x32_bf16 v[148:151], a[28:31], v[84:87], v[148:151]\n"
"ds_read_b64_tr_b16 a[22:23], v178 offset:10240\n"
"v_mfma_f32_16x16x32_bf16 v[148:151], a[32:35], v[88:91], v[148:151]\n"
"ds_read_b64_tr_b16 a[24:25], v178 offset:12288\n"
"v_mfma_f32_16x16x32_bf16 v[148:151], a[36:39], v[92:95], v[148:151]\n"
"ds_read_b64_tr_b16 a[26:27], v178 offset:14336\n"
"v_mfma_f32_16x16x32_bf16 v[148:151], a[40:43], v[96:99], v[148:151]\n"
"ds_read_b64_tr_b16 a[28:29], v178 offset:16384\n"
"v_mfma_f32_16x16x32_bf16 v[148:151], a[44:47], v[100:103], v[148:151]\n"
"s_waitcnt lgkmcnt(15)\n"
"ds_read_b64_tr_b16 a[30:31], v178 offset:18432\n"
"v_mfma_f32_16x16x32_bf16 v[152:155], a[48:51], v[24:27], v[152:155]\n"
"ds_read_b64_tr_b16 a[32:33], v178 offset:20480\n"
"v_mfma_f32_16x16x32_bf16 v[152:155], a[52:55], v[28:31], v[152:155]\n"
"s_waitcnt lgkmcnt(15)\n"
"ds_read_b64_tr_b16 a[34:35], v178 offset:22528\n"
"v_mfma_f32_16x16x32_bf16 v[152:155], a[56:59], v[32:35], v[152:155]\n"
"ds_read_b64_tr_b16 a[36:37], v179 offset:0\n"
"v_mfma_f32_16x16x32_bf16 v[152:155], a[0:3], v[36:39], v[152:155]\n"
"s_waitcnt lgkmcnt(13)\n"
"ds_read_b64_tr_b16 a[38:39], v179 offset:2048\n"
"v_mfma_f32_16x16x32_bf16 v[152:155], a[4:7], v[40:43], v[152:155]\n"
"s_nop 0\n"
"ds_read_b64_tr_b16 a[40:41], v179 offset:4096\n"
"ds_read_b64_tr_b16 a[42:43], v179 offset:6144\n"
"v_mfma_f32_16x16x32_bf16 v[152:155], a[8:11], v[44:47], v[152:155]\n"
"s_nop 0\n"
"ds_read_b64_tr_b16 a[44:45], v179 offset:8192\n"
"ds_read_b64_tr_b16 a[46:47], v179 offset:10240\n"
"v_mfma_f32_16x16x32_bf16 v[156:159], a[48:51], v[80:83], v[156:159]\n"
"s_nop 0\n"
"ds_read_b64_tr_b16 a[48:49], v179 offset:12288\n"
"ds_read_b64_tr_b16 a[50:51], v179 offset:14336\n"
"v_mfma_f32_16x16x32_bf16 v[156:159], a[52:55], v[84:87], v[156:159]\n"
"s_nop 0\n"
"buffer_load_dwordx4 v182, s[48:51], s56 offen lds\n"
"v_mfma_f32_16x16x32_bf16 v[156:159], a[56:59], v[88:91], v[156:159]\n"
"s_addk_i32 m0, 4096\n"
"s_add_u32 s56, s56, s62\n"
"ds_read_b64_tr_b16 a[52:53], v179 offset:16384\n"
"v_mfma_f32_16x16x32_bf16 v[156:159], a[0:3], v[92:95], v[156:159]\n"
"s_nop 0\n"
"ds_read_b64_tr_b16 a[54:55], v179 offset:18432\n"
"ds_read_b64_tr_b16 a[56:57], v179 offset:20480\n"
"v_mfma_f32_16x16x32_bf16 v[156:159], a[4:7], v[96:99], v[156:159]\n"
"ds_read_b64_tr_b16 a[58:59], v179 offset:22528\n"
"v_mfma_f32_16x16x32_bf16 v[156:159], a[8:11], v[100:103], v[156:159]\n"
"s_waitcnt lgkmcnt(15)\n"
"v_mfma_f32_16x16x32_bf16 v[160:163], a[12:15], v[24:27], v[160:163]\n"
"v_mfma_f32_16x16x32_bf16 v[160:163], a[16:19], v[28:31], v[160:163]\n"
"v_mfma_f32_16x16x32_bf16 v[160:163], a[20:23], v[32:35], v[160:163]\n"
"v_mfma_f32_16x16x32_bf16 v[160:163], a[24:27], v[36:39], v[160:163]\n"
"s_waitcnt lgkmcnt(12)\n"
"buffer_load_dwordx4 v182, s[48:51], s56 offen lds\n"
"v_mfma_f32_16x16x32_bf16 v[160:163], a[28:31], v[40:43], v[160:163]\n"
"s_addk_i32 m0, 4096\n"
"s_add_u32 s56, s56, s62\n"
"v_mfma_f32_16x16x32_bf16 v[160:163], a[32:35], v[44:47], v[160:163]\n"
"v_mfma_f32_16x16x32_bf16 v[164:167], a[12:15], v[80:83], v[164:167]\n"
"v_mfma_f32_16x16x32_bf16 v[164:167], a[16:19], v[84:87], v[164:167]\n"
"v_mfma_f32_16x16x32_bf16 v[164:167], a[20:23], v[88:91], v[164:167]\n"
"v_mfma_f32_16x16x32_bf16 v[164:167], a[24:27], v[92:95], v[164:167]\n"
"s_waitcnt vmcnt(12) lgkmcnt(0)\n"
"v_mfma_f32_16x16x32_bf16 v[164:167], a[28:31], v[96:99], v[164:167]\n"
"s_barrier\n"
"ds_read_b64_tr_b16 a[0:1], v176 offset:24576\n"
"ds_read_b64_tr_b16 a[2:3], v176 offset:26624\n"
"v_mfma_f32_16x16x32_bf16 v[164:167], a[32:35], v[100:103], v[164:167]\n"
"s_nop 0\n"
"buffer_load_dwordx4 v182, s[48:51], s56 offen lds\n"
"v_mfma_f32_16x16x32_bf16 v[168:171], a[36:39], v[24:27], v[168:171]\n"
"s_addk_i32 m0, 4096\n"
"ds_read_b64_tr_b16 a[4:5], v176 offset:28672\n"
"v_lshlrev_b32 v181, 7, v187\n"
"v_mfma_f32_16x16x32_bf16 v[168:171], a[40:43], v[28:31], v[168:171]\n"
"s_add_u32 s56, s56, s62\n"
"ds_read_b64_tr_b16 a[6:7], v176 offset:30720\n"
"v_min_u32 v181, 1280, v181\n"
"v_mfma_f32_16x16x32_bf16 v[168:171], a[44:47], v[32:35], v[168:171]\n"
"s_add_u32 s79, s63, 0\n"
"ds_read_b64_tr_b16 a[8:9], v176 offset:32768\n"
"s_mul_i32 s79, s79, 1408\n"
"v_mfma_f32_16x16x32_bf16 v[168:171], a[48:51], v[36:39], v[168:171]\n"
"s_min_u32 s79, s79, 40064\n"
"ds_read_b64_tr_b16 a[10:11], v176 offset:34816\n"
"s_lshr_b32 s80, s53, 0\n"
"v_mfma_f32_16x16x32_bf16 v[168:171], a[52:55], v[40:43], v[168:171]\n"
"s_mul_i32 s80, s80, 41472\n"
"ds_read_b64_tr_b16 a[12:13], v176 offset:36864\n"
"s_add_u32 s79, s79, s80\n"
"v_mfma_f32_16x16x32_bf16 v[168:171], a[56:59], v[44:47], v[168:171]\n"
"ds_read_b64_tr_b16 a[14:15], v176 offset:38912\n"
"v_accvgpr_read_b32 v252, a250\n"
"v_mfma_f32_16x16x32_bf16 v[172:175], a[36:39], v[80:83], v[172:175]\n"
"s_nop 0\n"
"ds_read_b64_tr_b16 a[16:17], v176 offset:40960\n"
"ds_read_b64_tr_b16 a[18:19], v176 offset:43008\n"
"v_mfma_f32_16x16x32_bf16 v[172:175], a[40:43], v[84:87], v[172:175]\n"
"s_nop 0\n"
"buffer_load_dword v203, v181, s[20:23], s79 offen\n"
"v_mfma_f32_16x16x32_bf16 v[172:175], a[44:47], v[88:91], v[172:175]\n"
"ds_read_b64_tr_b16 a[20:21], v176 offset:45056\n"
"v_lshlrev_b32 v181, 7, v187\n"
"v_mfma_f32_16x16x32_bf16 v[172:175], a[48:51], v[92:95], v[172:175]\n"
"ds_read_b64_tr_b16 a[22:23], v176 offset:47104\n"
"v_min_u32 v181, 2048, v181\n"
"v_mfma_f32_16x16x32_bf16 v[172:175], a[52:55], v[96:99], v[172:175]\n"
"s_add_u32 s79, s63, 128\n"
"ds_read_b64_tr_b16 a[24:25], v177 offset:24576\n"
"s_mul_i32 s79, s79, 2176\n"
"v_mfma_f32_16x16x32_bf16 v[172:175], a[56:59], v[100:103], v[172:175]\n"
"s_waitcnt lgkmcnt(9)\n"
"ds_read_b64_tr_b16 a[26:27], v177 offset:26624\n"
"s_min_u32 s79, s79, 329600\n"
"v_mfma_f32_16x16x32_bf16 v[144:147], a[0:3], v[48:51], v[144:147]\n"
"s_lshr_b32 s80, s53, 0\n"
"ds_read_b64_tr_b16 a[28:29], v177 offset:28672\n"
"s_mul_i32 s80, s80, 331776\n"
"v_mfma_f32_16x16x32_bf16 v[144:147], a[4:7], v[52:55], v[144:147]\n"
"s_waitcnt lgkmcnt(7)\n"
"ds_read_b64_tr_b16 a[30:31], v177 offset:30720\n"
"s_add_u32 s79, s79, s80\n"
"v_mfma_f32_16x16x32_bf16 v[144:147], a[8:11], a[172:175], v[144:147]\n"
"s_nop 0\n"
"ds_read_b64_tr_b16 a[32:33], v177 offset:32768\n"
"ds_read_b64_tr_b16 a[34:35], v177 offset:34816\n"
"v_mfma_f32_16x16x32_bf16 v[144:147], a[12:15], a[176:179], v[144:147]\n"
"s_waitcnt lgkmcnt(6)\n"
"v_cmp_gt_u32 vcc, 16, v187\n"
"s_nop 1\n"
"v_cndmask_b32 v228, v228, v252, vcc\n"
"v_cndmask_b32 v229, v229, v252, vcc\n"
"ds_write_b64 v189, v[232:233] offset:0\n"
"ds_write_b64 v189, v[234:235] offset:8\n"
"ds_write_b64 v189, v[236:237] offset:16\n"
"ds_write_b64 v189, v[238:239] offset:24\n"
"ds_write_b64 v189, v[240:241] offset:32\n"
"ds_write_b32 v191, v228\n"
"ds_write_b32 v192, v229 offset:8\n"
"ds_write_b64 v190, v[222:223] offset:152\n"
"ds_write_b64 v190, v[230:231] offset:192\n"
"v_mfma_f32_16x16x32_bf16 v[144:147], a[16:19], a[180:183], v[144:147]\n"
"s_nop 0\n"
"ds_read_b64_tr_b16 a[36:37], v177 offset:36864\n"
"ds_read_b64_tr_b16 a[38:39], v177 offset:38912\n"
"v_mfma_f32_16x16x32_bf16 v[144:147], a[20:23], a[184:187], v[144:147]\n"
"s_nop 0\n"
"buffer_load_dword v203, v181, s[36:39], s79 offen\n"
"v_mfma_f32_16x16x32_bf16 v[148:151], a[0:3], v[104:107], v[148:151]\n"
"s_nop 0\n"
"ds_read_b64_tr_b16 a[40:41], v177 offset:40960\n"
"ds_read_b64_tr_b16 a[42:43], v177 offset:43008\n"
"v_mfma_f32_16x16x32_bf16 v[148:151], a[4:7], v[108:111], v[148:151]\n"
"s_nop 0\n"
"s_cmp_lt_u32 s54, 3\n"
"s_cselect_b32 s60, 0, 0x07000000\n"
"v_mfma_f32_16x16x32_bf16 v[148:151], a[8:11], a[188:191], v[148:151]\n"
"ds_read_b64_tr_b16 a[44:45], v177 offset:45056\n"
"v_mul_f32 v144, v112, v144\n"
"v_mfma_f32_16x16x32_bf16 v[148:151], a[12:15], a[192:195], v[148:151]\n"
"ds_read_b64_tr_b16 a[46:47], v177 offset:47104\n"
"v_mul_f32 v145, v113, v145\n"
"v_mfma_f32_16x16x32_bf16 v[148:151], a[16:19], a[196:199], v[148:151]\n"
"ds_read_b64_tr_b16 a[48:49], v178 offset:24576\n"
"v_mul_f32 v146, v114, v146\n"
"v_mfma_f32_16x16x32_bf16 v[148:151], a[20:23], a[200:203], v[148:151]\n"
"s_waitcnt lgkmcnt(15)\n"
"ds_read_b64_tr_b16 a[50:51], v178 offset:26624\n"
"v_mul_f32 v147, v115, v147\n"
"v_mfma_f32_16x16x32_bf16 v[152:155], a[24:27], v[48:51], v[152:155]\n"
"ds_read_b64_tr_b16 a[52:53], v178 offset:28672\n"
"v_or_b32 v189, s60, v195\n"
"v_mfma_f32_16x16x32_bf16 v[152:155], a[28:31], v[52:55], v[152:155]\n"
"s_waitcnt lgkmcnt(7)\n"
"ds_read_b64_tr_b16 a[54:55], v178 offset:30720\n"
"ds_read_b64_tr_b16 a[56:57], v178 offset:32768\n"
"v_mfma_f32_16x16x32_bf16 v[152:155], a[32:35], a[172:175], v[152:155]\n"
"s_nop 0\n"
"buffer_load_dword v203, v196, s[28:31], s99 offen offset:320\n"
"v_mfma_f32_16x16x32_bf16 v[152:155], a[36:39], a[176:179], v[152:155]\n"
"s_waitcnt lgkmcnt(5)\n"
"ds_read_b64_tr_b16 a[58:59], v178 offset:34816\n"
"ds_read_b64_tr_b16 a[0:1], v178 offset:36864\n"
"v_mfma_f32_16x16x32_bf16 v[152:155], a[40:43], a[180:183], v[152:155]\n"
"s_nop 0\n"
"buffer_load_dwordx4 v182, s[48:51], s56 offen lds\n"
"v_mfma_f32_16x16x32_bf16 v[152:155], a[44:47], a[184:187], v[152:155]\n"
"s_addk_i32 m0, 4096\n"
"ds_read_b64_tr_b16 a[2:3], v178 offset:38912\n"
"v_mul_f32 v148, v116, v148\n"
"v_mfma_f32_16x16x32_bf16 v[156:159], a[24:27], v[104:107], v[156:159]\n"
"s_add_u32 s56, s56, s62\n"
"ds_read_b64_tr_b16 a[4:5], v178 offset:40960\n"
"v_mul_f32 v149, v117, v149\n"
"v_mfma_f32_16x16x32_bf16 v[156:159], a[28:31], v[108:111], v[156:159]\n"
"ds_read_b64_tr_b16 a[6:7], v178 offset:43008\n"
"v_mul_f32 v150, v118, v150\n"
"v_mfma_f32_16x16x32_bf16 v[156:159], a[32:35], a[188:191], v[156:159]\n"
"ds_read_b64_tr_b16 a[8:9], v178 offset:45056\n"
"v_mul_f32 v151, v119, v151\n"
"v_mfma_f32_16x16x32_bf16 v[156:159], a[36:39], a[192:195], v[156:159]\n"
"ds_read_b64_tr_b16 a[10:11], v178 offset:47104\n"
"v_mul_f32 v152, v120, v152\n"
"v_mfma_f32_16x16x32_bf16 v[156:159], a[40:43], a[196:199], v[156:159]\n"
"s_nop 0\n"
"ds_read_b64_tr_b16 a[12:13], v179 offset:24576\n"
"ds_read_b64_tr_b16 a[14:15], v179 offset:26624\n"
"v_mfma_f32_16x16x32_bf16 v[156:159], a[44:47], a[200:203], v[156:159]\n"
"s_waitcnt lgkmcnt(10)\n"
"buffer_load_dwordx4 v182, s[48:51], s56 offen lds\n"
"v_mfma_f32_16x16x32_bf16 v[160:163], a[48:51], v[48:51], v[160:163]\n"
"s_addk_i32 m0, 4096\n"
"ds_read_b64_tr_b16 a[16:17], v179 offset:28672\n"
"ds_read_b64_tr_b16 a[18:19], v179 offset:30720\n"
"v_mfma_f32_16x16x32_bf16 v[160:163], a[52:55], v[52:55], v[160:163]\n"
"s_waitcnt lgkmcnt(8)\n"
"ds_read_b64_tr_b16 a[20:21], v179 offset:32768\n"
"ds_read_b64_tr_b16 a[22:23], v179 offset:34816\n"
"v_mfma_f32_16x16x32_bf16 v[160:163], a[56:59], a[172:175], v[160:163]\n"
"s_add_u32 s56, s56, s62\n"
"ds_read_b64_tr_b16 a[24:25], v179 offset:36864\n"
"ds_read_b64_tr_b16 a[26:27], v179 offset:38912\n"
"v_mfma_f32_16x16x32_bf16 v[160:163], a[0:3], a[176:179], v[160:163]\n"
"s_waitcnt lgkmcnt(8)\n"
"ds_read_b64_tr_b16 a[28:29], v179 offset:40960\n"
"ds_read_b64_tr_b16 a[30:31], v179 offset:43008\n"
"v_mfma_f32_16x16x32_bf16 v[160:163], a[4:7], a[180:183], v[160:163]\n"
"s_nop 0\n"
"ds_read_b64_tr_b16 a[32:33], v179 offset:45056\n"
"ds_read_b64_tr_b16 a[34:35], v179 offset:47104\n"
"v_mfma_f32_16x16x32_bf16 v[160:163], a[8:11], a[184:187], v[160:163]\n"
"v_mul_f32 v153, v121, v153\n"
"v_mul_f32 v154, v122, v154\n"
"v_mfma_f32_16x16x32_bf16 v[164:167], a[48:51], v[104:107], v[164:167]\n"
"v_mul_f32 v155, v123, v155\n"
"v_mul_f32 v156, v124, v156\n"
"v_mfma_f32_16x16x32_bf16 v[164:167], a[52:55], v[108:111], v[164:167]\n"
"v_mul_f32 v157, v125, v157\n"
"v_mul_f32 v158, v126, v158\n"
"v_mfma_f32_16x16x32_bf16 v[164:167], a[56:59], a[188:191], v[164:167]\n"
"s_nop 0\n"
"buffer_load_dwordx4 v182, s[48:51], s56 offen lds\n"
"v_mfma_f32_16x16x32_bf16 v[164:167], a[0:3], a[192:195], v[164:167]\n"
"s_waitcnt vmcnt(12) lgkmcnt(0)\n"
"v_mul_f32 v159, v127, v159\n"
"v_mul_f32 v160, v128, v160\n"
"v_mfma_f32_16x16x32_bf16 v[164:167], a[4:7], a[196:199], v[164:167]\n"
"s_barrier\n"
"ds_read_b64_tr_b16 a[36:37], v200 offset:0\n"
"v_mul_f32 v161, v129, v161\n"
"v_mfma_f32_16x16x32_bf16 v[164:167], a[8:11], a[200:203], v[164:167]\n"
"s_addk_i32 m0, 4096\n"
"ds_read_b64_tr_b16 a[38:39], v200 offset:1024\n"
"v_mul_f32 v162, v130, v162\n"
"v_mfma_f32_16x16x32_bf16 v[168:171], a[12:15], v[48:51], v[168:171]\n"
"s_add_u32 s56, s56, s62\n"
"ds_read_b64_tr_b16 a[40:41], v200 offset:4096\n"
"v_mul_f32 v163, v131, v163\n"
"v_mfma_f32_16x16x32_bf16 v[168:171], a[16:19], v[52:55], v[168:171]\n"
"ds_read_b64_tr_b16 a[42:43], v200 offset:5120\n"
"v_mul_f32 v164, v132, v164\n"
"v_mfma_f32_16x16x32_bf16 v[168:171], a[20:23], a[172:175], v[168:171]\n"
"ds_read_b64_tr_b16 a[44:45], v200 offset:8192\n"
"v_mul_f32 v165, v133, v165\n"
"v_mfma_f32_16x16x32_bf16 v[168:171], a[24:27], a[176:179], v[168:171]\n"
"ds_read_b64_tr_b16 a[46:47], v200 offset:9216\n"
"v_mul_f32 v166, v134, v166\n"
"v_mfma_f32_16x16x32_bf16 v[168:171], a[28:31], a[180:183], v[168:171]\n"
"ds_read_b64_tr_b16 a[48:49], v200 offset:12288\n"
"v_mul_f32 v167, v135, v167\n"
"v_mfma_f32_16x16x32_bf16 v[168:171], a[32:35], a[184:187], v[168:171]\n"
"ds_read_b64_tr_b16 a[50:51], v200 offset:13312\n"
"v_lshlrev_b32 v190, 3, v189\n"
"v_mfma_f32_16x16x32_bf16 v[172:175], a[12:15], v[104:107], v[172:175]\n"
"ds_read_b64_tr_b16 a[52:53], v200 offset:16384\n"
"v_lshlrev_b32 v189, 4, v189\n"
"v_mfma_f32_16x16x32_bf16 v[172:175], a[16:19], v[108:111], v[172:175]\n"
"s_lshl_b32 s58, s53, 4\n"
"ds_read_b64_tr_b16 a[54:55], v200 offset:17408\n"
"v_mul_f32 v168, v136, v168\n"
"v_mfma_f32_16x16x32_bf16 v[172:175], a[20:23], a[188:191], v[172:175]\n"
"s_add_u32 s58, s58, s52\n"
"ds_read_b64_tr_b16 a[56:57], v200 offset:20480\n"
"v_mul_f32 v169, v137, v169\n"
"v_mfma_f32_16x16x32_bf16 v[172:175], a[24:27], a[192:195], v[172:175]\n"
"s_lshl_b32 s61, s54, 8\n"
"ds_read_b64_tr_b16 a[58:59], v200 offset:21504\n"
"v_mul_f32 v170, v138, v170\n"
"v_mfma_f32_16x16x32_bf16 v[172:175], a[28:31], a[196:199], v[172:175]\n"
"s_add_u32 s61, s61, s58\n"
"ds_read_b64_tr_b16 a[0:1], v201 offset:0\n"
"v_mul_f32 v171, v139, v171\n"
"v_mfma_f32_16x16x32_bf16 v[172:175], a[32:35], a[200:203], v[172:175]\n"
"s_waitcnt lgkmcnt(9)\n"
"ds_read_b64_tr_b16 a[2:3], v201 offset:1024\n"
"s_mul_i32 s61, s61, 128\n"
"v_mfma_f32_16x16x32_bf16 v[112:115], a[36:39], v[0:3], 0\n"
"s_add_u32 s61, s61, 4096\n"
"ds_read_b64_tr_b16 a[4:5], v201 offset:4096\n"
"s_lshl_b32 s58, s58, 12\n"
"v_mfma_f32_16x16x32_bf16 v[112:115], a[40:43], v[4:7], v[112:115]\n"
"s_waitcnt lgkmcnt(7)\n"
"ds_read_b64_tr_b16 a[6:7], v201 offset:5120\n"
"v_mul_f32 v172, v140, v172\n"
"v_mfma_f32_16x16x32_bf16 v[112:115], a[44:47], v[8:11], v[112:115]\n"
"s_add_u32 s58, s58, 524288\n"
"ds_read_b64_tr_b16 a[8:9], v201 offset:8192\n"
"v_mul_f32 v173, v141, v173\n"
"v_mfma_f32_16x16x32_bf16 v[112:115], a[48:51], v[12:15], v[112:115]\n"
"s_waitcnt lgkmcnt(5)\n"
"ds_read_b64_tr_b16 a[10:11], v201 offset:9216\n"
"v_mul_f32 v174, v142, v174\n"
"v_mfma_f32_16x16x32_bf16 v[112:115], a[52:55], v[16:19], v[112:115]\n"
"s_lshl_b32 s59, s54, 8\n"
"ds_read_b64_tr_b16 a[12:13], v201 offset:12288\n"
"v_mul_f32 v175, v143, v175\n"
"v_mfma_f32_16x16x32_bf16 v[112:115], a[56:59], v[20:23], v[112:115]\n"
"s_add_u32 s59, s59, s58\n"
"ds_read_b64_tr_b16 a[14:15], v201 offset:13312\n"
"s_add_u32 s58, s58, s55\n"
"v_mfma_f32_16x16x32_bf16 v[116:119], a[36:39], v[56:59], 0\n"
"s_nop 0\n"
"ds_read_b64_tr_b16 a[16:17], v201 offset:16384\n"
"ds_read_b64_tr_b16 a[96:97], v194 offset:0\n"
"v_mfma_f32_16x16x32_bf16 v[116:119], a[40:43], v[60:63], v[116:119]\n"
"s_nop 0\n"
"ds_read_b64_tr_b16 a[18:19], v201 offset:17408\n"
"ds_read_b64_tr_b16 a[98:99], v194 offset:3968\n"
"v_mfma_f32_16x16x32_bf16 v[116:119], a[44:47], v[64:67], v[116:119]\n"
"s_nop 0\n"
"ds_read_b64_tr_b16 a[20:21], v201 offset:20480\n"
"ds_read_b64_tr_b16 a[144:145], v193 offset:0\n"
"v_mfma_f32_16x16x32_bf16 v[116:119], a[48:51], v[68:71], v[116:119]\n"
"s_nop 0\n"
"ds_read_b64_tr_b16 a[22:23], v201 offset:21504\n"
"ds_read_b64_tr_b16 a[146:147], v193 offset:3968\n"
"v_mfma_f32_16x16x32_bf16 v[116:119], a[52:55], v[72:75], v[116:119]\n"
"s_nop 0\n"
"ds_read_b64_tr_b16 a[24:25], v200 offset:2048\n"
"ds_read_b64_tr_b16 a[148:149], v193 offset:8\n"
"v_mfma_f32_16x16x32_bf16 v[116:119], a[56:59], v[76:79], v[116:119]\n"
"s_waitcnt lgkmcnt(14)\n"
"ds_read_b64_tr_b16 a[26:27], v200 offset:3072\n"
"ds_read_b64_tr_b16 a[150:151], v193 offset:3976\n"
"v_mfma_f32_16x16x32_bf16 v[120:123], a[0:3], v[0:3], 0\n"
"s_nop 0\n"
"ds_read_b64_tr_b16 a[28:29], v200 offset:6144\n"
"ds_read_b64_tr_b16 a[152:153], v193 offset:16\n"
"v_mfma_f32_16x16x32_bf16 v[120:123], a[4:7], v[4:7], v[120:123]\n"
"s_waitcnt lgkmcnt(14)\n"
"ds_read_b64_tr_b16 a[30:31], v200 offset:7168\n"
"ds_read_b64_tr_b16 a[154:155], v193 offset:3984\n"
"v_mfma_f32_16x16x32_bf16 v[120:123], a[8:11], v[8:11], v[120:123]\n"
"s_nop 0\n"
"ds_read_b64_tr_b16 a[32:33], v200 offset:10240\n"
"ds_read_b64_tr_b16 a[156:157], v193 offset:24\n"
"v_mfma_f32_16x16x32_bf16 v[120:123], a[12:15], v[12:15], v[120:123]\n"
"s_waitcnt lgkmcnt(11)\n"
"ds_read_b64_tr_b16 a[34:35], v200 offset:11264\n"
"ds_read_b64_tr_b16 a[158:159], v193 offset:3992\n"
"v_mfma_f32_16x16x32_bf16 v[120:123], a[16:19], v[16:19], v[120:123]\n"
"s_nop 0\n"
"ds_read_b64_tr_b16 a[36:37], v200 offset:14336\n"
"ds_read_b64_tr_b16 a[160:161], v193 offset:32\n"
"v_mfma_f32_16x16x32_bf16 v[120:123], a[20:23], v[20:23], v[120:123]\n"
"s_nop 0\n"
"ds_read_b64_tr_b16 a[38:39], v200 offset:15360\n"
"ds_read_b64_tr_b16 a[162:163], v193 offset:4000\n"
"v_mfma_f32_16x16x32_bf16 v[124:127], a[0:3], v[56:59], 0\n"
"ds_read_b64_tr_b16 a[40:41], v200 offset:18432\n"
"v_cvt_pk_bf16_f32 v144, v144, v145\n"
"v_mfma_f32_16x16x32_bf16 v[124:127], a[4:7], v[60:63], v[124:127]\n"
"ds_read_b64_tr_b16 a[42:43], v200 offset:19456\n"
"v_cvt_pk_bf16_f32 v145, v146, v147\n"
"v_mfma_f32_16x16x32_bf16 v[124:127], a[8:11], v[64:67], v[124:127]\n"
"ds_read_b64_tr_b16 a[44:45], v200 offset:22528\n"
"v_accvgpr_write_b32 a68, v144\n"
"v_mfma_f32_16x16x32_bf16 v[124:127], a[12:15], v[68:71], v[124:127]\n"
"ds_read_b64_tr_b16 a[46:47], v200 offset:23552\n"
"v_accvgpr_write_b32 a69, v145\n"
"v_mfma_f32_16x16x32_bf16 v[124:127], a[16:19], v[72:75], v[124:127]\n"
"106:\n"
"s_cbranch_execz 110f\n"
"ds_read_b64_tr_b16 a[48:49], v201 offset:2048\n"
"v_cvt_pk_bf16_f32 v148, v148, v149\n"
"v_mfma_f32_16x16x32_bf16 v[124:127], a[20:23], v[76:79], v[124:127]\n"
"s_waitcnt lgkmcnt(14)\n"
"ds_read_b64_tr_b16 a[50:51], v201 offset:3072\n"
"v_cvt_pk_bf16_f32 v149, v150, v151\n"
"v_mfma_f32_16x16x32_bf16 v[244:247], a[144:147], a[96:99], v[244:247]\n"
"v_accvgpr_write_b32 a110, v148\n"
"v_accvgpr_write_b32 a111, v149\n"
"v_mfma_f32_16x16x32_bf16 v[248:251], a[148:151], a[96:99], v[248:251]\n"
"v_cvt_pk_bf16_f32 v152, v152, v153\n"
"v_cvt_pk_bf16_f32 v153, v154, v155\n"
"v_mfma_f32_16x16x32_bf16 v[128:131], a[24:27], v[0:3], 0\n"
"s_nop 0\n"
"ds_read_b64_tr_b16 a[52:53], v201 offset:6144\n"
"ds_read_b64_tr_b16 a[54:55], v201 offset:7168\n"
"v_mfma_f32_16x16x32_bf16 v[128:131], a[28:31], v[4:7], v[128:131]\n"
"s_waitcnt lgkmcnt(9)\n"
"ds_read_b64_tr_b16 a[56:57], v201 offset:10240\n"
"ds_read_b64_tr_b16 a[58:59], v201 offset:11264\n"
"v_mfma_f32_16x16x32_bf16 v[128:131], a[32:35], v[8:11], v[128:131]\n"
"s_nop 0\n"
"ds_read_b64_tr_b16 a[0:1], v201 offset:14336\n"
"ds_read_b64_tr_b16 a[2:3], v201 offset:15360\n"
"v_mfma_f32_16x16x32_bf16 v[128:131], a[36:39], v[12:15], v[128:131]\n"
"s_waitcnt lgkmcnt(8)\n"
"ds_read_b64_tr_b16 a[4:5], v201 offset:18432\n"
"ds_read_b64_tr_b16 a[6:7], v201 offset:19456\n"
"v_mfma_f32_16x16x32_bf16 v[128:131], a[40:43], v[16:19], v[128:131]\n"
"s_nop 0\n"
"ds_read_b64_tr_b16 a[8:9], v201 offset:22528\n"
"ds_read_b64_tr_b16 a[10:11], v201 offset:23552\n"
"v_mfma_f32_16x16x32_bf16 v[128:131], a[44:47], v[20:23], v[128:131]\n"
"s_nop 0\n"
"buffer_load_dwordx4 v182, s[48:51], s56 offen lds\n"
"v_mfma_f32_16x16x32_bf16 v[210:213], a[152:155], a[96:99], v[210:213]\n"
"s_addk_i32 m0, 4096\n"
"v_accvgpr_write_b32 a70, v152\n"
"v_accvgpr_write_b32 a71, v153\n"
"v_mfma_f32_16x16x32_bf16 v[214:217], a[156:159], a[96:99], v[214:217]\n"
"s_add_u32 s56, s56, s62\n"
"v_cvt_pk_bf16_f32 v156, v156, v157\n"
"v_cvt_pk_bf16_f32 v157, v158, v159\n"
"v_accvgpr_write_b32 a112, v156\n"
"v_mfma_f32_16x16x32_bf16 v[224:227], a[160:163], a[96:99], v[224:227]\n"
"s_nop 0\n"
"ds_read_b64_tr_b16 a[96:97], v194 offset:7936\n"
"ds_read_b64_tr_b16 a[98:99], v194 offset:11904\n"
"v_mfma_f32_16x16x32_bf16 v[132:135], a[24:27], v[56:59], 0\n"
"s_nop 0\n"
"buffer_load_dwordx4 v182, s[48:51], s56 offen lds\n"
"v_mfma_f32_16x16x32_bf16 v[132:135], a[28:31], v[60:63], v[132:135]\n"
"s_addk_i32 m0, 4096\n"
"ds_read_b64_tr_b16 a[144:145], v193 offset:7936\n"
"ds_read_b64_tr_b16 a[146:147], v193 offset:11904\n"
"ds_read_b64_tr_b16 a[148:149], v193 offset:7944\n"
"ds_read_b64_tr_b16 a[150:151], v193 offset:11912\n"
"v_mfma_f32_16x16x32_bf16 v[132:135], a[32:35], v[64:67], v[132:135]\n"
"s_add_u32 s56, s56, s62\n"
"ds_read_b64_tr_b16 a[152:153], v193 offset:7952\n"
"ds_read_b64_tr_b16 a[154:155], v193 offset:11920\n"
"v_mfma_f32_16x16x32_bf16 v[132:135], a[36:39], v[68:71], v[132:135]\n"
"s_waitcnt vmcnt(8) lgkmcnt(8)\n"
"ds_read_b64_tr_b16 a[156:157], v193 offset:7960\n"
"ds_read_b64_tr_b16 a[158:159], v193 offset:11928\n"
"v_mfma_f32_16x16x32_bf16 v[132:135], a[40:43], v[72:75], v[132:135]\n"
"s_barrier\n"
"ds_read_b64_tr_b16 a[12:13], v200 offset:24576\n"
"ds_read_b64_tr_b16 a[160:161], v193 offset:7968\n"
"v_mfma_f32_16x16x32_bf16 v[132:135], a[44:47], v[76:79], v[132:135]\n"
"s_nop 0\n"
"ds_read_b64_tr_b16 a[14:15], v200 offset:25600\n"
"ds_read_b64_tr_b16 a[162:163], v193 offset:11936\n"
"v_mfma_f32_16x16x32_bf16 v[136:139], a[48:51], v[0:3], 0\n"
"s_nop 0\n"
"ds_read_b64_tr_b16 a[16:17], v200 offset:28672\n"
"ds_read_b64_tr_b16 a[18:19], v200 offset:29696\n"
"v_mfma_f32_16x16x32_bf16 v[136:139], a[52:55], v[4:7], v[136:139]\n"
"s_nop 0\n"
"buffer_load_dwordx4 v182, s[48:51], s56 offen lds\n"
"v_mfma_f32_16x16x32_bf16 v[136:139], a[56:59], v[8:11], v[136:139]\n"
"s_addk_i32 m0, 4096\n"
"ds_read_b64_tr_b16 a[20:21], v200 offset:32768\n"
"v_accvgpr_write_b32 a113, v157\n"
"v_cvt_pk_bf16_f32 v160, v160, v161\n"
"v_mfma_f32_16x16x32_bf16 v[136:139], a[0:3], v[12:15], v[136:139]\n"
"s_mov_b32 s56, 256\n"
"ds_read_b64_tr_b16 a[22:23], v200 offset:33792\n"
"v_cvt_pk_bf16_f32 v161, v162, v163\n"
"v_mfma_f32_16x16x32_bf16 v[136:139], a[4:7], v[16:19], v[136:139]\n"
"ds_read_b64_tr_b16 a[24:25], v200 offset:36864\n"
"v_accvgpr_write_b32 a72, v160\n"
"v_mfma_f32_16x16x32_bf16 v[136:139], a[8:11], v[20:23], v[136:139]\n"
"ds_read_b64_tr_b16 a[26:27], v200 offset:37888\n"
"v_accvgpr_write_b32 a73, v161\n"
"v_cvt_pk_bf16_f32 v164, v164, v165\n"
"v_mfma_f32_16x16x32_bf16 v[140:143], a[48:51], v[56:59], 0\n"
"ds_read_b64_tr_b16 a[28:29], v200 offset:40960\n"
"v_cvt_pk_bf16_f32 v165, v166, v167\n"
"v_mfma_f32_16x16x32_bf16 v[140:143], a[52:55], v[60:63], v[140:143]\n"
"ds_read_b64_tr_b16 a[30:31], v200 offset:41984\n"
"v_accvgpr_write_b32 a114, v164\n"
"v_accvgpr_write_b32 a115, v165\n"
"v_mfma_f32_16x16x32_bf16 v[140:143], a[56:59], v[64:67], v[140:143]\n"
"s_nop 0\n"
"ds_read_b64_tr_b16 a[32:33], v200 offset:45056\n"
"ds_read_b64_tr_b16 a[34:35], v200 offset:46080\n"
"v_cvt_pk_bf16_f32 v168, v168, v169\n"
"v_mfma_f32_16x16x32_bf16 v[140:143], a[0:3], v[68:71], v[140:143]\n"
"s_nop 0\n"
"buffer_load_dwordx4 v180, s[24:27], s56 offen lds\n"
"v_mfma_f32_16x16x32_bf16 v[140:143], a[4:7], v[72:75], v[140:143]\n"
"s_addk_i32 m0, 4096\n"
"ds_read_b64_tr_b16 a[36:37], v201 offset:24576\n"
"v_cvt_pk_bf16_f32 v169, v170, v171\n"
"v_accvgpr_write_b32 a74, v168\n"
"v_accvgpr_write_b32 a75, v169\n"
"v_mfma_f32_16x16x32_bf16 v[140:143], a[8:11], v[76:79], v[140:143]\n"
"s_waitcnt lgkmcnt(9)\n"
"ds_read_b64_tr_b16 a[38:39], v201 offset:25600\n"
"v_cvt_pk_bf16_f32 v172, v172, v173\n"
"v_cvt_pk_bf16_f32 v173, v174, v175\n"
"v_accvgpr_write_b32 a116, v172\n"
"v_mfma_f32_16x16x32_bf16 v[244:247], a[144:147], a[96:99], v[244:247]\n"
"s_add_u32 s56, s56, s62\n"
"v_accvgpr_write_b32 a117, v173\n"
"v_mfma_f32_16x16x32_bf16 v[248:251], a[148:151], a[96:99], v[248:251]\n"
"s_nop 0\n"
"ds_read_b64_tr_b16 a[40:41], v201 offset:28672\n"
"ds_read_b64_tr_b16 a[42:43], v201 offset:29696\n"
"v_mfma_f32_16x16x32_bf16 v[210:213], a[152:155], a[96:99], v[210:213]\n"
"s_nop 0\n"
"ds_read_b64_tr_b16 a[44:45], v201 offset:32768\n"
"ds_read_b64_tr_b16 a[46:47], v201 offset:33792\n"
"v_mfma_f32_16x16x32_bf16 v[214:217], a[156:159], a[96:99], v[214:217]\n"
"s_nop 0\n"
"buffer_load_dwordx4 v180, s[24:27], s56 offen lds\n"
"v_mfma_f32_16x16x32_bf16 v[224:227], a[160:163], a[96:99], v[224:227]\n"
"s_addk_i32 m0, 4096\n"
"v_cvt_pk_bf16_f32 v232, v244, v245\n"
"v_cvt_pk_bf16_f32 v233, v246, v247\n"
"v_cvt_pk_bf16_f32 v234, v248, v249\n"
"v_cvt_pk_bf16_f32 v235, v250, v251\n"
"v_mfma_f32_16x16x32_bf16 v[112:115], a[12:15], v[24:27], v[112:115]\n"
"s_add_u32 s56, s56, s62\n"
"v_cvt_pk_bf16_f32 v236, v210, v211\n"
"v_cvt_pk_bf16_f32 v237, v212, v213\n"
"v_cvt_pk_bf16_f32 v238, v214, v215\n"
"v_cvt_pk_bf16_f32 v239, v216, v217\n"
"v_mfma_f32_16x16x32_bf16 v[112:115], a[16:19], v[28:31], v[112:115]\n"
"s_waitcnt lgkmcnt(10)\n"
"v_cvt_pk_bf16_f32 v240, v224, v225\n"
"v_cvt_pk_bf16_f32 v241, v226, v227\n"
"v_mfma_f32_16x16x32_bf16 v[112:115], a[20:23], v[32:35], v[112:115]\n"
"s_nop 0\n"
"ds_read_b64_tr_b16 a[48:49], v201 offset:36864\n"
"ds_read_b64_tr_b16 a[50:51], v201 offset:37888\n"
"v_mfma_f32_16x16x32_bf16 v[112:115], a[24:27], v[36:39], v[112:115]\n"
"s_waitcnt lgkmcnt(8)\n"
"ds_read_b64_tr_b16 a[52:53], v201 offset:40960\n"
"ds_read_b64_tr_b16 a[54:55], v201 offset:41984\n"
"v_mfma_f32_16x16x32_bf16 v[112:115], a[28:31], v[40:43], v[112:115]\n"
"s_nop 0\n"
"ds_read_b64_tr_b16 a[56:57], v201 offset:45056\n"
"ds_read_b64_tr_b16 a[58:59], v201 offset:46080\n"
"v_mfma_f32_16x16x32_bf16 v[112:115], a[32:35], v[44:47], v[112:115]\n"
"s_nop 0\n"
"ds_read_b64_tr_b16 a[0:1], v200 offset:26624\n"
"ds_read_b64_tr_b16 a[2:3], v200 offset:27648\n"
"v_mfma_f32_16x16x32_bf16 v[116:119], a[12:15], v[80:83], v[116:119]\n"
"s_nop 0\n"
"buffer_load_dwordx4 v180, s[24:27], s56 offen lds\n"
"v_mfma_f32_16x16x32_bf16 v[116:119], a[16:19], v[84:87], v[116:119]\n"
"s_addk_i32 m0, 4096\n"
"s_add_u32 s56, s56, s62\n"
"ds_read_b64_tr_b16 a[4:5], v200 offset:30720\n"
"v_mfma_f32_16x16x32_bf16 v[116:119], a[20:23], v[88:91], v[116:119]\n"
"s_nop 0\n"
"ds_read_b64_tr_b16 a[6:7], v200 offset:31744\n"
"ds_read_b64_tr_b16 a[8:9], v200 offset:34816\n"
"v_mfma_f32_16x16x32_bf16 v[116:119], a[24:27], v[92:95], v[116:119]\n"
"s_nop 0\n"
"ds_read_b64_tr_b16 a[10:11], v200 offset:35840\n"
"ds_read_b64_tr_b16 a[12:13], v200 offset:38912\n"
"v_mfma_f32_16x16x32_bf16 v[116:119], a[28:31], v[96:99], v[116:119]\n"
"s_nop 0\n"
"buffer_load_dwordx4 v180, s[24:27], s56 offen lds\n"
"v_mfma_f32_16x16x32_bf16 v[116:119], a[32:35], v[100:103], v[116:119]\n"
"s_waitcnt lgkmcnt(15)\n"
"s_addk_i32 m0, 4096\n"
"s_add_u32 s56, s56, s62\n"
"v_mfma_f32_16x16x32_bf16 v[120:123], a[36:39], v[24:27], v[120:123]\n"
"s_nop 0\n"
"ds_read_b64_tr_b16 a[14:15], v200 offset:39936\n"
"ds_read_b64_tr_b16 a[16:17], v200 offset:43008\n"
"v_mfma_f32_16x16x32_bf16 v[120:123], a[40:43], v[28:31], v[120:123]\n"
"s_waitcnt lgkmcnt(13)\n"
"ds_read_b64_tr_b16 a[18:19], v200 offset:44032\n"
"ds_read_b64_tr_b16 a[20:21], v200 offset:47104\n"
"v_mfma_f32_16x16x32_bf16 v[120:123], a[44:47], v[32:35], v[120:123]\n"
"s_nop 0\n"
"buffer_load_dwordx4 v180, s[24:27], s56 offen lds\n"
"v_mfma_f32_16x16x32_bf16 v[120:123], a[48:51], v[36:39], v[120:123]\n"
"s_waitcnt lgkmcnt(11)\n"
"s_addk_i32 m0, 4096\n"
"s_add_u32 s56, s56, s62\n"
"v_mfma_f32_16x16x32_bf16 v[120:123], a[52:55], v[40:43], v[120:123]\n"
"ds_read_b64_tr_b16 a[22:23], v200 offset:48128\n"
"v_add_u32 v200, 49152, v200\n"
"v_mfma_f32_16x16x32_bf16 v[120:123], a[56:59], v[44:47], v[120:123]\n"
"s_nop 0\n"
"ds_read_b64_tr_b16 a[24:25], v201 offset:26624\n"
"ds_read_b64_tr_b16 a[26:27], v201 offset:27648\n"
"v_mfma_f32_16x16x32_bf16 v[124:127], a[36:39], v[80:83], v[124:127]\n"
"s_nop 0\n"
"ds_read_b64_tr_b16 a[28:29], v201 offset:30720\n"
"ds_read_b64_tr_b16 a[30:31], v201 offset:31744\n"
"v_mfma_f32_16x16x32_bf16 v[124:127], a[40:43], v[84:87], v[124:127]\n"
"s_nop 0\n"
"buffer_load_dwordx4 v180, s[24:27], s56 offen lds\n"
"v_mfma_f32_16x16x32_bf16 v[124:127], a[44:47], v[88:91], v[124:127]\n"
"s_addk_i32 m0, 4096\n"
"s_mov_b32 s56, 1769728\n"
"ds_read_b64_tr_b16 a[32:33], v201 offset:34816\n"
"v_mfma_f32_16x16x32_bf16 v[124:127], a[48:51], v[92:95], v[124:127]\n"
"s_nop 0\n"
"ds_read_b64_tr_b16 a[34:35], v201 offset:35840\n"
"ds_read_b64_tr_b16 a[36:37], v201 offset:38912\n"
"v_mfma_f32_16x16x32_bf16 v[124:127], a[52:55], v[96:99], v[124:127]\n"
"s_nop 0\n"
"ds_read_b64_tr_b16 a[38:39], v201 offset:39936\n"
"ds_read_b64_tr_b16 a[40:41], v201 offset:43008\n"
"v_mfma_f32_16x16x32_bf16 v[124:127], a[56:59], v[100:103], v[124:127]\n"
"s_waitcnt lgkmcnt(15)\n"
"buffer_store_dwordx4 v[232:235], v189, s[44:47], s58 offen sc1\n"
"v_mfma_f32_16x16x32_bf16 v[128:131], a[0:3], v[24:27], v[128:131]\n"
"s_nop 0\n"
"ds_read_b64_tr_b16 a[42:43], v201 offset:44032\n"
"ds_read_b64_tr_b16 a[44:45], v201 offset:47104\n"
"v_mfma_f32_16x16x32_bf16 v[128:131], a[4:7], v[28:31], v[128:131]\n"
"s_waitcnt lgkmcnt(15)\n"
"ds_read_b64_tr_b16 a[46:47], v201 offset:48128\n"
"v_add_u32 v201, 49152, v201\n"
"v_mfma_f32_16x16x32_bf16 v[128:131], a[8:11], v[32:35], v[128:131]\n"
"s_nop 0\n"
"buffer_load_dwordx4 v180, s[24:27], s56 offen lds\n"
"v_mfma_f32_16x16x32_bf16 v[128:131], a[12:15], v[36:39], v[128:131]\n"
"s_waitcnt lgkmcnt(12)\n"
"s_addk_i32 m0, 4096\n"
"s_add_u32 s56, s56, s62\n"
"v_mfma_f32_16x16x32_bf16 v[128:131], a[16:19], v[40:43], v[128:131]\n"
"v_mfma_f32_16x16x32_bf16 v[128:131], a[20:23], v[44:47], v[128:131]\n"
"v_mfma_f32_16x16x32_bf16 v[132:135], a[0:3], v[80:83], v[132:135]\n"
"s_nop 0\n"
"buffer_load_dwordx4 v180, s[24:27], s56 offen lds\n"
"v_mfma_f32_16x16x32_bf16 v[132:135], a[4:7], v[84:87], v[132:135]\n"
"s_addk_i32 m0, 4096\n"
"s_add_u32 s56, s56, s62\n"
"v_mfma_f32_16x16x32_bf16 v[132:135], a[8:11], v[88:91], v[132:135]\n"
"v_mfma_f32_16x16x32_bf16 v[132:135], a[12:15], v[92:95], v[132:135]\n"
"s_waitcnt vmcnt(3) lgkmcnt(0)\n"
"buffer_load_dwordx4 v180, s[24:27], s56 offen lds\n"
"v_mfma_f32_16x16x32_bf16 v[132:135], a[16:19], v[96:99], v[132:135]\n"
"s_barrier\n"
"ds_read_b64_tr_b16 a[48:49], v200 offset:0\n"
"buffer_store_dwordx4 v[236:239], v189, s[44:47], s58 offen offset:512 sc1\n"
"v_mfma_f32_16x16x32_bf16 v[132:135], a[20:23], v[100:103], v[132:135]\n"
"s_addk_i32 m0, 4096\n"
"ds_read_b64_tr_b16 a[50:51], v200 offset:1024\n"
"buffer_store_dwordx2 v[240:241], v190, s[44:47], s59 offen offset:3072 sc1\n"
"s_add_u32 s7, s77, 0\n"
"v_mfma_f32_16x16x32_bf16 v[232:235], a[204:207], v[204:207], 0\n"
"s_add_u32 s56, s56, s62\n"
"buffer_load_dwordx4 v180, s[24:27], s56 offen lds\n"
"v_mfma_f32_16x16x32_bf16 v[236:239], a[210:213], v[204:207], 0\n"
"s_addk_i32 m0, 4096\n"
"s_add_u32 s56, s56, s62\n"
"ds_read_b64_tr_b16 a[52:53], v200 offset:4096\n"
"v_mfma_f32_16x16x32_bf16 v[240:243], a[216:219], v[204:207], 0\n"
"s_nop 0\n"
"buffer_load_dwordx4 v180, s[24:27], s56 offen lds\n"
"v_mfma_f32_16x16x32_bf16 v[244:247], a[222:225], v[204:207], 0\n"
"s_addk_i32 m0, 4096\n"
"buffer_store_dwordx4 a[60:63], v188, s[44:47], s7 offen sc1\n"
"v_mfma_f32_16x16x32_bf16 v[248:251], a[228:231], v[204:207], 0\n"
"s_add_u32 s56, s56, s62\n"
"s_add_u32 s7, s77, 18432\n"
"ds_read_b64_tr_b16 a[54:55], v200 offset:5120\n"
"v_mfma_f32_16x16x16_bf16 v[232:235], a[208:209], v[208:209], v[232:235]\n"
"s_nop 0\n"
"ds_read_b64_tr_b16 a[56:57], v200 offset:8192\n"
"ds_read_b64_tr_b16 a[58:59], v200 offset:9216\n"
"v_mfma_f32_16x16x32_bf16 v[136:139], a[24:27], v[24:27], v[136:139]\n"
"s_nop 0\n"
"buffer_load_dwordx4 v180, s[24:27], s56 offen lds\n"
"v_mfma_f32_16x16x32_bf16 v[136:139], a[28:31], v[28:31], v[136:139]\n"
"s_addk_i32 m0, 4096\n"
"s_mov_b32 s56, 3539200\n"
"ds_read_b64_tr_b16 a[0:1], v200 offset:12288\n"
"v_mfma_f32_16x16x32_bf16 v[136:139], a[32:35], v[32:35], v[136:139]\n"
"s_nop 0\n"
"ds_read_b64_tr_b16 a[2:3], v200 offset:13312\n"
"ds_read_b64_tr_b16 a[4:5], v200 offset:16384\n"
"v_mfma_f32_16x16x32_bf16 v[136:139], a[36:39], v[36:39], v[136:139]\n"
"s_nop 0\n"
"ds_read_b64_tr_b16 a[6:7], v200 offset:17408\n"
"ds_read_b64_tr_b16 a[8:9], v200 offset:20480\n"
"v_mfma_f32_16x16x32_bf16 v[136:139], a[40:43], v[40:43], v[136:139]\n"
"s_nop 0\n"
"ds_read_b64_tr_b16 a[10:11], v200 offset:21504\n"
"ds_read_b64_tr_b16 a[12:13], v201 offset:0\n"
"v_mfma_f32_16x16x32_bf16 v[136:139], a[44:47], v[44:47], v[136:139]\n"
"s_nop 0\n"
"ds_read_b64_tr_b16 a[14:15], v201 offset:1024\n"
"ds_read_b64_tr_b16 a[16:17], v201 offset:4096\n"
"v_mfma_f32_16x16x16_bf16 v[236:239], a[214:215], v[208:209], v[236:239]\n"
"ds_read_b64_tr_b16 a[18:19], v201 offset:5120\n"
"v_mfma_f32_16x16x16_bf16 v[240:243], a[220:221], v[208:209], v[240:243]\n"
"v_mfma_f32_16x16x16_bf16 v[244:247], a[226:227], v[208:209], v[244:247]\n"
"v_mfma_f32_16x16x16_bf16 v[248:251], a[232:233], v[208:209], v[248:251]\n"
"v_mul_f32 v254, 0x3fb8aa3b, v185\n"
"v_max3_f32 v252, v232, v233, v234\n"
"v_mfma_f32_16x16x32_bf16 v[140:143], a[24:27], v[80:83], v[140:143]\n"
"ds_read_b64_tr_b16 a[20:21], v201 offset:8192\n"
"v_max3_f32 v252, v252, v235, v236\n"
"v_mfma_f32_16x16x32_bf16 v[140:143], a[28:31], v[84:87], v[140:143]\n"
"v_max3_f32 v252, v252, v237, v238\n"
"v_max3_f32 v252, v252, v239, v240\n"
"v_mfma_f32_16x16x32_bf16 v[140:143], a[32:35], v[88:91], v[140:143]\n"
"v_max3_f32 v252, v252, v241, v242\n"
"v_max3_f32 v252, v252, v243, v244\n"
"v_mfma_f32_16x16x32_bf16 v[140:143], a[36:39], v[92:95], v[140:143]\n"
"v_max3_f32 v252, v252, v245, v246\n"
"v_max3_f32 v252, v252, v247, v248\n"
"v_mfma_f32_16x16x32_bf16 v[140:143], a[40:43], v[96:99], v[140:143]\n"
"v_max_f32 v252, v252, v249\n"
"v_mul_f32_e64 v250, v254, -v252\n"
"v_mfma_f32_16x16x32_bf16 v[140:143], a[44:47], v[100:103], v[140:143]\n"
"s_waitcnt lgkmcnt(13)\n"
"v_pk_fma_f32 v[232:233], v[232:233], v[254:255], v[250:251] op_sel_hi:[1,0,0]\n"
"v_pk_fma_f32 v[234:235], v[234:235], v[254:255], v[250:251] op_sel_hi:[1,0,0]\n"
"v_pk_fma_f32 v[236:237], v[236:237], v[254:255], v[250:251] op_sel_hi:[1,0,0]\n"
"v_pk_fma_f32 v[238:239], v[238:239], v[254:255], v[250:251] op_sel_hi:[1,0,0]\n"
"v_pk_fma_f32 v[240:241], v[240:241], v[254:255], v[250:251] op_sel_hi:[1,0,0]\n"
"v_pk_fma_f32 v[242:243], v[242:243], v[254:255], v[250:251] op_sel_hi:[1,0,0]\n"
"v_pk_fma_f32 v[244:245], v[244:245], v[254:255], v[250:251] op_sel_hi:[1,0,0]\n"
"v_pk_fma_f32 v[246:247], v[246:247], v[254:255], v[250:251] op_sel_hi:[1,0,0]\n"
"v_pk_fma_f32 v[248:249], v[248:249], v[254:255], v[250:251] op_sel_hi:[1,0,0]\n"
"v_mfma_f32_16x16x32_bf16 v[112:115], a[48:51], v[48:51], v[112:115]\n"
"s_nop 0\n"
"v_exp_f32 v232, v232\n"
"v_mfma_f32_16x16x32_bf16 v[112:115], a[52:55], v[52:55], v[112:115]\n"
"s_waitcnt lgkmcnt(9)\n"
"v_exp_f32 v233, v233\n"
"v_mfma_f32_16x16x32_bf16 v[112:115], a[56:59], a[172:175], v[112:115]\n"
"s_nop 0\n"
"buffer_load_dwordx4 v180, s[24:27], s56 offen lds\n"
"v_mfma_f32_16x16x32_bf16 v[112:115], a[0:3], a[176:179], v[112:115]\n"
"s_waitcnt lgkmcnt(5)\n"
"ds_read_b64_tr_b16 a[22:23], v201 offset:9216\n"
"ds_read_b64_tr_b16 a[24:25], v201 offset:12288\n"
"v_mfma_f32_16x16x32_bf16 v[112:115], a[4:7], a[180:183], v[112:115]\n"
"s_addk_i32 m0, 4096\n"
"v_exp_f32 v234, v234\n"
"v_mfma_f32_16x16x32_bf16 v[112:115], a[8:11], a[184:187], v[112:115]\n"
"s_add_u32 s56, s56, s62\n"
"ds_read_b64_tr_b16 a[26:27], v201 offset:13312\n"
"ds_read_b64_tr_b16 a[28:29], v201 offset:16384\n"
"v_mfma_f32_16x16x32_bf16 v[116:119], a[48:51], v[104:107], v[116:119]\n"
"s_nop 0\n"
"buffer_load_dwordx4 v180, s[24:27], s56 offen lds\n"
"v_mfma_f32_16x16x32_bf16 v[116:119], a[52:55], v[108:111], v[116:119]\n"
"s_addk_i32 m0, 4096\n"
"ds_read_b64_tr_b16 a[30:31], v201 offset:17408\n"
"v_max_f32 v112, 0, v112\n"
"v_mfma_f32_16x16x32_bf16 v[116:119], a[56:59], a[188:191], v[116:119]\n"
"s_add_u32 s56, s56, s62\n"
"ds_read_b64_tr_b16 a[32:33], v201 offset:20480\n"
"v_max_f32 v113, 0, v113\n"
"v_mfma_f32_16x16x32_bf16 v[116:119], a[0:3], a[192:195], v[116:119]\n"
"ds_read_b64_tr_b16 a[34:35], v201 offset:21504\n"
"v_max_f32 v114, 0, v114\n"
"v_mfma_f32_16x16x32_bf16 v[116:119], a[4:7], a[196:199], v[116:119]\n"
"s_nop 0\n"
"ds_read_b64_tr_b16 a[36:37], v200 offset:2048\n"
"ds_read_b64_tr_b16 a[38:39], v200 offset:3072\n"
"v_mfma_f32_16x16x32_bf16 v[116:119], a[8:11], a[200:203], v[116:119]\n"
"s_waitcnt lgkmcnt(10)\n"
"buffer_load_dwordx4 v180, s[24:27], s56 offen lds\n"
"v_mfma_f32_16x16x32_bf16 v[120:123], a[12:15], v[48:51], v[120:123]\n"
"s_addk_i32 m0, 4096\n"
"ds_read_b64_tr_b16 a[40:41], v200 offset:6144\n"
"v_max_f32 v115, 0, v115\n"
"v_mfma_f32_16x16x32_bf16 v[120:123], a[16:19], v[52:55], v[120:123]\n"
"s_waitcnt lgkmcnt(7)\n"
"ds_read_b64_tr_b16 a[42:43], v200 offset:7168\n"
"v_max_f32 v116, 0, v116\n"
"v_mfma_f32_16x16x32_bf16 v[120:123], a[20:23], a[172:175], v[120:123]\n"
"s_add_u32 s56, s56, s62\n"
"ds_read_b64_tr_b16 a[44:45], v200 offset:10240\n"
"v_max_f32 v117, 0, v117\n"
"v_mfma_f32_16x16x32_bf16 v[120:123], a[24:27], a[176:179], v[120:123]\n"
"s_waitcnt lgkmcnt(5)\n"
"ds_read_b64_tr_b16 a[46:47], v200 offset:11264\n"
"ds_read_b64_tr_b16 a[48:49], v200 offset:14336\n"
"v_mfma_f32_16x16x32_bf16 v[120:123], a[28:31], a[180:183], v[120:123]\n"
"s_nop 0\n"
"buffer_load_dwordx4 v180, s[24:27], s56 offen lds\n"
"v_mfma_f32_16x16x32_bf16 v[120:123], a[32:35], a[184:187], v[120:123]\n"
"s_addk_i32 m0, 4096\n"
"ds_read_b64_tr_b16 a[50:51], v200 offset:15360\n"
"v_max_f32 v118, 0, v118\n"
"v_mfma_f32_16x16x32_bf16 v[124:127], a[12:15], v[104:107], v[124:127]\n"
"s_add_u32 s56, s56, s62\n"
"ds_read_b64_tr_b16 a[52:53], v200 offset:18432\n"
"v_max_f32 v119, 0, v119\n"
"v_mfma_f32_16x16x32_bf16 v[124:127], a[16:19], v[108:111], v[124:127]\n"
"ds_read_b64_tr_b16 a[54:55], v200 offset:19456\n"
"v_max_f32 v120, 0, v120\n"
"v_mfma_f32_16x16x32_bf16 v[124:127], a[20:23], a[188:191], v[124:127]\n"
"ds_read_b64_tr_b16 a[56:57], v200 offset:22528\n"
"v_add_u32 v176, 4294942720, v176\n"
"v_mfma_f32_16x16x32_bf16 v[124:127], a[24:27], a[192:195], v[124:127]\n"
"ds_read_b64_tr_b16 a[58:59], v200 offset:23552\n"
"v_max_f32 v121, 0, v121\n"
"v_mfma_f32_16x16x32_bf16 v[124:127], a[28:31], a[196:199], v[124:127]\n"
"ds_read_b64_tr_b16 a[0:1], v201 offset:2048\n"
"v_max_f32 v122, 0, v122\n"
"v_mfma_f32_16x16x32_bf16 v[124:127], a[32:35], a[200:203], v[124:127]\n"
"s_waitcnt lgkmcnt(9)\n"
"ds_read_b64_tr_b16 a[2:3], v201 offset:3072\n"
"v_max_f32 v123, 0, v123\n"
"v_mfma_f32_16x16x32_bf16 v[128:131], a[36:39], v[48:51], v[128:131]\n"
"s_nop 0\n"
"ds_read_b64_tr_b16 a[4:5], v201 offset:6144\n"
"ds_read_b64_tr_b16 a[6:7], v201 offset:7168\n"
"v_mfma_f32_16x16x32_bf16 v[128:131], a[40:43], v[52:55], v[128:131]\n"
"s_waitcnt lgkmcnt(8)\n"
"v_max_f32 v124, 0, v124\n"
"v_max_f32 v125, 0, v125\n"
"v_mfma_f32_16x16x32_bf16 v[128:131], a[44:47], a[172:175], v[128:131]\n"
"s_nop 0\n"
"ds_read_b64_tr_b16 a[8:9], v201 offset:10240\n"
"ds_read_b64_tr_b16 a[10:11], v201 offset:11264\n"
"v_mfma_f32_16x16x32_bf16 v[128:131], a[48:51], a[176:179], v[128:131]\n"
"s_waitcnt lgkmcnt(6)\n"
"buffer_load_dwordx4 v180, s[24:27], s56 offen lds\n"
"v_mfma_f32_16x16x32_bf16 v[128:131], a[52:55], a[180:183], v[128:131]\n"
"s_addk_i32 m0, 4096\n"
"ds_read_b64_tr_b16 a[12:13], v201 offset:14336\n"
"v_max_f32 v126, 0, v126\n"
"v_mfma_f32_16x16x32_bf16 v[128:131], a[56:59], a[184:187], v[128:131]\n"
"s_add_u32 s56, s56, s62\n"
"ds_read_b64_tr_b16 a[14:15], v201 offset:15360\n"
"v_max_f32 v127, 0, v127\n"
"v_mfma_f32_16x16x32_bf16 v[132:135], a[36:39], v[104:107], v[132:135]\n"
"s_nop 0\n"
"ds_read_b64_tr_b16 a[16:17], v201 offset:18432\n"
"ds_read_b64_tr_b16 a[18:19], v201 offset:19456\n"
"v_mfma_f32_16x16x32_bf16 v[132:135], a[40:43], v[108:111], v[132:135]\n"
"s_nop 0\n"
"buffer_load_dwordx4 v180, s[24:27], s56 offen lds\n"
"v_mfma_f32_16x16x32_bf16 v[132:135], a[44:47], a[188:191], v[132:135]\n"
"s_add_u32 m0, s55, 0\n"
"ds_read_b64_tr_b16 a[20:21], v201 offset:22528\n"
"v_add_u32 v177, 4294942720, v177\n"
"v_mfma_f32_16x16x32_bf16 v[132:135], a[48:51], a[192:195], v[132:135]\n"
"s_mov_b32 s56, 256\n"
"ds_read_b64_tr_b16 a[22:23], v201 offset:23552\n"
"v_mul_f32 v128, v112, v128\n"
"v_mfma_f32_16x16x32_bf16 v[132:135], a[52:55], a[196:199], v[132:135]\n"
"ds_read_b64_tr_b16 a[24:25], v176 offset:0\n"
"v_mul_f32 v129, v113, v129\n"
"v_mfma_f32_16x16x32_bf16 v[132:135], a[56:59], a[200:203], v[132:135]\n"
"s_waitcnt lgkmcnt(9)\n"
"ds_read_b64_tr_b16 a[26:27], v176 offset:2048\n"
"ds_read_b64_tr_b16 a[28:29], v176 offset:4096\n"
"v_mfma_f32_16x16x32_bf16 v[136:139], a[0:3], v[48:51], v[136:139]\n"
"s_nop 0\n"
"buffer_store_dwordx4 a[102:105], v188, s[44:47], s7 offen sc1\n"
"v_mfma_f32_16x16x32_bf16 v[136:139], a[4:7], v[52:55], v[136:139]\n"
"s_waitcnt lgkmcnt(7)\n"
"ds_read_b64_tr_b16 a[30:31], v176 offset:6144\n"
"s_waitcnt vmcnt(11)\n"
"v_mfma_f32_16x16x32_bf16 v[136:139], a[8:11], a[172:175], v[136:139]\n"
"s_add_u32 s7, s77, 1024\n"
"ds_read_b64_tr_b16 a[32:33], v176 offset:8192\n"
"v_mul_f32 v130, v114, v130\n"
"v_mfma_f32_16x16x32_bf16 v[136:139], a[12:15], a[176:179], v[136:139]\n"
"s_waitcnt lgkmcnt(5)\n"
"ds_read_b64_tr_b16 a[34:35], v176 offset:10240\n"
"v_mul_f32 v131, v115, v131\n"
"v_mfma_f32_16x16x32_bf16 v[136:139], a[16:19], a[180:183], v[136:139]\n"
"ds_read_b64_tr_b16 a[36:37], v176 offset:12288\n"
"v_mul_f32 v132, v116, v132\n"
"v_mfma_f32_16x16x32_bf16 v[136:139], a[20:23], a[184:187], v[136:139]\n"
"ds_read_b64_tr_b16 a[38:39], v176 offset:14336\n"
"v_mul_f32 v133, v117, v133\n"
"v_mfma_f32_16x16x32_bf16 v[140:143], a[0:3], v[104:107], v[140:143]\n"
"ds_read_b64_tr_b16 a[40:41], v176 offset:16384\n"
"v_mul_f32 v134, v118, v134\n"
"v_mfma_f32_16x16x32_bf16 v[140:143], a[4:7], v[108:111], v[140:143]\n"
"ds_read_b64_tr_b16 a[42:43], v176 offset:18432\n"
"v_mul_f32 v135, v119, v135\n"
"v_mfma_f32_16x16x32_bf16 v[140:143], a[8:11], a[188:191], v[140:143]\n"
"ds_read_b64_tr_b16 a[44:45], v176 offset:20480\n"
"v_add_u32 v178, 4294942720, v178\n"
"v_mfma_f32_16x16x32_bf16 v[140:143], a[12:15], a[192:195], v[140:143]\n"
"ds_read_b64_tr_b16 a[46:47], v176 offset:22528\n"
"v_mul_f32 v136, v120, v136\n"
"v_mfma_f32_16x16x32_bf16 v[140:143], a[16:19], a[196:199], v[140:143]\n"
"ds_read_b64_tr_b16 a[48:49], v177 offset:0\n"
"v_mul_f32 v137, v121, v137\n"
"v_mfma_f32_16x16x32_bf16 v[140:143], a[20:23], a[200:203], v[140:143]\n"
"s_waitcnt lgkmcnt(9)\n"
"ds_read_b64_tr_b16 a[50:51], v177 offset:2048\n"
"ds_read_b64_tr_b16 a[52:53], v177 offset:4096\n"
"v_mfma_f32_16x16x32_bf16 v[112:115], a[24:27], v[0:3], 0\n"
"s_nop 0\n"
"v_add_u32_e64 v199, s65, 1\n"
"buffer_store_dwordx2 v[198:199], v197, s[44:47], s61 offen offset:0 sc1\n"
"v_mfma_f32_16x16x32_bf16 v[112:115], a[28:31], v[4:7], v[112:115]\n"
"s_waitcnt lgkmcnt(7)\n"
"ds_read_b64_tr_b16 a[54:55], v177 offset:6144\n"
"s_lshl_b32 s60, s54, 28\n"
"v_mfma_f32_16x16x32_bf16 v[112:115], a[32:35], v[8:11], v[112:115]\n"
"ds_read_b64_tr_b16 a[56:57], v177 offset:8192\n"
"v_or_b32 v190, s60, v197\n"
"v_mfma_f32_16x16x32_bf16 v[112:115], a[36:39], v[12:15], v[112:115]\n"
"s_waitcnt lgkmcnt(5)\n"
"ds_read_b64_tr_b16 a[58:59], v177 offset:10240\n"
"ds_read_b64_tr_b16 a[0:1], v177 offset:12288\n"
"v_mfma_f32_16x16x32_bf16 v[112:115], a[40:43], v[16:19], v[112:115]\n"
"s_nop 0\n"
"buffer_store_dwordx4 a[64:67], v188, s[44:47], s7 offen sc1\n"
"v_mfma_f32_16x16x32_bf16 v[112:115], a[44:47], v[20:23], v[112:115]\n"
"s_add_u32 s7, s77, 19456\n"
"ds_read_b64_tr_b16 a[2:3], v177 offset:14336\n"
"v_and_b32 v194, 15, v187\n"
"v_mfma_f32_16x16x32_bf16 v[116:119], a[24:27], v[56:59], 0\n"
"ds_read_b64_tr_b16 a[4:5], v177 offset:16384\n"
"v_mul_u32_u24 v191, 128, v194\n"
"v_mfma_f32_16x16x32_bf16 v[116:119], a[28:31], v[60:63], v[116:119]\n"
"s_lshr_b32 s59, s52, 2\n"
"ds_read_b64_tr_b16 a[6:7], v177 offset:18432\n"
"ds_read_b64_tr_b16 a[8:9], v177 offset:20480\n"
"v_mfma_f32_16x16x32_bf16 v[116:119], a[32:35], v[64:67], v[116:119]\n"
"s_sub_u32 s60, s52, 12\n"
"buffer_store_dwordx4 a[106:109], v188, s[44:47], s7 offen sc1\n"
"v_mfma_f32_16x16x32_bf16 v[116:119], a[36:39], v[68:71], v[116:119]\n"
"s_min_u32 s59, s59, s60\n"
"ds_read_b64_tr_b16 a[10:11], v177 offset:22528\n"
"v_add_u32 v179, 4294942720, v179\n"
"v_mfma_f32_16x16x32_bf16 v[116:119], a[40:43], v[72:75], v[116:119]\n"
"s_lshl_b32 s59, s59, 4\n"
"ds_read_b64_tr_b16 a[12:13], v178 offset:0\n"
"s_add_u32 s59, s59, s53\n"
"v_mfma_f32_16x16x32_bf16 v[116:119], a[44:47], v[76:79], v[116:119]\n"
"s_waitcnt lgkmcnt(9)\n"
"ds_read_b64_tr_b16 a[14:15], v178 offset:2048\n"
"s_mul_i32 s59, s59, 2048\n"
"v_mul_f32 v138, v122, v138\n"
"v_mul_f32 v139, v123, v139\n"
"v_mfma_f32_16x16x32_bf16 v[120:123], a[48:51], v[0:3], 0\n"
"s_add_u32 s59, s59, 4096\n"
"ds_read_b64_tr_b16 a[16:17], v178 offset:4096\n"
"s_add_u32 s67, s65, 1\n"
"v_mfma_f32_16x16x32_bf16 v[120:123], a[52:55], v[4:7], v[120:123]\n"
"s_waitcnt lgkmcnt(7)\n"
"ds_read_b64_tr_b16 a[18:19], v178 offset:6144\n"
"s_mov_b32 s66, s64\n"
"v_mfma_f32_16x16x32_bf16 v[120:123], a[56:59], v[8:11], v[120:123]\n"
"s_lshl_b32 s60, s52, 11\n"
"ds_read_b64_tr_b16 a[20:21], v178 offset:8192\n"
"s_add_u32 s60, s60, 167936\n"
"v_mfma_f32_16x16x32_bf16 v[120:123], a[0:3], v[12:15], v[120:123]\n"
"s_waitcnt lgkmcnt(5)\n"
"ds_read_b64_tr_b16 a[22:23], v178 offset:10240\n"
"s_sub_u32 s60, s60, s59\n"
"v_mfma_f32_16x16x32_bf16 v[120:123], a[4:7], v[16:19], v[120:123]\n"
"s_add_u32 s7, s77, 2048\n"
"ds_read_b64_tr_b16 a[24:25], v178 offset:12288\n"
"ds_read_b64_tr_b16 a[26:27], v178 offset:14336\n"
"v_mfma_f32_16x16x32_bf16 v[120:123], a[8:11], v[20:23], v[120:123]\n"
"s_nop 0\n"
"buffer_store_dwordx4 a[68:71], v188, s[44:47], s7 offen sc1\n"
"v_mul_f32 v140, v124, v140\n"
"v_mul_f32 v141, v125, v141\n"
"v_mul_f32 v142, v126, v142\n"
"v_mul_f32 v143, v127, v143\n"
"v_mfma_f32_16x16x32_bf16 v[124:127], a[48:51], v[56:59], 0\n"
"s_add_u32 s7, s77, 20480\n"
"ds_read_b64_tr_b16 a[28:29], v178 offset:16384\n"
"v_bfe_u32 v189, v187, 4, 1\n"
"v_mfma_f32_16x16x32_bf16 v[124:127], a[52:55], v[60:63], v[124:127]\n"
"ds_read_b64_tr_b16 a[30:31], v178 offset:18432\n"
"v_mad_u32_u24 v189, v189, s60, v191\n"
"v_mfma_f32_16x16x32_bf16 v[124:127], a[56:59], v[64:67], v[124:127]\n"
"107:\n"
"s_cbranch_execz 111f\n"
"s_nop 0\n"
"ds_read_b64_tr_b16 a[32:33], v178 offset:20480\n"
"ds_read_b64_tr_b16 a[34:35], v178 offset:22528\n"
"v_mfma_f32_16x16x32_bf16 v[124:127], a[0:3], v[68:71], v[124:127]\n"
"s_nop 0\n"
"buffer_store_dwordx4 a[110:113], v188, s[44:47], s7 offen sc1\n"
"v_mfma_f32_16x16x32_bf16 v[124:127], a[4:7], v[72:75], v[124:127]\n"
"s_add_u32 s7, s77, 3072\n"
"ds_read_b64_tr_b16 a[36:37], v179 offset:0\n"
"v_cvt_pk_bf16_f32 v128, v128, v129\n"
"v_mfma_f32_16x16x32_bf16 v[124:127], a[8:11], v[76:79], v[124:127]\n"
"s_waitcnt lgkmcnt(9)\n"
"ds_read_b64_tr_b16 a[38:39], v179 offset:2048\n"
"v_cvt_pk_bf16_f32 v129, v130, v131\n"
"v_accvgpr_write_b32 a92, v128\n"
"v_accvgpr_write_b32 a93, v129\n"
"v_mfma_f32_16x16x32_bf16 v[128:131], a[12:15], v[0:3], 0\n"
"s_nop 0\n"
"ds_read_b64_tr_b16 a[40:41], v179 offset:4096\n"
"ds_read_b64_tr_b16 a[42:43], v179 offset:6144\n"
"v_mfma_f32_16x16x32_bf16 v[128:131], a[16:19], v[4:7], v[128:131]\n"
"s_waitcnt lgkmcnt(8)\n"
"ds_read_b64_tr_b16 a[44:45], v179 offset:8192\n"
"ds_read_b64_tr_b16 a[46:47], v179 offset:10240\n"
"v_mfma_f32_16x16x32_bf16 v[128:131], a[20:23], v[8:11], v[128:131]\n"
"s_nop 0\n"
"ds_read_b64_tr_b16 a[48:49], v179 offset:12288\n"
"ds_read_b64_tr_b16 a[50:51], v179 offset:14336\n"
"v_mfma_f32_16x16x32_bf16 v[128:131], a[24:27], v[12:15], v[128:131]\n"
"s_waitcnt lgkmcnt(8)\n"
"ds_read_b64_tr_b16 a[52:53], v179 offset:16384\n"
"ds_read_b64_tr_b16 a[54:55], v179 offset:18432\n"
"v_mfma_f32_16x16x32_bf16 v[128:131], a[28:31], v[16:19], v[128:131]\n"
"s_nop 0\n"
"ds_read_b64_tr_b16 a[56:57], v179 offset:20480\n"
"ds_read_b64_tr_b16 a[58:59], v179 offset:22528\n"
"v_mfma_f32_16x16x32_bf16 v[128:131], a[32:35], v[20:23], v[128:131]\n"
"s_nop 0\n"
"buffer_load_dwordx4 v180, s[28:31], s56 offen lds\n"
"v_cvt_pk_bf16_f32 v132, v132, v133\n"
"v_cvt_pk_bf16_f32 v133, v134, v135\n"
"v_accvgpr_write_b32 a134, v132\n"
"v_accvgpr_write_b32 a135, v133\n"
"v_mfma_f32_16x16x32_bf16 v[132:135], a[12:15], v[56:59], 0\n"
"s_addk_i32 m0, 4096\n"
"buffer_store_dwordx4 a[72:75], v188, s[44:47], s7 offen sc1\n"
"v_mfma_f32_16x16x32_bf16 v[132:135], a[16:19], v[60:63], v[132:135]\n"
"s_add_u32 s56, s56, s62\n"
"buffer_load_dwordx4 v180, s[28:31], s56 offen lds\n"
"v_mfma_f32_16x16x32_bf16 v[132:135], a[20:23], v[64:67], v[132:135]\n"
"s_addk_i32 m0, 4096\n"
"v_cvt_pk_bf16_f32 v136, v136, v137\n"
"v_cvt_pk_bf16_f32 v137, v138, v139\n"
"v_mfma_f32_16x16x32_bf16 v[132:135], a[24:27], v[68:71], v[132:135]\n"
"s_waitcnt vmcnt(9) lgkmcnt(0)\n"
"v_accvgpr_write_b32 a94, v136\n"
"v_accvgpr_write_b32 a95, v137\n"
"v_mfma_f32_16x16x32_bf16 v[132:135], a[28:31], v[72:75], v[132:135]\n"
"s_barrier\n"
"ds_read_b64_tr_b16 a[0:1], v176 offset:24576\n"
"v_cvt_pk_bf16_f32 v140, v140, v141\n"
"v_mfma_f32_16x16x32_bf16 v[132:135], a[32:35], v[76:79], v[132:135]\n"
"s_add_u32 s56, s56, s62\n"
"ds_read_b64_tr_b16 a[2:3], v176 offset:26624\n"
"ds_read_b64_tr_b16 a[4:5], v176 offset:28672\n"
"v_mfma_f32_16x16x32_bf16 v[136:139], a[36:39], v[0:3], 0\n"
"s_add_u32 s7, s77, 21504\n"
"buffer_load_dwordx4 v180, s[28:31], s56 offen lds\n"
"v_mfma_f32_16x16x32_bf16 v[136:139], a[40:43], v[4:7], v[136:139]\n"
"s_addk_i32 m0, 4096\n"
"ds_read_b64_tr_b16 a[6:7], v176 offset:30720\n"
"v_cvt_pk_bf16_f32 v141, v142, v143\n"
"v_mfma_f32_16x16x32_bf16 v[136:139], a[44:47], v[8:11], v[136:139]\n"
"s_add_u32 s56, s56, s62\n"
"ds_read_b64_tr_b16 a[8:9], v176 offset:32768\n"
"v_accvgpr_write_b32 a136, v140\n"
"v_mfma_f32_16x16x32_bf16 v[136:139], a[48:51], v[12:15], v[136:139]\n"
"s_nop 0\n"
"ds_read_b64_tr_b16 a[10:11], v176 offset:34816\n"
"ds_read_b64_tr_b16 a[12:13], v176 offset:36864\n"
"v_mfma_f32_16x16x32_bf16 v[136:139], a[52:55], v[16:19], v[136:139]\n"
"s_nop 0\n"
"buffer_load_dwordx4 v180, s[28:31], s56 offen lds\n"
"v_mfma_f32_16x16x32_bf16 v[136:139], a[56:59], v[20:23], v[136:139]\n"
"s_addk_i32 m0, 4096\n"
"ds_read_b64_tr_b16 a[14:15], v176 offset:38912\n"
"v_accvgpr_write_b32 a137, v141\n"
"v_mfma_f32_16x16x32_bf16 v[140:143], a[36:39], v[56:59], 0\n"
"s_add_u32 s56, s56, s62\n"
"ds_read_b64_tr_b16 a[16:17], v176 offset:40960\n"
"ds_read_b64_tr_b16 a[18:19], v176 offset:43008\n"
"v_mfma_f32_16x16x32_bf16 v[140:143], a[40:43], v[60:63], v[140:143]\n"
"s_nop 0\n"
"v_exp_f32 v235, v235\n"
"v_mfma_f32_16x16x32_bf16 v[140:143], a[44:47], v[64:67], v[140:143]\n"
"s_nop 0\n"
"ds_read_b64_tr_b16 a[20:21], v176 offset:45056\n"
"ds_read_b64_tr_b16 a[22:23], v176 offset:47104\n"
"v_mfma_f32_16x16x32_bf16 v[140:143], a[48:51], v[68:71], v[140:143]\n"
"s_nop 0\n"
"v_exp_f32 v236, v236\n"
"v_mfma_f32_16x16x32_bf16 v[140:143], a[52:55], v[72:75], v[140:143]\n"
"s_nop 0\n"
"ds_read_b64_tr_b16 a[24:25], v177 offset:24576\n"
"ds_read_b64_tr_b16 a[26:27], v177 offset:26624\n"
"v_mfma_f32_16x16x32_bf16 v[140:143], a[56:59], v[76:79], v[140:143]\n"
"s_waitcnt lgkmcnt(10)\n"
"v_exp_f32 v237, v237\n"
"v_mfma_f32_16x16x32_bf16 v[112:115], a[0:3], v[24:27], v[112:115]\n"
"s_nop 0\n"
"ds_read_b64_tr_b16 a[28:29], v177 offset:28672\n"
"ds_read_b64_tr_b16 a[30:31], v177 offset:30720\n"
"v_mfma_f32_16x16x32_bf16 v[112:115], a[4:7], v[28:31], v[112:115]\n"
"s_waitcnt lgkmcnt(8)\n"
"buffer_load_dwordx4 v180, s[28:31], s56 offen lds\n"
"v_mfma_f32_16x16x32_bf16 v[112:115], a[8:11], v[32:35], v[112:115]\n"
"s_addk_i32 m0, 4096\n"
"ds_read_b64_tr_b16 a[32:33], v177 offset:32768\n"
"ds_read_b64_tr_b16 a[34:35], v177 offset:34816\n"
"v_mfma_f32_16x16x32_bf16 v[112:115], a[12:15], v[36:39], v[112:115]\n"
"s_waitcnt lgkmcnt(6)\n"
"v_exp_f32 v238, v238\n"
"v_mfma_f32_16x16x32_bf16 v[112:115], a[16:19], v[40:43], v[112:115]\n"
"s_add_u32 s56, s56, s62\n"
"ds_read_b64_tr_b16 a[36:37], v177 offset:36864\n"
"ds_read_b64_tr_b16 a[38:39], v177 offset:38912\n"
"v_mfma_f32_16x16x32_bf16 v[112:115], a[20:23], v[44:47], v[112:115]\n"
"s_nop 0\n"
"v_exp_f32 v239, v239\n"
"v_mfma_f32_16x16x32_bf16 v[116:119], a[0:3], v[80:83], v[116:119]\n"
"s_nop 0\n"
"ds_read_b64_tr_b16 a[40:41], v177 offset:40960\n"
"ds_read_b64_tr_b16 a[42:43], v177 offset:43008\n"
"v_mfma_f32_16x16x32_bf16 v[116:119], a[4:7], v[84:87], v[116:119]\n"
"s_nop 0\n"
"buffer_load_dwordx4 v180, s[28:31], s56 offen lds\n"
"v_mfma_f32_16x16x32_bf16 v[116:119], a[8:11], v[88:91], v[116:119]\n"
"s_addk_i32 m0, 4096\n"
"ds_read_b64_tr_b16 a[44:45], v177 offset:45056\n"
"ds_read_b64_tr_b16 a[46:47], v177 offset:47104\n"
"v_mfma_f32_16x16x32_bf16 v[116:119], a[12:15], v[92:95], v[116:119]\n"
"s_mov_b32 s56, 1769728\n"
"v_exp_f32 v240, v240\n"
"v_mfma_f32_16x16x32_bf16 v[116:119], a[16:19], v[96:99], v[116:119]\n"
"s_nop 0\n"
"ds_read_b64_tr_b16 a[48:49], v178 offset:24576\n"
"ds_read_b64_tr_b16 a[50:51], v178 offset:26624\n"
"v_mfma_f32_16x16x32_bf16 v[116:119], a[20:23], v[100:103], v[116:119]\n"
"s_waitcnt lgkmcnt(10)\n"
"buffer_load_dwordx4 v180, s[28:31], s56 offen lds\n"
"v_mfma_f32_16x16x32_bf16 v[120:123], a[24:27], v[24:27], v[120:123]\n"
"s_addk_i32 m0, 4096\n"
"ds_read_b64_tr_b16 a[52:53], v178 offset:28672\n"
"ds_read_b64_tr_b16 a[54:55], v178 offset:30720\n"
"v_mfma_f32_16x16x32_bf16 v[120:123], a[28:31], v[28:31], v[120:123]\n"
"s_waitcnt lgkmcnt(8)\n"
"v_exp_f32 v241, v241\n"
"v_mfma_f32_16x16x32_bf16 v[120:123], a[32:35], v[32:35], v[120:123]\n"
"s_add_u32 s56, s56, s62\n"
"ds_read_b64_tr_b16 a[56:57], v178 offset:32768\n"
"ds_read_b64_tr_b16 a[58:59], v178 offset:34816\n"
"v_mfma_f32_16x16x32_bf16 v[120:123], a[36:39], v[36:39], v[120:123]\n"
"s_waitcnt lgkmcnt(6)\n"
"buffer_load_dwordx4 v180, s[28:31], s56 offen lds\n"
"v_mfma_f32_16x16x32_bf16 v[120:123], a[40:43], v[40:43], v[120:123]\n"
"s_addk_i32 m0, 4096\n"
"ds_read_b64_tr_b16 a[0:1], v178 offset:36864\n"
"ds_read_b64_tr_b16 a[2:3], v178 offset:38912\n"
"v_mfma_f32_16x16x32_bf16 v[120:123], a[44:47], v[44:47], v[120:123]\n"
"s_add_u32 s56, s56, s62\n"
"v_exp_f32 v242, v242\n"
"v_mfma_f32_16x16x32_bf16 v[124:127], a[24:27], v[80:83], v[124:127]\n"
"s_nop 0\n"
"ds_read_b64_tr_b16 a[4:5], v178 offset:40960\n"
"ds_read_b64_tr_b16 a[6:7], v178 offset:43008\n"
"v_mfma_f32_16x16x32_bf16 v[124:127], a[28:31], v[84:87], v[124:127]\n"
"s_nop 0\n"
"buffer_load_dwordx4 v180, s[28:31], s56 offen lds\n"
"v_mfma_f32_16x16x32_bf16 v[124:127], a[32:35], v[88:91], v[124:127]\n"
"s_addk_i32 m0, 4096\n"
"ds_read_b64_tr_b16 a[8:9], v178 offset:45056\n"
"ds_read_b64_tr_b16 a[10:11], v178 offset:47104\n"
"v_mfma_f32_16x16x32_bf16 v[124:127], a[36:39], v[92:95], v[124:127]\n"
"s_add_u32 s56, s56, s62\n"
"v_exp_f32 v243, v243\n"
"v_mfma_f32_16x16x32_bf16 v[124:127], a[40:43], v[96:99], v[124:127]\n"
"s_nop 0\n"
"ds_read_b64_tr_b16 a[12:13], v179 offset:24576\n"
"ds_read_b64_tr_b16 a[14:15], v179 offset:26624\n"
"v_mfma_f32_16x16x32_bf16 v[124:127], a[44:47], v[100:103], v[124:127]\n"
"s_waitcnt lgkmcnt(10)\n"
"v_exp_f32 v244, v244\n"
"v_mfma_f32_16x16x32_bf16 v[128:131], a[48:51], v[24:27], v[128:131]\n"
"s_nop 0\n"
"ds_read_b64_tr_b16 a[16:17], v179 offset:28672\n"
"ds_read_b64_tr_b16 a[18:19], v179 offset:30720\n"
"v_mfma_f32_16x16x32_bf16 v[128:131], a[52:55], v[28:31], v[128:131]\n"
"s_waitcnt lgkmcnt(8)\n"
"v_exp_f32 v245, v245\n"
"v_mfma_f32_16x16x32_bf16 v[128:131], a[56:59], v[32:35], v[128:131]\n"
"s_nop 0\n"
"ds_read_b64_tr_b16 a[20:21], v179 offset:32768\n"
"ds_read_b64_tr_b16 a[22:23], v179 offset:34816\n"
"v_mfma_f32_16x16x32_bf16 v[128:131], a[0:3], v[36:39], v[128:131]\n"
"s_waitcnt lgkmcnt(6)\n"
"v_exp_f32 v246, v246\n"
"v_mfma_f32_16x16x32_bf16 v[128:131], a[4:7], v[40:43], v[128:131]\n"
"s_nop 0\n"
"ds_read_b64_tr_b16 a[24:25], v179 offset:36864\n"
"ds_read_b64_tr_b16 a[26:27], v179 offset:38912\n"
"v_mfma_f32_16x16x32_bf16 v[128:131], a[8:11], v[44:47], v[128:131]\n"
"s_nop 0\n"
"buffer_load_dwordx4 v180, s[28:31], s56 offen lds\n"
"v_mfma_f32_16x16x32_bf16 v[132:135], a[48:51], v[80:83], v[132:135]\n"
"s_addk_i32 m0, 4096\n"
"ds_read_b64_tr_b16 a[28:29], v179 offset:40960\n"
"ds_read_b64_tr_b16 a[30:31], v179 offset:43008\n"
"v_mfma_f32_16x16x32_bf16 v[132:135], a[52:55], v[84:87], v[132:135]\n"
"s_add_u32 s56, s56, s62\n"
"v_exp_f32 v247, v247\n"
"v_mfma_f32_16x16x32_bf16 v[132:135], a[56:59], v[88:91], v[132:135]\n"
"s_nop 0\n"
"ds_read_b64_tr_b16 a[32:33], v179 offset:45056\n"
"ds_read_b64_tr_b16 a[34:35], v179 offset:47104\n"
"v_mfma_f32_16x16x32_bf16 v[132:135], a[0:3], v[92:95], v[132:135]\n"
"s_nop 0\n"
"v_exp_f32 v248, v248\n"
"v_mfma_f32_16x16x32_bf16 v[132:135], a[4:7], v[96:99], v[132:135]\n"
"v_add_u32 v176, 49152, v176\n"
"ds_read_b64_tr_b16 a[36:37], v176 offset:0\n"
"v_add_u32 v177, 49152, v177\n"
"v_mfma_f32_16x16x32_bf16 v[132:135], a[8:11], v[100:103], v[132:135]\n"
"s_waitcnt lgkmcnt(9)\n"
"ds_read_b64_tr_b16 a[38:39], v176 offset:2048\n"
"ds_read_b64_tr_b16 a[40:41], v176 offset:4096\n"
"v_mfma_f32_16x16x32_bf16 v[136:139], a[12:15], v[24:27], v[136:139]\n"
"s_nop 0\n"
"v_exp_f32 v249, v249\n"
"v_mfma_f32_16x16x32_bf16 v[136:139], a[16:19], v[28:31], v[136:139]\n"
"s_waitcnt lgkmcnt(7)\n"
"ds_read_b64_tr_b16 a[42:43], v176 offset:6144\n"
"ds_read_b64_tr_b16 a[44:45], v176 offset:8192\n"
"v_mfma_f32_16x16x32_bf16 v[136:139], a[20:23], v[32:35], v[136:139]\n"
"s_nop 0\n"
"buffer_load_dwordx4 v180, s[28:31], s56 offen lds\n"
"v_mfma_f32_16x16x32_bf16 v[136:139], a[24:27], v[36:39], v[136:139]\n"
"s_waitcnt lgkmcnt(5)\n"
"ds_read_b64_tr_b16 a[46:47], v176 offset:10240\n"
"ds_read_b64_tr_b16 a[48:49], v176 offset:12288\n"
"v_mfma_f32_16x16x32_bf16 v[136:139], a[28:31], v[40:43], v[136:139]\n"
"s_addk_i32 m0, 4096\n"
"v_pk_add_f32 v[252:253], v[232:233], v[234:235]\n"
"v_pk_add_f32 v[250:251], v[236:237], v[238:239]\n"
"v_pk_add_f32 v[250:251], v[250:251], v[240:241]\n"
"v_pk_add_f32 v[252:253], v[252:253], v[242:243]\n"
"v_pk_add_f32 v[250:251], v[250:251], v[244:245]\n"
"v_pk_add_f32 v[252:253], v[252:253], v[246:247]\n"
"v_pk_add_f32 v[250:251], v[250:251], v[248:249]\n"
"v_pk_add_f32 v[252:253], v[252:253], v[250:251]\n"
"v_add_f32 v252, v252, v253\n"
"v_rcp_f32 v252, v252\n"
"s_nop 0\n"
"v_pk_mul_f32 v[232:233], v[232:233], v[252:253] op_sel_hi:[1,0]\n"
"v_pk_mul_f32 v[234:235], v[234:235], v[252:253] op_sel_hi:[1,0]\n"
"v_pk_mul_f32 v[236:237], v[236:237], v[252:253] op_sel_hi:[1,0]\n"
"v_pk_mul_f32 v[238:239], v[238:239], v[252:253] op_sel_hi:[1,0]\n"
"v_pk_mul_f32 v[240:241], v[240:241], v[252:253] op_sel_hi:[1,0]\n"
"v_pk_mul_f32 v[242:243], v[242:243], v[252:253] op_sel_hi:[1,0]\n"
"v_pk_mul_f32 v[244:245], v[244:245], v[252:253] op_sel_hi:[1,0]\n"
"v_pk_mul_f32 v[246:247], v[246:247], v[252:253] op_sel_hi:[1,0]\n"
"v_pk_mul_f32 v[248:249], v[248:249], v[252:253] op_sel_hi:[1,0]\n"
"v_mfma_f32_16x16x32_bf16 v[136:139], a[32:35], v[44:47], v[136:139]\n"
"s_add_u32 s56, s56, s62\n"
"ds_read_b64_tr_b16 a[50:51], v176 offset:14336\n"
"ds_read_b64_tr_b16 a[52:53], v176 offset:16384\n"
"v_mfma_f32_16x16x32_bf16 v[140:143], a[12:15], v[80:83], v[140:143]\n"
"s_nop 0\n"
"buffer_load_dwordx4 v180, s[28:31], s56 offen lds\n"
"v_mfma_f32_16x16x32_bf16 v[140:143], a[16:19], v[84:87], v[140:143]\n"
"s_addk_i32 m0, 4096\n"
"ds_read_b64_tr_b16 a[54:55], v176 offset:18432\n"
"ds_read_b64_tr_b16 a[56:57], v176 offset:20480\n"
"v_mfma_f32_16x16x32_bf16 v[140:143], a[20:23], v[88:91], v[140:143]\n"
"s_mov_b32 s56, 3539200\n"
"buffer_load_dwordx2 v[192:193], v189, s[44:47], s59 offen sc1\n"
"v_mfma_f32_16x16x32_bf16 v[140:143], a[24:27], v[92:95], v[140:143]\n"
"s_lshl_b32 s58, s53, 8\n"
"ds_read_b64_tr_b16 a[58:59], v176 offset:22528\n"
"ds_read_b64_tr_b16 a[0:1], v177 offset:0\n"
"v_mfma_f32_16x16x32_bf16 v[140:143], a[28:31], v[96:99], v[140:143]\n"
"s_add_u32 s58, s58, s52\n"
"buffer_store_dwordx4 a[114:117], v188, s[44:47], s7 offen sc1\n"
"v_mfma_f32_16x16x32_bf16 v[140:143], a[32:35], v[100:103], v[140:143]\n"
"s_waitcnt lgkmcnt(9)\n"
"ds_read_b64_tr_b16 a[2:3], v177 offset:2048\n"
"v_add_u32 v178, 49152, v178\n"
"v_mfma_f32_16x16x32_bf16 v[112:115], a[36:39], v[48:51], v[112:115]\n"
"s_lshl_b32 s58, s58, 8\n"
"ds_read_b64_tr_b16 a[4:5], v177 offset:4096\n"
"ds_read_b64_tr_b16 a[6:7], v177 offset:6144\n"
"v_mfma_f32_16x16x32_bf16 v[112:115], a[40:43], v[52:55], v[112:115]\n"
"s_waitcnt lgkmcnt(8)\n"
"buffer_load_dwordx4 v180, s[28:31], s56 offen lds\n"
"v_mfma_f32_16x16x32_bf16 v[112:115], a[44:47], a[172:175], v[112:115]\n"
"s_addk_i32 m0, 4096\n"
"ds_read_b64_tr_b16 a[8:9], v177 offset:8192\n"
"s_add_u32 s58, s58, 524288\n"
"v_mfma_f32_16x16x32_bf16 v[112:115], a[48:51], a[176:179], v[112:115]\n"
"s_waitcnt lgkmcnt(5)\n"
"ds_read_b64_tr_b16 a[10:11], v177 offset:10240\n"
"s_add_u32 s60, s52, 1\n"
"v_mfma_f32_16x16x32_bf16 v[112:115], a[52:55], a[180:183], v[112:115]\n"
"s_add_u32 s56, s56, s62\n"
"ds_read_b64_tr_b16 a[12:13], v177 offset:12288\n"
"ds_read_b64_tr_b16 a[14:15], v177 offset:14336\n"
"v_mfma_f32_16x16x32_bf16 v[112:115], a[56:59], a[184:187], v[112:115]\n"
"s_lshr_b32 s60, s60, 4\n"
"buffer_load_dwordx4 v180, s[28:31], s56 offen lds\n"
"v_mfma_f32_16x16x32_bf16 v[116:119], a[36:39], v[104:107], v[116:119]\n"
"s_addk_i32 m0, 4096\n"
"ds_read_b64_tr_b16 a[16:17], v177 offset:16384\n"
"s_or_b32 s60, s60, s54\n"
"v_mfma_f32_16x16x32_bf16 v[116:119], a[40:43], v[108:111], v[116:119]\n"
"s_add_u32 s56, s56, s62\n"
"ds_read_b64_tr_b16 a[18:19], v177 offset:18432\n"
"ds_read_b64_tr_b16 a[20:21], v177 offset:20480\n"
"v_mfma_f32_16x16x32_bf16 v[116:119], a[44:47], a[188:191], v[116:119]\n"
"s_lshl_b32 s60, s60, 28\n"
"buffer_load_dwordx4 v180, s[28:31], s56 offen lds\n"
"v_mfma_f32_16x16x32_bf16 v[116:119], a[48:51], a[192:195], v[116:119]\n"
"s_addk_i32 m0, 4096\n"
"ds_read_b64_tr_b16 a[22:23], v177 offset:22528\n"
"v_add_u32 v179, 49152, v179\n"
"v_mfma_f32_16x16x32_bf16 v[116:119], a[52:55], a[196:199], v[116:119]\n"
"s_add_u32 s56, s56, s62\n"
"ds_read_b64_tr_b16 a[24:25], v178 offset:0\n"
"ds_read_b64_tr_b16 a[26:27], v178 offset:2048\n"
"v_mfma_f32_16x16x32_bf16 v[116:119], a[56:59], a[200:203], v[116:119]\n"
"s_waitcnt lgkmcnt(10)\n"
"buffer_load_dwordx4 v180, s[28:31], s56 offen lds\n"
"v_mfma_f32_16x16x32_bf16 v[120:123], a[0:3], v[48:51], v[120:123]\n"
"s_addk_i32 m0, 4096\n"
"ds_read_b64_tr_b16 a[28:29], v178 offset:4096\n"
"v_cvt_pk_bf16_f32 v232, v232, v233\n"
"v_mfma_f32_16x16x32_bf16 v[120:123], a[4:7], v[52:55], v[120:123]\n"
"s_waitcnt lgkmcnt(7)\n"
"ds_read_b64_tr_b16 a[30:31], v178 offset:6144\n"
"v_cvt_pk_bf16_f32 v233, v234, v235\n"
"v_mfma_f32_16x16x32_bf16 v[120:123], a[8:11], a[172:175], v[120:123]\n"
"s_add_u32 s56, s56, s62\n"
"ds_read_b64_tr_b16 a[32:33], v178 offset:8192\n"
"v_cvt_pk_bf16_f32 v234, v236, v237\n"
"v_mfma_f32_16x16x32_bf16 v[120:123], a[12:15], a[176:179], v[120:123]\n"
"s_waitcnt lgkmcnt(5)\n"
"ds_read_b64_tr_b16 a[34:35], v178 offset:10240\n"
"ds_read_b64_tr_b16 a[36:37], v178 offset:12288\n"
"v_mfma_f32_16x16x32_bf16 v[120:123], a[16:19], a[180:183], v[120:123]\n"
"s_nop 0\n"
"buffer_load_dwordx4 v180, s[28:31], s56 offen lds\n"
"v_mfma_f32_16x16x32_bf16 v[120:123], a[20:23], a[184:187], v[120:123]\n"
"s_addk_i32 m0, 4096\n"
"ds_read_b64_tr_b16 a[38:39], v178 offset:14336\n"
"v_cvt_pk_bf16_f32 v235, v238, v239\n"
"v_mfma_f32_16x16x32_bf16 v[124:127], a[0:3], v[104:107], v[124:127]\n"
"s_add_u32 s56, s56, s62\n"
"ds_read_b64_tr_b16 a[40:41], v178 offset:16384\n"
"v_cvt_pk_bf16_f32 v236, v240, v241\n"
"v_mfma_f32_16x16x32_bf16 v[124:127], a[4:7], v[108:111], v[124:127]\n"
"s_nop 0\n"
"ds_read_b64_tr_b16 a[42:43], v178 offset:18432\n"
"ds_read_b64_tr_b16 a[44:45], v178 offset:20480\n"
"v_mfma_f32_16x16x32_bf16 v[124:127], a[8:11], a[188:191], v[124:127]\n"
"s_nop 0\n"
"buffer_load_dwordx4 v180, s[28:31], s56 offen lds\n"
"v_mfma_f32_16x16x32_bf16 v[124:127], a[12:15], a[192:195], v[124:127]\n"
"s_addk_i32 m0, 4096\n"
"ds_read_b64_tr_b16 a[46:47], v178 offset:22528\n"
"v_add_u32 v176, 4294844416, v176\n"
"v_mfma_f32_16x16x32_bf16 v[124:127], a[16:19], a[196:199], v[124:127]\n"
"s_mov_b32 s56, 384\n"
"ds_read_b64_tr_b16 a[48:49], v179 offset:0\n"
"v_cvt_pk_bf16_f32 v237, v242, v243\n"
"v_mfma_f32_16x16x32_bf16 v[124:127], a[20:23], a[200:203], v[124:127]\n"
"s_waitcnt lgkmcnt(9)\n"
"ds_read_b64_tr_b16 a[50:51], v179 offset:2048\n"
"ds_read_b64_tr_b16 a[52:53], v179 offset:4096\n"
"v_mfma_f32_16x16x32_bf16 v[128:131], a[24:27], v[48:51], v[128:131]\n"
"s_nop 0\n"
"ds_read_b64_tr_b16 a[54:55], v179 offset:6144\n"
"ds_read_b64_tr_b16 a[56:57], v179 offset:8192\n"
"v_mfma_f32_16x16x32_bf16 v[128:131], a[28:31], v[52:55], v[128:131]\n"
"s_waitcnt lgkmcnt(9)\n"
"ds_read_b64_tr_b16 a[58:59], v179 offset:10240\n"
"ds_read_b64_tr_b16 a[0:1], v179 offset:12288\n"
"v_mfma_f32_16x16x32_bf16 v[128:131], a[32:35], a[172:175], v[128:131]\n"
"s_nop 0\n"
"ds_read_b64_tr_b16 a[2:3], v179 offset:14336\n"
"ds_read_b64_tr_b16 a[4:5], v179 offset:16384\n"
"v_mfma_f32_16x16x32_bf16 v[128:131], a[36:39], a[176:179], v[128:131]\n"
"s_waitcnt lgkmcnt(9)\n"
"ds_read_b64_tr_b16 a[6:7], v179 offset:18432\n"
"ds_read_b64_tr_b16 a[8:9], v179 offset:20480\n"
"v_mfma_f32_16x16x32_bf16 v[128:131], a[40:43], a[180:183], v[128:131]\n"
"ds_read_b64_tr_b16 a[10:11], v179 offset:22528\n"
"v_add_u32 v177, 4294844416, v177\n"
"v_mfma_f32_16x16x32_bf16 v[128:131], a[44:47], a[184:187], v[128:131]\n"
"s_nop 0\n"
"buffer_load_dwordx4 v180, s[24:27], s56 offen lds\n"
"v_mfma_f32_16x16x32_bf16 v[132:135], a[24:27], v[104:107], v[132:135]\n"
"s_addk_i32 m0, 4096\n"
"v_cvt_pk_bf16_f32 v238, v244, v245\n"
"v_cvt_pk_bf16_f32 v239, v246, v247\n"
"v_mfma_f32_16x16x32_bf16 v[132:135], a[28:31], v[108:111], v[132:135]\n"
"s_add_u32 s56, s56, s62\n"
"buffer_load_dwordx4 v180, s[24:27], s56 offen lds\n"
"v_mfma_f32_16x16x32_bf16 v[132:135], a[32:35], a[188:191], v[132:135]\n"
"s_addk_i32 m0, 4096\n"
"v_cvt_pk_bf16_f32 v240, v248, v249\n"
"v_mov_b32 v241, 0\n"
"v_mfma_f32_16x16x32_bf16 v[132:135], a[36:39], a[192:195], v[132:135]\n"
"s_waitcnt vmcnt(10) lgkmcnt(0)\n"
"v_accvgpr_write_b32 a144, v232\n"
"v_accvgpr_write_b32 a145, v233\n"
"v_mfma_f32_16x16x32_bf16 v[132:135], a[40:43], a[196:199], v[132:135]\n"
"s_barrier\n"
"ds_read_b64_tr_b16 a[12:13], v176 offset:0\n"
"v_accvgpr_write_b32 a146, v234\n"
"v_mfma_f32_16x16x32_bf16 v[132:135], a[44:47], a[200:203], v[132:135]\n"
"s_add_u32 s56, s56, s62\n"
"ds_read_b64_tr_b16 a[14:15], v176 offset:2048\n"
"v_accvgpr_write_b32 a147, v235\n"
"v_mfma_f32_16x16x32_bf16 v[136:139], a[48:51], v[48:51], v[136:139]\n"
"ds_read_b64_tr_b16 a[16:17], v176 offset:4096\n"
"v_accvgpr_write_b32 a148, v236\n"
"v_mfma_f32_16x16x32_bf16 v[136:139], a[52:55], v[52:55], v[136:139]\n"
"ds_read_b64_tr_b16 a[18:19], v176 offset:6144\n"
"v_accvgpr_write_b32 a149, v237\n"
"v_mfma_f32_16x16x32_bf16 v[136:139], a[56:59], a[172:175], v[136:139]\n"
"ds_read_b64_tr_b16 a[20:21], v176 offset:8192\n"
"v_accvgpr_write_b32 a150, v238\n"
"v_mfma_f32_16x16x32_bf16 v[136:139], a[0:3], a[176:179], v[136:139]\n"
"ds_read_b64_tr_b16 a[22:23], v176 offset:10240\n"
"v_accvgpr_write_b32 a151, v239\n"
"v_mfma_f32_16x16x32_bf16 v[136:139], a[4:7], a[180:183], v[136:139]\n"
"ds_read_b64_tr_b16 a[24:25], v176 offset:12288\n"
"v_accvgpr_write_b32 a152, v240\n"
"v_mfma_f32_16x16x32_bf16 v[136:139], a[8:11], a[184:187], v[136:139]\n"
"ds_read_b64_tr_b16 a[26:27], v176 offset:14336\n"
"v_accvgpr_write_b32 a153, v241\n"
"v_mfma_f32_16x16x32_bf16 v[232:235], a[204:207], v[218:221], 0\n"
"v_max_f32 v112, 0, v112\n"
"v_max_f32 v113, 0, v113\n"
"v_mfma_f32_16x16x32_bf16 v[236:239], a[210:213], v[218:221], 0\n"
"v_max_f32 v114, 0, v114\n"
"v_max_f32 v115, 0, v115\n"
"v_mfma_f32_16x16x32_bf16 v[240:243], a[216:219], v[218:221], 0\n"
"v_max_f32 v116, 0, v116\n"
"v_max_f32 v117, 0, v117\n"
"v_mfma_f32_16x16x32_bf16 v[244:247], a[222:225], v[218:221], 0\n"
"v_max_f32 v118, 0, v118\n"
"v_max_f32 v119, 0, v119\n"
"v_mfma_f32_16x16x32_bf16 v[248:251], a[228:231], v[218:221], 0\n"
"v_max_f32 v120, 0, v120\n"
"v_max_f32 v121, 0, v121\n"
"v_mfma_f32_16x16x16_bf16 v[232:235], a[208:209], v[222:223], v[232:235]\n"
"v_max_f32 v122, 0, v122\n"
"v_max_f32 v123, 0, v123\n"
"v_mfma_f32_16x16x32_bf16 v[140:143], a[48:51], v[104:107], v[140:143]\n"
"ds_read_b64_tr_b16 a[28:29], v176 offset:16384\n"
"v_max_f32 v124, 0, v124\n"
"v_mfma_f32_16x16x32_bf16 v[140:143], a[52:55], v[108:111], v[140:143]\n"
"ds_read_b64_tr_b16 a[30:31], v176 offset:18432\n"
"v_max_f32 v125, 0, v125\n"
"v_mfma_f32_16x16x32_bf16 v[140:143], a[56:59], a[188:191], v[140:143]\n"
"ds_read_b64_tr_b16 a[32:33], v176 offset:20480\n"
"v_add_u32 v178, 4294844416, v178\n"
"v_mfma_f32_16x16x32_bf16 v[140:143], a[0:3], a[192:195], v[140:143]\n"
"ds_read_b64_tr_b16 a[34:35], v176 offset:22528\n"
"v_max_f32 v126, 0, v126\n"
"v_mfma_f32_16x16x32_bf16 v[140:143], a[4:7], a[196:199], v[140:143]\n"
"s_nop 0\n"
"ds_read_b64_tr_b16 a[36:37], v177 offset:0\n"
"ds_read_b64_tr_b16 a[38:39], v177 offset:2048\n"
"v_mfma_f32_16x16x32_bf16 v[140:143], a[8:11], a[200:203], v[140:143]\n"
"s_waitcnt lgkmcnt(10)\n"
"buffer_load_dwordx4 v180, s[24:27], s56 offen lds\n"
"v_mfma_f32_16x16x16_bf16 v[236:239], a[214:215], v[222:223], v[236:239]\n"
"s_addk_i32 m0, 4096\n"
"v_max_f32 v127, 0, v127\n"
"v_max_f32 v128, 0, v128\n"
"v_mfma_f32_16x16x16_bf16 v[240:243], a[220:221], v[222:223], v[240:243]\n"
"s_add_u32 s56, s56, s62\n"
"v_max_f32 v129, 0, v129\n"
"v_max_f32 v130, 0, v130\n"
"v_mfma_f32_16x16x16_bf16 v[244:247], a[226:227], v[222:223], v[244:247]\n"
"v_max_f32 v131, 0, v131\n"
"v_max_f32 v132, 0, v132\n"
"v_mfma_f32_16x16x16_bf16 v[248:251], a[232:233], v[222:223], v[248:251]\n"
"s_nop 0\n"
"buffer_load_dwordx4 v180, s[24:27], s56 offen lds\n"
"v_mfma_f32_16x16x32_bf16 v[144:147], a[12:15], v[0:3], 0\n"
"s_addk_i32 m0, 4096\n"
"ds_read_b64_tr_b16 a[40:41], v177 offset:4096\n"
"s_waitcnt vmcnt(11)\n"
"v_mfma_f32_16x16x32_bf16 v[144:147], a[16:19], v[4:7], v[144:147]\n"
"s_waitcnt lgkmcnt(7)\n"
"ds_read_b64_tr_b16 a[42:43], v177 offset:6144\n"
"ds_read_b64_tr_b16 a[44:45], v177 offset:8192\n"
"v_mfma_f32_16x16x32_bf16 v[144:147], a[20:23], v[8:11], v[144:147]\n"
"s_add_u32 s56, s56, s62\n"
"v_subrev_u32 v193, s65, v193\n"
"v_xor_b32 v192, s64, v192\n"
"v_and_b32 v199, -2, v193\n"
"v_or_b32 v192, v192, v199\n"
"v_cmp_ne_u32 vcc, 0, v192\n"
"s_nop 1\n"
"s_cbranch_vccnz 53f\n"
"54:\n"
"v_mfma_f32_16x16x32_bf16 v[144:147], a[24:27], v[12:15], v[144:147]\n"
"s_waitcnt lgkmcnt(5)\n"
"ds_read_b64_tr_b16 a[46:47], v177 offset:10240\n"
"ds_read_b64_tr_b16 a[48:49], v177 offset:12288\n"
"v_mfma_f32_16x16x32_bf16 v[144:147], a[28:31], v[16:19], v[144:147]\n"
"s_nop 0\n"
"buffer_load_dwordx4 v180, s[24:27], s56 offen lds\n"
"v_mfma_f32_16x16x32_bf16 v[144:147], a[32:35], v[20:23], v[144:147]\n"
"s_addk_i32 m0, 4096\n"
"ds_read_b64_tr_b16 a[50:51], v177 offset:14336\n"
"v_accvgpr_read_b32 v189, a241\n"
"v_mfma_f32_16x16x32_bf16 v[148:151], a[12:15], v[56:59], 0\n"
"s_add_u32 s56, s56, s62\n"
"ds_read_b64_tr_b16 a[52:53], v177 offset:16384\n"
"ds_read_b64_tr_b16 a[54:55], v177 offset:18432\n"
"v_mfma_f32_16x16x32_bf16 v[148:151], a[16:19], v[60:63], v[148:151]\n"
"s_nop 0\n"
"buffer_load_dwordx4 v180, s[24:27], s56 offen lds\n"
"v_mfma_f32_16x16x32_bf16 v[148:151], a[20:23], v[64:67], v[148:151]\n"
"s_addk_i32 m0, 4096\n"
"ds_read_b64_tr_b16 a[56:57], v177 offset:20480\n"
"v_add_u32 v179, 4294844416, v179\n"
"v_mfma_f32_16x16x32_bf16 v[148:151], a[24:27], v[68:71], v[148:151]\n"
"s_mov_b32 s56, 1769856\n"
"ds_read_b64_tr_b16 a[58:59], v177 offset:22528\n"
"v_or_b32 v189, s60, v189\n"
"v_mfma_f32_16x16x32_bf16 v[148:151], a[28:31], v[72:75], v[148:151]\n"
"s_nop 0\n"
"ds_read_b64_tr_b16 a[0:1], v178 offset:0\n"
"ds_read_b64_tr_b16 a[2:3], v178 offset:2048\n"
"v_mfma_f32_16x16x32_bf16 v[148:151], a[32:35], v[76:79], v[148:151]\n"
"s_waitcnt lgkmcnt(10)\n"
"buffer_load_dwordx4 v[204:207], v189, s[44:47], s58 offen sc1\n"
"v_mfma_f32_16x16x32_bf16 v[152:155], a[36:39], v[0:3], 0\n"
"s_addk_i32 s58, 16384\n"
"ds_read_b64_tr_b16 a[4:5], v178 offset:4096\n"
"v_mul_f32 v254, 0x3fb8aa3b, v186\n"
"v_mfma_f32_16x16x32_bf16 v[152:155], a[40:43], v[4:7], v[152:155]\n"
"s_waitcnt lgkmcnt(7)\n"
"ds_read_b64_tr_b16 a[6:7], v178 offset:6144\n"
"v_max3_f32 v252, v232, v233, v234\n"
"v_mfma_f32_16x16x32_bf16 v[152:155], a[44:47], v[8:11], v[152:155]\n"
"ds_read_b64_tr_b16 a[8:9], v178 offset:8192\n"
"v_max3_f32 v252, v252, v235, v236\n"
"v_mfma_f32_16x16x32_bf16 v[152:155], a[48:51], v[12:15], v[152:155]\n"
"s_waitcnt lgkmcnt(5)\n"
"ds_read_b64_tr_b16 a[10:11], v178 offset:10240\n"
"ds_read_b64_tr_b16 a[12:13], v178 offset:12288\n"
"v_mfma_f32_16x16x32_bf16 v[152:155], a[52:55], v[16:19], v[152:155]\n"
"s_nop 0\n"
"buffer_load_dwordx4 v[208:211], v189, s[44:47], s58 offen sc1\n"
"v_mfma_f32_16x16x32_bf16 v[152:155], a[56:59], v[20:23], v[152:155]\n"
"s_addk_i32 s58, 16384\n"
"ds_read_b64_tr_b16 a[14:15], v178 offset:14336\n"
"v_max3_f32 v252, v252, v237, v238\n"
"v_mfma_f32_16x16x32_bf16 v[156:159], a[36:39], v[56:59], 0\n"
"ds_read_b64_tr_b16 a[16:17], v178 offset:16384\n"
"v_max3_f32 v252, v252, v239, v240\n"
"v_mfma_f32_16x16x32_bf16 v[156:159], a[40:43], v[60:63], v[156:159]\n"
"s_nop 0\n"
"ds_read_b64_tr_b16 a[18:19], v178 offset:18432\n"
"ds_read_b64_tr_b16 a[20:21], v178 offset:20480\n"
"v_mfma_f32_16x16x32_bf16 v[156:159], a[44:47], v[64:67], v[156:159]\n"
"s_nop 0\n"
"buffer_load_dwordx4 v[212:215], v189, s[44:47], s58 offen sc1\n"
"v_mfma_f32_16x16x32_bf16 v[156:159], a[48:51], v[68:71], v[156:159]\n"
"s_addk_i32 s58, 16384\n"
"ds_read_b64_tr_b16 a[22:23], v178 offset:22528\n"
"ds_read_b64_tr_b16 a[24:25], v179 offset:0\n"
"v_mfma_f32_16x16x32_bf16 v[156:159], a[52:55], v[72:75], v[156:159]\n"
"s_nop 0\n"
"buffer_load_dwordx4 v[224:227], v189, s[44:47], s58 offen sc1\n"
"v_mfma_f32_16x16x32_bf16 v[156:159], a[56:59], v[76:79], v[156:159]\n"
"s_waitcnt lgkmcnt(9)\n"
"ds_read_b64_tr_b16 a[26:27], v179 offset:2048\n"
"ds_read_b64_tr_b16 a[28:29], v179 offset:4096\n"
"v_mfma_f32_16x16x32_bf16 v[160:163], a[0:3], v[0:3], 0\n"
"s_add_u32 s7, s77, 16384\n"
"buffer_store_dwordx4 a[92:95], v188, s[44:47], s7 offen sc1\n"
"v_mfma_f32_16x16x32_bf16 v[160:163], a[4:7], v[4:7], v[160:163]\n"
"s_waitcnt lgkmcnt(7)\n"
"ds_read_b64_tr_b16 a[30:31], v179 offset:6144\n"
"ds_read_b64_tr_b16 a[32:33], v179 offset:8192\n"
"v_mfma_f32_16x16x32_bf16 v[160:163], a[8:11], v[8:11], v[160:163]\n"
"s_add_u32 s7, s77, 34816\n"
"v_cmp_eq_u32 vcc, 1, v193\n"
"s_nop 1\n"
"s_lshr_b32 s5, vcc_lo, 16\n"
"v_mfma_f32_16x16x32_bf16 v[160:163], a[12:15], v[12:15], v[160:163]\n"
"108:\n"
"s_cbranch_execz 112f\n"
"s_waitcnt lgkmcnt(5)\n"
"ds_read_b64_tr_b16 a[34:35], v179 offset:10240\n"
"ds_read_b64_tr_b16 a[36:37], v179 offset:12288\n"
"v_mfma_f32_16x16x32_bf16 v[160:163], a[16:19], v[16:19], v[160:163]\n"
"s_xor_b32 s81, s53, 1\n"
"buffer_store_dwordx4 a[134:137], v188, s[44:47], s7 offen sc1\n"
"v_mfma_f32_16x16x32_bf16 v[160:163], a[20:23], v[20:23], v[160:163]\n"
"s_lshr_b32 s81, s5, s81\n"
"ds_read_b64_tr_b16 a[38:39], v179 offset:14336\n"
"ds_read_b64_tr_b16 a[40:41], v179 offset:16384\n"
"v_mfma_f32_16x16x32_bf16 v[164:167], a[0:3], v[56:59], 0\n"
"s_and_b32 s81, s81, 1\n"
"buffer_load_dwordx4 v180, s[24:27], s56 offen lds\n"
"v_mfma_f32_16x16x32_bf16 v[164:167], a[4:7], v[60:63], v[164:167]\n"
"s_addk_i32 m0, 4096\n"
"ds_read_b64_tr_b16 a[42:43], v179 offset:18432\n"
"s_lshl_b32 s81, s81, 13\n"
"v_mfma_f32_16x16x32_bf16 v[164:167], a[8:11], v[64:67], v[164:167]\n"
"s_add_u32 s56, s56, s62\n"
"ds_read_b64_tr_b16 a[44:45], v179 offset:20480\n"
"ds_read_b64_tr_b16 a[46:47], v179 offset:22528\n"
"s_add_u32 s78, s78, s81\n"
"v_mfma_f32_16x16x32_bf16 v[164:167], a[12:15], v[68:71], v[164:167]\n"
"s_nop 0\n"
"buffer_load_dwordx4 v180, s[24:27], s56 offen lds\n"
"v_mfma_f32_16x16x32_bf16 v[164:167], a[16:19], v[72:75], v[164:167]\n"
"s_addk_i32 m0, 4096\n"
"ds_read_b64_tr_b16 a[48:49], v176 offset:24576\n"
"v_accvgpr_read_b32 v189, a242\n"
"v_mfma_f32_16x16x32_bf16 v[164:167], a[20:23], v[76:79], v[164:167]\n"
"s_waitcnt lgkmcnt(9)\n"
"ds_read_b64_tr_b16 a[50:51], v176 offset:26624\n"
"v_or_b32 v189, s60, v189\n"
"v_mfma_f32_16x16x32_bf16 v[168:171], a[24:27], v[0:3], 0\n"
"s_add_u32 s56, s56, s62\n"
"ds_read_b64_tr_b16 a[52:53], v176 offset:28672\n"
"ds_read_b64_tr_b16 a[54:55], v176 offset:30720\n"
"v_mfma_f32_16x16x32_bf16 v[168:171], a[28:31], v[4:7], v[168:171]\n"
"s_waitcnt lgkmcnt(8)\n"
"buffer_load_dwordx4 v180, s[24:27], s56 offen lds\n"
"v_mfma_f32_16x16x32_bf16 v[168:171], a[32:35], v[8:11], v[168:171]\n"
"s_addk_i32 m0, 4096\n"
"ds_read_b64_tr_b16 a[56:57], v176 offset:32768\n"
"s_lshl_b32 s58, s53, 4\n"
"v_mfma_f32_16x16x32_bf16 v[168:171], a[36:39], v[12:15], v[168:171]\n"
"s_waitcnt lgkmcnt(5)\n"
"ds_read_b64_tr_b16 a[58:59], v176 offset:34816\n"
"s_add_u32 s58, s58, s52\n"
"v_mfma_f32_16x16x32_bf16 v[168:171], a[40:43], v[16:19], v[168:171]\n"
"s_add_u32 s56, s56, s62\n"
"ds_read_b64_tr_b16 a[0:1], v176 offset:36864\n"
"ds_read_b64_tr_b16 a[2:3], v176 offset:38912\n"
"s_mul_i32 s61, s58, 128\n"
"v_mfma_f32_16x16x32_bf16 v[168:171], a[44:47], v[20:23], v[168:171]\n"
"s_add_u32 s61, s61, 135168\n"
"buffer_load_dwordx4 v180, s[24:27], s56 offen lds\n"
"v_mfma_f32_16x16x32_bf16 v[172:175], a[24:27], v[56:59], 0\n"
"s_addk_i32 m0, 4096\n"
"ds_read_b64_tr_b16 a[4:5], v176 offset:40960\n"
"s_lshl_b32 s58, s58, 8\n"
"v_mfma_f32_16x16x32_bf16 v[172:175], a[28:31], v[60:63], v[172:175]\n"
"s_add_u32 s56, s56, s62\n"
"ds_read_b64_tr_b16 a[6:7], v176 offset:43008\n"
"ds_read_b64_tr_b16 a[8:9], v176 offset:45056\n"
"v_mfma_f32_16x16x32_bf16 v[172:175], a[32:35], v[64:67], v[172:175]\n"
"s_add_u32 s58, s58, 299008\n"
"buffer_load_dwordx4 v180, s[24:27], s56 offen lds\n"
"v_mfma_f32_16x16x32_bf16 v[172:175], a[36:39], v[68:71], v[172:175]\n"
"s_addk_i32 m0, 4096\n"
"ds_read_b64_tr_b16 a[10:11], v176 offset:47104\n"
"s_mul_i32 s59, s53, 2048\n"
"v_mfma_f32_16x16x32_bf16 v[172:175], a[40:43], v[72:75], v[172:175]\n"
"s_add_u32 s56, s56, s62\n"
"ds_read_b64_tr_b16 a[12:13], v177 offset:24576\n"
"ds_read_b64_tr_b16 a[14:15], v177 offset:26624\n"
"s_add_u32 s59, s59, 135168\n"
"v_mfma_f32_16x16x32_bf16 v[172:175], a[44:47], v[76:79], v[172:175]\n"
"s_waitcnt lgkmcnt(10)\n"
"buffer_load_dwordx4 v180, s[24:27], s56 offen lds\n"
"v_mfma_f32_16x16x32_bf16 v[144:147], a[48:51], v[24:27], v[144:147]\n"
"s_addk_i32 m0, 4096\n"
"ds_read_b64_tr_b16 a[16:17], v177 offset:28672\n"
"v_max3_f32 v252, v252, v241, v242\n"
"v_mfma_f32_16x16x32_bf16 v[144:147], a[52:55], v[28:31], v[144:147]\n"
"s_waitcnt lgkmcnt(7)\n"
"ds_read_b64_tr_b16 a[18:19], v177 offset:30720\n"
"v_max3_f32 v252, v252, v243, v244\n"
"v_mfma_f32_16x16x32_bf16 v[144:147], a[56:59], v[32:35], v[144:147]\n"
"s_mov_b32 s56, 3539328\n"
"ds_read_b64_tr_b16 a[20:21], v177 offset:32768\n"
"v_max3_f32 v252, v252, v245, v246\n"
"v_mfma_f32_16x16x32_bf16 v[144:147], a[0:3], v[36:39], v[144:147]\n"
"s_waitcnt lgkmcnt(5)\n"
"ds_read_b64_tr_b16 a[22:23], v177 offset:34816\n"
"v_max3_f32 v252, v252, v247, v248\n"
"v_max_f32 v252, v252, v249\n"
"v_mfma_f32_16x16x32_bf16 v[144:147], a[4:7], v[40:43], v[144:147]\n"
"ds_read_b64_tr_b16 a[24:25], v177 offset:36864\n"
"v_mul_f32_e64 v250, v254, -v252\n"
"v_mfma_f32_16x16x32_bf16 v[144:147], a[8:11], v[44:47], v[144:147]\n"
"s_nop 0\n"
"ds_read_b64_tr_b16 a[26:27], v177 offset:38912\n"
"ds_read_b64_tr_b16 a[28:29], v177 offset:40960\n"
"v_mfma_f32_16x16x32_bf16 v[148:151], a[48:51], v[80:83], v[148:151]\n"
"s_nop 0\n"
"v_pk_fma_f32 v[232:233], v[232:233], v[254:255], v[250:251] op_sel_hi:[1,0,0]\n"
"v_pk_fma_f32 v[234:235], v[234:235], v[254:255], v[250:251] op_sel_hi:[1,0,0]\n"
"v_pk_fma_f32 v[236:237], v[236:237], v[254:255], v[250:251] op_sel_hi:[1,0,0]\n"
"v_pk_fma_f32 v[238:239], v[238:239], v[254:255], v[250:251] op_sel_hi:[1,0,0]\n"
"v_pk_fma_f32 v[240:241], v[240:241], v[254:255], v[250:251] op_sel_hi:[1,0,0]\n"
"v_pk_fma_f32 v[242:243], v[242:243], v[254:255], v[250:251] op_sel_hi:[1,0,0]\n"
"v_pk_fma_f32 v[244:245], v[244:245], v[254:255], v[250:251] op_sel_hi:[1,0,0]\n"
"v_pk_fma_f32 v[246:247], v[246:247], v[254:255], v[250:251] op_sel_hi:[1,0,0]\n"
"v_pk_fma_f32 v[248:249], v[248:249], v[254:255], v[250:251] op_sel_hi:[1,0,0]\n"
"v_mfma_f32_16x16x32_bf16 v[148:151], a[52:55], v[84:87], v[148:151]\n"
"s_nop 0\n"
"ds_read_b64_tr_b16 a[30:31], v177 offset:43008\n"
"ds_read_b64_tr_b16 a[32:33], v177 offset:45056\n"
"v_mfma_f32_16x16x32_bf16 v[148:151], a[56:59], v[88:91], v[148:151]\n"
"s_nop 0\n"
"v_exp_f32 v232, v232\n"
"v_mfma_f32_16x16x32_bf16 v[148:151], a[0:3], v[92:95], v[148:151]\n"
"ds_read_b64_tr_b16 a[34:35], v177 offset:47104\n"
"v_exp_f32 v233, v233\n"
"v_mfma_f32_16x16x32_bf16 v[148:151], a[4:7], v[96:99], v[148:151]\n"
"s_nop 0\n"
"ds_read_b64_tr_b16 a[36:37], v178 offset:24576\n"
"ds_read_b64_tr_b16 a[38:39], v178 offset:26624\n"
"v_mfma_f32_16x16x32_bf16 v[148:151], a[8:11], v[100:103], v[148:151]\n"
"s_waitcnt lgkmcnt(10)\n"
"v_exp_f32 v234, v234\n"
"v_mfma_f32_16x16x32_bf16 v[152:155], a[12:15], v[24:27], v[152:155]\n"
"s_nop 0\n"
"ds_read_b64_tr_b16 a[40:41], v178 offset:28672\n"
"ds_read_b64_tr_b16 a[42:43], v178 offset:30720\n"
"v_mfma_f32_16x16x32_bf16 v[152:155], a[16:19], v[28:31], v[152:155]\n"
"s_waitcnt lgkmcnt(8)\n"
"v_exp_f32 v235, v235\n"
"v_mfma_f32_16x16x32_bf16 v[152:155], a[20:23], v[32:35], v[152:155]\n"
"s_nop 0\n"
"ds_read_b64_tr_b16 a[44:45], v178 offset:32768\n"
"ds_read_b64_tr_b16 a[46:47], v178 offset:34816\n"
"v_mfma_f32_16x16x32_bf16 v[152:155], a[24:27], v[36:39], v[152:155]\n"
"s_waitcnt lgkmcnt(6)\n"
"v_exp_f32 v236, v236\n"
"v_mfma_f32_16x16x32_bf16 v[152:155], a[28:31], v[40:43], v[152:155]\n"
"ds_read_b64_tr_b16 a[48:49], v178 offset:36864\n"
"v_exp_f32 v237, v237\n"
"v_mfma_f32_16x16x32_bf16 v[152:155], a[32:35], v[44:47], v[152:155]\n"
"s_nop 0\n"
"ds_read_b64_tr_b16 a[50:51], v178 offset:38912\n"
"ds_read_b64_tr_b16 a[52:53], v178 offset:40960\n"
"v_mfma_f32_16x16x32_bf16 v[156:159], a[12:15], v[80:83], v[156:159]\n"
"s_nop 0\n"
"buffer_load_dwordx4 v180, s[24:27], s56 offen lds\n"
"v_mfma_f32_16x16x32_bf16 v[156:159], a[16:19], v[84:87], v[156:159]\n"
"s_addk_i32 m0, 4096\n"
"ds_read_b64_tr_b16 a[54:55], v178 offset:43008\n"
"ds_read_b64_tr_b16 a[56:57], v178 offset:45056\n"
"v_mfma_f32_16x16x32_bf16 v[156:159], a[20:23], v[88:91], v[156:159]\n"
"s_add_u32 s56, s56, s62\n"
"v_exp_f32 v238, v238\n"
"v_mfma_f32_16x16x32_bf16 v[156:159], a[24:27], v[92:95], v[156:159]\n"
"s_nop 0\n"
"ds_read_b64_tr_b16 a[58:59], v178 offset:47104\n"
"ds_read_b64_tr_b16 a[0:1], v179 offset:24576\n"
"v_mfma_f32_16x16x32_bf16 v[156:159], a[28:31], v[96:99], v[156:159]\n"
"s_nop 0\n"
"v_exp_f32 v239, v239\n"
"v_mfma_f32_16x16x32_bf16 v[156:159], a[32:35], v[100:103], v[156:159]\n"
"s_waitcnt lgkmcnt(9)\n"
"ds_read_b64_tr_b16 a[2:3], v179 offset:26624\n"
"ds_read_b64_tr_b16 a[4:5], v179 offset:28672\n"
"v_mfma_f32_16x16x32_bf16 v[160:163], a[36:39], v[24:27], v[160:163]\n"
"s_nop 0\n"
"ds_read_b64_tr_b16 a[6:7], v179 offset:30720\n"
"ds_read_b64_tr_b16 a[8:9], v179 offset:32768\n"
"v_mfma_f32_16x16x32_bf16 v[160:163], a[40:43], v[28:31], v[160:163]\n"
"s_waitcnt lgkmcnt(9)\n"
"ds_read_b64_tr_b16 a[10:11], v179 offset:34816\n"
"ds_read_b64_tr_b16 a[12:13], v179 offset:36864\n"
"v_mfma_f32_16x16x32_bf16 v[160:163], a[44:47], v[32:35], v[160:163]\n"
"s_nop 0\n"
"ds_read_b64_tr_b16 a[14:15], v179 offset:38912\n"
"ds_read_b64_tr_b16 a[16:17], v179 offset:40960\n"
"v_mfma_f32_16x16x32_bf16 v[160:163], a[48:51], v[36:39], v[160:163]\n"
"s_waitcnt lgkmcnt(9)\n"
"ds_read_b64_tr_b16 a[18:19], v179 offset:43008\n"
"ds_read_b64_tr_b16 a[20:21], v179 offset:45056\n"
"v_mfma_f32_16x16x32_bf16 v[160:163], a[52:55], v[40:43], v[160:163]\n"
"ds_read_b64_tr_b16 a[22:23], v179 offset:47104\n"
"v_add_u32 v176, 49152, v176\n"
"v_mfma_f32_16x16x32_bf16 v[160:163], a[56:59], v[44:47], v[160:163]\n"
"s_nop 0\n"
"buffer_load_dwordx4 v180, s[24:27], s56 offen lds\n"
"v_mfma_f32_16x16x32_bf16 v[164:167], a[36:39], v[80:83], v[164:167]\n"
"s_addk_i32 m0, 4096\n"
"v_exp_f32 v240, v240\n"
"v_mfma_f32_16x16x32_bf16 v[164:167], a[40:43], v[84:87], v[164:167]\n"
"s_add_u32 s56, s56, s62\n"
"buffer_load_dwordx4 v180, s[24:27], s56 offen lds\n"
"v_mfma_f32_16x16x32_bf16 v[164:167], a[44:47], v[88:91], v[164:167]\n"
"s_addk_i32 m0, 4096\n"
"v_add_u32 v177, 49152, v177\n"
"s_waitcnt vmcnt(11)\n"
"v_exp_f32 v241, v241\n"
"v_mfma_f32_16x16x32_bf16 v[164:167], a[48:51], v[92:95], v[164:167]\n"
"s_waitcnt lgkmcnt(0)\n"
"v_exp_f32 v242, v242\n"
"v_mfma_f32_16x16x32_bf16 v[164:167], a[52:55], v[96:99], v[164:167]\n"
"s_barrier\n"
"ds_read_b64_tr_b16 a[24:25], v176 offset:0\n"
"ds_read_b64_tr_b16 a[26:27], v176 offset:2048\n"
"v_mfma_f32_16x16x32_bf16 v[164:167], a[56:59], v[100:103], v[164:167]\n"
"s_add_u32 s56, s56, s62\n"
"buffer_load_dwordx4 v180, s[24:27], s56 offen lds\n"
"v_mfma_f32_16x16x32_bf16 v[228:231], a[164:167], v[204:207], 0\n"
"s_addk_i32 m0, 4096\n"
"v_exp_f32 v243, v243\n"
"v_mfma_f32_16x16x32_bf16 v[228:231], a[164:167], v[208:211], v[228:231]\n"
"s_add_u32 s56, s56, s62\n"
"buffer_load_dwordx4 v180, s[24:27], s56 offen lds\n"
"v_mfma_f32_16x16x32_bf16 v[228:231], a[164:167], v[212:215], v[228:231]\n"
"s_addk_i32 m0, 4096\n"
"v_exp_f32 v244, v244\n"
"v_mfma_f32_16x16x32_bf16 v[228:231], a[164:167], v[224:227], v[228:231]\n"
"s_add_u32 s56, s56, s62\n"
"v_exp_f32 v245, v245\n"
"v_mfma_f32_16x16x32_bf16 v[168:171], a[0:3], v[24:27], v[168:171]\n"
"s_nop 0\n"
"ds_read_b64_tr_b16 a[28:29], v176 offset:4096\n"
"ds_read_b64_tr_b16 a[30:31], v176 offset:6144\n"
"v_mfma_f32_16x16x32_bf16 v[168:171], a[4:7], v[28:31], v[168:171]\n"
"s_nop 0\n"
"buffer_load_dwordx4 v180, s[24:27], s56 offen lds\n"
"v_mfma_f32_16x16x32_bf16 v[168:171], a[8:11], v[32:35], v[168:171]\n"
"ds_read_b64_tr_b16 a[32:33], v176 offset:8192\n"
"v_cvt_pk_bf16_f32 v228, v228, v229\n"
"v_mfma_f32_16x16x32_bf16 v[168:171], a[12:15], v[36:39], v[168:171]\n"
"ds_read_b64_tr_b16 a[34:35], v176 offset:10240\n"
"v_cvt_pk_bf16_f32 v229, v230, v231\n"
"v_mfma_f32_16x16x32_bf16 v[168:171], a[16:19], v[40:43], v[168:171]\n"
"s_add_u32 m0, s55, 0\n"
"ds_read_b64_tr_b16 a[36:37], v176 offset:12288\n"
"ds_read_b64_tr_b16 a[38:39], v176 offset:14336\n"
"v_mfma_f32_16x16x32_bf16 v[168:171], a[20:23], v[44:47], v[168:171]\n"
"s_mov_b32 s56, 384\n"
"buffer_store_dwordx2 v[228:229], v189, s[44:47], s58 offen sc1\n"
"v_mfma_f32_16x16x32_bf16 v[172:175], a[0:3], v[80:83], v[172:175]\n"
"s_nop 0\n"
"ds_read_b64_tr_b16 a[40:41], v176 offset:16384\n"
"ds_read_b64_tr_b16 a[42:43], v176 offset:18432\n"
"v_mfma_f32_16x16x32_bf16 v[172:175], a[4:7], v[84:87], v[172:175]\n"
"s_nop 0\n"
"v_exp_f32 v246, v246\n"
"v_mfma_f32_16x16x32_bf16 v[172:175], a[8:11], v[88:91], v[172:175]\n"
"s_nop 0\n"
"ds_read_b64_tr_b16 a[44:45], v176 offset:20480\n"
"ds_read_b64_tr_b16 a[46:47], v176 offset:22528\n"
"v_mfma_f32_16x16x32_bf16 v[172:175], a[12:15], v[92:95], v[172:175]\n"
"s_nop 0\n"
"buffer_load_dwordx4 v180, s[28:31], s56 offen lds\n"
"v_mfma_f32_16x16x32_bf16 v[172:175], a[16:19], v[96:99], v[172:175]\n"
"s_addk_i32 m0, 4096\n"
"ds_read_b64_tr_b16 a[48:49], v177 offset:0\n"
"ds_read_b64_tr_b16 a[50:51], v177 offset:2048\n"
"v_mfma_f32_16x16x32_bf16 v[172:175], a[20:23], v[100:103], v[172:175]\n"
"s_waitcnt lgkmcnt(10)\n"
"v_exp_f32 v247, v247\n"
"v_mfma_f32_16x16x32_bf16 v[144:147], a[24:27], v[48:51], v[144:147]\n"
"s_add_u32 s56, s56, s62\n"
"ds_read_b64_tr_b16 a[52:53], v177 offset:4096\n"
"ds_read_b64_tr_b16 a[54:55], v177 offset:6144\n"
"v_mfma_f32_16x16x32_bf16 v[144:147], a[28:31], v[52:55], v[144:147]\n"
"s_waitcnt lgkmcnt(8)\n"
"v_exp_f32 v248, v248\n"
"v_mfma_f32_16x16x32_bf16 v[144:147], a[32:35], a[172:175], v[144:147]\n"
"s_nop 0\n"
"ds_read_b64_tr_b16 a[56:57], v177 offset:8192\n"
"ds_read_b64_tr_b16 a[58:59], v177 offset:10240\n"
"v_mfma_f32_16x16x32_bf16 v[144:147], a[36:39], a[176:179], v[144:147]\n"
"s_waitcnt lgkmcnt(6)\n"
"buffer_load_dwordx4 v180, s[28:31], s56 offen lds\n"
"v_mfma_f32_16x16x32_bf16 v[144:147], a[40:43], a[180:183], v[144:147]\n"
"s_addk_i32 m0, 4096\n"
"ds_read_b64_tr_b16 a[0:1], v177 offset:12288\n"
"ds_read_b64_tr_b16 a[2:3], v177 offset:14336\n"
"v_mfma_f32_16x16x32_bf16 v[144:147], a[44:47], a[184:187], v[144:147]\n"
"s_add_u32 s56, s56, s62\n"
"buffer_load_dwordx4 v180, s[28:31], s56 offen lds\n"
"v_mfma_f32_16x16x32_bf16 v[148:151], a[24:27], v[104:107], v[148:151]\n"
"ds_read_b64_tr_b16 a[4:5], v177 offset:16384\n"
"v_add_u32 v178, 49152, v178\n"
"v_mfma_f32_16x16x32_bf16 v[148:151], a[28:31], v[108:111], v[148:151]\n"
"s_addk_i32 m0, 4096\n"
"ds_read_b64_tr_b16 a[6:7], v177 offset:18432\n"
"v_mul_f32 v144, v112, v144\n"
"v_mul_f32 v145, v113, v145\n"
"v_mfma_f32_16x16x32_bf16 v[148:151], a[32:35], a[188:191], v[148:151]\n"
"s_add_u32 s56, s56, s62\n"
"ds_read_b64_tr_b16 a[8:9], v177 offset:20480\n"
"ds_read_b64_tr_b16 a[10:11], v177 offset:22528\n"
"v_mfma_f32_16x16x32_bf16 v[148:151], a[36:39], a[192:195], v[148:151]\n"
"s_nop 0\n"
"buffer_load_dwordx4 v180, s[28:31], s56 offen lds\n"
"v_mfma_f32_16x16x32_bf16 v[148:151], a[40:43], a[196:199], v[148:151]\n"
"ds_read_b64_tr_b16 a[12:13], v178 offset:0\n"
"v_add_u32 v179, 49152, v179\n"
"v_mfma_f32_16x16x32_bf16 v[148:151], a[44:47], a[200:203], v[148:151]\n"
"s_waitcnt lgkmcnt(9)\n"
"ds_read_b64_tr_b16 a[14:15], v178 offset:2048\n"
"v_mul_f32 v146, v114, v146\n"
"v_mfma_f32_16x16x32_bf16 v[152:155], a[48:51], v[48:51], v[152:155]\n"
"s_addk_i32 m0, 4096\n"
"ds_read_b64_tr_b16 a[16:17], v178 offset:4096\n"
"v_mul_f32 v147, v115, v147\n"
"v_mfma_f32_16x16x32_bf16 v[152:155], a[52:55], v[52:55], v[152:155]\n"
"s_waitcnt lgkmcnt(7)\n"
"ds_read_b64_tr_b16 a[18:19], v178 offset:6144\n"
"v_mul_f32 v148, v116, v148\n"
"v_mfma_f32_16x16x32_bf16 v[152:155], a[56:59], a[172:175], v[152:155]\n"
"s_add_u32 s56, s56, s62\n"
"ds_read_b64_tr_b16 a[20:21], v178 offset:8192\n"
"ds_read_b64_tr_b16 a[22:23], v178 offset:10240\n"
"v_mul_f32 v149, v117, v149\n"
"v_mfma_f32_16x16x32_bf16 v[152:155], a[0:3], a[176:179], v[152:155]\n"
"s_waitcnt lgkmcnt(6)\n"
"buffer_load_dwordx4 v180, s[28:31], s56 offen lds\n"
"v_mfma_f32_16x16x32_bf16 v[152:155], a[4:7], a[180:183], v[152:155]\n"
"ds_read_b64_tr_b16 a[24:25], v178 offset:12288\n"
"v_mul_f32 v150, v118, v150\n"
"v_mfma_f32_16x16x32_bf16 v[152:155], a[8:11], a[184:187], v[152:155]\n"
"s_addk_i32 m0, 4096\n"
"ds_read_b64_tr_b16 a[26:27], v178 offset:14336\n"
"v_mul_f32 v151, v119, v151\n"
"v_mfma_f32_16x16x32_bf16 v[156:159], a[48:51], v[104:107], v[156:159]\n"
"s_add_u32 s56, s56, s62\n"
"ds_read_b64_tr_b16 a[28:29], v178 offset:16384\n"
"ds_read_b64_tr_b16 a[30:31], v178 offset:18432\n"
"v_max_f32 v133, 0, v133\n"
"v_mfma_f32_16x16x32_bf16 v[156:159], a[52:55], v[108:111], v[156:159]\n"
"s_nop 0\n"
"buffer_load_dwordx4 v180, s[28:31], s56 offen lds\n"
"v_mfma_f32_16x16x32_bf16 v[156:159], a[56:59], a[188:191], v[156:159]\n"
"ds_read_b64_tr_b16 a[32:33], v178 offset:20480\n"
"v_mul_f32 v152, v120, v152\n"
"v_mfma_f32_16x16x32_bf16 v[156:159], a[0:3], a[192:195], v[156:159]\n"
"ds_read_b64_tr_b16 a[34:35], v178 offset:22528\n"
"v_mul_f32 v153, v121, v153\n"
"v_mul_f32 v154, v122, v154\n"
"v_mfma_f32_16x16x32_bf16 v[156:159], a[4:7], a[196:199], v[156:159]\n"
"s_addk_i32 m0, 4096\n"
"ds_read_b64_tr_b16 a[36:37], v179 offset:0\n"
"v_mul_f32 v155, v123, v155\n"
"v_mfma_f32_16x16x32_bf16 v[156:159], a[8:11], a[200:203], v[156:159]\n"
"s_waitcnt lgkmcnt(9)\n"
"ds_read_b64_tr_b16 a[38:39], v179 offset:2048\n"
"v_max_f32 v134, 0, v134\n"
"v_max_f32 v135, 0, v135\n"
"v_mfma_f32_16x16x32_bf16 v[160:163], a[12:15], v[48:51], v[160:163]\n"
"s_mov_b32 s56, 1769856\n"
"ds_read_b64_tr_b16 a[40:41], v179 offset:4096\n"
"ds_read_b64_tr_b16 a[42:43], v179 offset:6144\n"
"v_mfma_f32_16x16x32_bf16 v[160:163], a[16:19], v[52:55], v[160:163]\n"
"s_waitcnt lgkmcnt(8)\n"
"buffer_load_dwordx4 v180, s[28:31], s56 offen lds\n"
"v_mfma_f32_16x16x32_bf16 v[160:163], a[20:23], a[172:175], v[160:163]\n"
"ds_read_b64_tr_b16 a[44:45], v179 offset:8192\n"
"v_mul_f32 v156, v124, v156\n"
"v_mul_f32 v157, v125, v157\n"
"v_mul_f32 v158, v126, v158\n"
"v_mfma_f32_16x16x32_bf16 v[160:163], a[24:27], a[176:179], v[160:163]\n"
"s_waitcnt lgkmcnt(5)\n"
"ds_read_b64_tr_b16 a[46:47], v179 offset:10240\n"
"v_mul_f32 v159, v127, v159\n"
"v_mfma_f32_16x16x32_bf16 v[160:163], a[28:31], a[180:183], v[160:163]\n"
"s_addk_i32 m0, 4096\n"
"ds_read_b64_tr_b16 a[48:49], v179 offset:12288\n"
"v_max_f32 v136, 0, v136\n"
"v_max_f32 v137, 0, v137\n"
"v_mfma_f32_16x16x32_bf16 v[160:163], a[32:35], a[184:187], v[160:163]\n"
"s_add_u32 s56, s56, s62\n"
"ds_read_b64_tr_b16 a[50:51], v179 offset:14336\n"
"ds_read_b64_tr_b16 a[52:53], v179 offset:16384\n"
"v_max_f32 v138, 0, v138\n"
"v_mfma_f32_16x16x32_bf16 v[164:167], a[12:15], v[104:107], v[164:167]\n"
"s_nop 0\n"
"buffer_load_dwordx4 v180, s[28:31], s56 offen lds\n"
"v_mfma_f32_16x16x32_bf16 v[164:167], a[16:19], v[108:111], v[164:167]\n"
"ds_read_b64_tr_b16 a[54:55], v179 offset:18432\n"
"v_mul_f32 v160, v128, v160\n"
"v_mul_f32 v161, v129, v161\n"
"v_mul_f32 v162, v130, v162\n"
"v_mfma_f32_16x16x32_bf16 v[164:167], a[20:23], a[188:191], v[164:167]\n"
"s_addk_i32 m0, 4096\n"
"ds_read_b64_tr_b16 a[56:57], v179 offset:20480\n"
"v_mul_f32 v163, v131, v163\n"
"v_max_f32 v139, 0, v139\n"
"v_mfma_f32_16x16x32_bf16 v[164:167], a[24:27], a[192:195], v[164:167]\n"
"s_add_u32 s56, s56, s62\n"
"ds_read_b64_tr_b16 a[58:59], v179 offset:22528\n"
"ds_read_b64_tr_b16 a[0:1], v176 offset:24576\n"
"v_max_f32 v140, 0, v140\n"
"v_mfma_f32_16x16x32_bf16 v[164:167], a[28:31], a[196:199], v[164:167]\n"
"s_nop 0\n"
"buffer_load_dwordx4 v180, s[28:31], s56 offen lds\n"
"v_mfma_f32_16x16x32_bf16 v[164:167], a[32:35], a[200:203], v[164:167]\n"
"s_waitcnt lgkmcnt(9)\n"
"ds_read_b64_tr_b16 a[2:3], v176 offset:26624\n"
"v_max_f32 v141, 0, v141\n"
"v_max_f32 v142, 0, v142\n"
"v_max_f32 v143, 0, v143\n"
"v_mfma_f32_16x16x32_bf16 v[168:171], a[36:39], v[48:51], v[168:171]\n"
"ds_read_b64_tr_b16 a[4:5], v176 offset:28672\n"
"v_cvt_pk_bf16_f32 v144, v144, v145\n"
"v_mul_f32 v164, v132, v164\n"
"v_mfma_f32_16x16x32_bf16 v[168:171], a[40:43], v[52:55], v[168:171]\n"
"s_waitcnt lgkmcnt(7)\n"
"ds_read_b64_tr_b16 a[6:7], v176 offset:30720\n"
"v_mul_f32 v165, v133, v165\n"
"v_mul_f32 v166, v134, v166\n"
"v_mul_f32 v167, v135, v167\n"
"v_mfma_f32_16x16x32_bf16 v[168:171], a[44:47], a[172:175], v[168:171]\n"
"s_addk_i32 m0, 4096\n"
"ds_read_b64_tr_b16 a[8:9], v176 offset:32768\n"
"v_cvt_pk_bf16_f32 v145, v146, v147\n"
"v_exp_f32 v249, v249\n"
"v_mfma_f32_16x16x32_bf16 v[168:171], a[48:51], a[176:179], v[168:171]\n"
"s_waitcnt lgkmcnt(5)\n"
"ds_read_b64_tr_b16 a[10:11], v176 offset:34816\n"
"v_accvgpr_write_b32 a76, v144\n"
"v_pk_add_f32 v[252:253], v[232:233], v[234:235]\n"
"v_pk_add_f32 v[250:251], v[236:237], v[238:239]\n"
"v_pk_add_f32 v[250:251], v[250:251], v[240:241]\n"
"v_pk_add_f32 v[252:253], v[252:253], v[242:243]\n"
"v_pk_add_f32 v[250:251], v[250:251], v[244:245]\n"
"v_pk_add_f32 v[252:253], v[252:253], v[246:247]\n"
"v_pk_add_f32 v[250:251], v[250:251], v[248:249]\n"
"v_pk_add_f32 v[252:253], v[252:253], v[250:251]\n"
"v_add_f32 v252, v252, v253\n"
"v_rcp_f32 v252, v252\n"
"s_nop 0\n"
"v_pk_mul_f32 v[232:233], v[232:233], v[252:253] op_sel_hi:[1,0]\n"
"v_pk_mul_f32 v[234:235], v[234:235], v[252:253] op_sel_hi:[1,0]\n"
"v_pk_mul_f32 v[236:237], v[236:237], v[252:253] op_sel_hi:[1,0]\n"
"v_pk_mul_f32 v[238:239], v[238:239], v[252:253] op_sel_hi:[1,0]\n"
"v_pk_mul_f32 v[240:241], v[240:241], v[252:253] op_sel_hi:[1,0]\n"
"v_pk_mul_f32 v[242:243], v[242:243], v[252:253] op_sel_hi:[1,0]\n"
"v_pk_mul_f32 v[244:245], v[244:245], v[252:253] op_sel_hi:[1,0]\n"
"v_pk_mul_f32 v[246:247], v[246:247], v[252:253] op_sel_hi:[1,0]\n"
"v_pk_mul_f32 v[248:249], v[248:249], v[252:253] op_sel_hi:[1,0]\n"
"v_mfma_f32_16x16x32_bf16 v[168:171], a[52:55], a[180:183], v[168:171]\n"
"s_add_u32 s56, s56, s62\n"
"ds_read_b64_tr_b16 a[12:13], v176 offset:36864\n"
"v_cvt_pk_bf16_f32 v232, v232, v233\n"
"v_cvt_pk_bf16_f32 v233, v234, v235\n"
"v_cvt_pk_bf16_f32 v234, v236, v237\n"
"v_mfma_f32_16x16x32_bf16 v[168:171], a[56:59], a[184:187], v[168:171]\n"
"ds_read_b64_tr_b16 a[14:15], v176 offset:38912\n"
"v_accvgpr_write_b32 a77, v145\n"
"v_cvt_pk_bf16_f32 v148, v148, v149\n"
"v_cvt_pk_bf16_f32 v149, v150, v151\n"
"v_mfma_f32_16x16x32_bf16 v[172:175], a[36:39], v[104:107], v[172:175]\n"
"ds_read_b64_tr_b16 a[16:17], v176 offset:40960\n"
"v_accvgpr_write_b32 a118, v148\n"
"v_accvgpr_write_b32 a119, v149\n"
"v_mul_f32 v168, v136, v168\n"
"v_mfma_f32_16x16x32_bf16 v[172:175], a[40:43], v[108:111], v[172:175]\n"
"ds_read_b64_tr_b16 a[18:19], v176 offset:43008\n"
"v_mul_f32 v169, v137, v169\n"
"v_mul_f32 v170, v138, v170\n"
"v_mul_f32 v171, v139, v171\n"
"v_mfma_f32_16x16x32_bf16 v[172:175], a[44:47], a[188:191], v[172:175]\n"
"s_nop 0\n"
"ds_read_b64_tr_b16 a[20:21], v176 offset:45056\n"
"ds_read_b64_tr_b16 a[22:23], v176 offset:47104\n"
"v_cvt_pk_bf16_f32 v235, v238, v239\n"
"v_cvt_pk_bf16_f32 v236, v240, v241\n"
"v_mfma_f32_16x16x32_bf16 v[172:175], a[48:51], a[192:195], v[172:175]\n"
"s_nop 0\n"
"buffer_load_dwordx4 v180, s[28:31], s56 offen lds\n"
"v_mfma_f32_16x16x32_bf16 v[172:175], a[52:55], a[196:199], v[172:175]\n"
"s_addk_i32 m0, 4096\n"
"ds_read_b64_tr_b16 a[24:25], v177 offset:24576\n"
"v_cvt_pk_bf16_f32 v237, v242, v243\n"
"v_cvt_pk_bf16_f32 v238, v244, v245\n"
"v_cvt_pk_bf16_f32 v239, v246, v247\n"
"v_mfma_f32_16x16x32_bf16 v[172:175], a[56:59], a[200:203], v[172:175]\n"
"s_waitcnt lgkmcnt(9)\n"
"ds_read_b64_tr_b16 a[26:27], v177 offset:26624\n"
"v_cvt_pk_bf16_f32 v152, v152, v153\n"
"v_cvt_pk_bf16_f32 v153, v154, v155\n"
"v_accvgpr_write_b32 a78, v152\n"
"v_mfma_f32_16x16x32_bf16 v[112:115], a[0:3], v[0:3], 0\n"
"s_add_u32 s56, s56, s62\n"
"ds_read_b64_tr_b16 a[28:29], v177 offset:28672\n"
"ds_read_b64_tr_b16 a[30:31], v177 offset:30720\n"
"s_waitcnt vmcnt(10)\n"
"v_mfma_f32_16x16x32_bf16 v[112:115], a[4:7], v[4:7], v[112:115]\n"
"s_waitcnt lgkmcnt(8)\n"
"v_add_u32_e64 v199, s65, 1\n"
"buffer_store_dwordx2 v[198:199], v190, s[44:47], s61 offen offset:0 sc1\n"
"v_mfma_f32_16x16x32_bf16 v[112:115], a[8:11], v[8:11], v[112:115]\n"
"s_nop 0\n"
"ds_read_b64_tr_b16 a[32:33], v177 offset:32768\n"
"ds_read_b64_tr_b16 a[34:35], v177 offset:34816\n"
"v_accvgpr_read_b32 v189, a246\n"
"v_accvgpr_read_b32 v190, a247\n"
"v_mfma_f32_16x16x32_bf16 v[112:115], a[12:15], v[12:15], v[112:115]\n"
"s_waitcnt lgkmcnt(6)\n"
"buffer_load_dwordx4 v180, s[28:31], s56 offen lds\n"
"v_mfma_f32_16x16x32_bf16 v[112:115], a[16:19], v[16:19], v[112:115]\n"
"s_add_u32 m0, s55, 151552\n"
"ds_read_b64_tr_b16 a[36:37], v177 offset:36864\n"
"v_mul_f32 v172, v140, v172\n"
"v_mul_f32 v173, v141, v173\n"
"v_mul_f32 v174, v142, v174\n"
"v_mfma_f32_16x16x32_bf16 v[112:115], a[20:23], v[20:23], v[112:115]\n"
"ds_read_b64_tr_b16 a[38:39], v177 offset:38912\n"
"v_mul_f32 v175, v143, v175\n"
"v_accvgpr_write_b32 a79, v153\n"
"v_cvt_pk_bf16_f32 v156, v156, v157\n"
"v_mfma_f32_16x16x32_bf16 v[116:119], a[0:3], v[56:59], 0\n"
"s_nop 0\n"
"ds_read_b64_tr_b16 a[40:41], v177 offset:40960\n"
"ds_read_b64_tr_b16 a[42:43], v177 offset:43008\n"
"v_cvt_pk_bf16_f32 v157, v158, v159\n"
"v_accvgpr_write_b32 a120, v156\n"
"v_mfma_f32_16x16x32_bf16 v[116:119], a[4:7], v[60:63], v[116:119]\n"
"s_nop 0\n"
"buffer_load_dwordx4 v189, s[20:23], 0 offen lds\n"
"v_mfma_f32_16x16x32_bf16 v[116:119], a[8:11], v[64:67], v[116:119]\n"
"s_add_u32 m0, s55, 45056\n"
"ds_read_b64_tr_b16 a[44:45], v177 offset:45056\n"
"v_accvgpr_write_b32 a121, v157\n"
"v_cvt_pk_bf16_f32 v160, v160, v161\n"
"v_cvt_pk_bf16_f32 v161, v162, v163\n"
"v_mfma_f32_16x16x32_bf16 v[116:119], a[12:15], v[68:71], v[116:119]\n"
"s_add_u32 s56, s56, s62\n"
"ds_read_b64_tr_b16 a[46:47], v177 offset:47104\n"
"ds_read_b64_tr_b16 a[48:49], v178 offset:24576\n"
"v_accvgpr_write_b32 a80, v160\n"
"v_accvgpr_write_b32 a81, v161\n"
"v_mfma_f32_16x16x32_bf16 v[116:119], a[16:19], v[72:75], v[116:119]\n"
"s_nop 0\n"
"buffer_load_dwordx4 v180, s[28:31], s56 offen lds\n"
"v_mfma_f32_16x16x32_bf16 v[116:119], a[20:23], v[76:79], v[116:119]\n"
"s_waitcnt lgkmcnt(9)\n"
"ds_read_b64_tr_b16 a[50:51], v178 offset:26624\n"
"v_cvt_pk_bf16_f32 v164, v164, v165\n"
"v_cvt_pk_bf16_f32 v165, v166, v167\n"
"v_accvgpr_write_b32 a122, v164\n"
"v_mfma_f32_16x16x32_bf16 v[120:123], a[24:27], v[0:3], 0\n"
"s_add_u32 m0, s55, 155648\n"
"ds_read_b64_tr_b16 a[52:53], v178 offset:28672\n"
"ds_read_b64_tr_b16 a[54:55], v178 offset:30720\n"
"v_accvgpr_write_b32 a123, v165\n"
"v_cvt_pk_bf16_f32 v168, v168, v169\n"
"v_mfma_f32_16x16x32_bf16 v[120:123], a[28:31], v[4:7], v[120:123]\n"
"s_waitcnt lgkmcnt(8)\n"
"buffer_load_dwordx4 v190, s[20:23], 0 offen lds\n"
"v_mfma_f32_16x16x32_bf16 v[120:123], a[32:35], v[8:11], v[120:123]\n"
"s_lshl_b32 s60, s54, 1\n"
"ds_read_b64_tr_b16 a[56:57], v178 offset:32768\n"
"s_add_u32 s60, s60, s57\n"
"s_lshl_b32 s60, s60, 6\n"
"s_add_u32 s60, s60, 159744\n"
"v_mfma_f32_16x16x32_bf16 v[120:123], a[36:39], v[12:15], v[120:123]\n"
"s_waitcnt lgkmcnt(5)\n"
"ds_read_b64_tr_b16 a[58:59], v178 offset:34816\n"
"v_lshl_add_u32 v189, v194, 2, s60\n"
"v_xor_b32 v190, 64, v189\n"
"v_cvt_pk_bf16_f32 v169, v170, v171\n"
"v_mfma_f32_16x16x32_bf16 v[120:123], a[40:43], v[16:19], v[120:123]\n"
"ds_read_b64_tr_b16 a[0:1], v178 offset:36864\n"
"v_accvgpr_write_b32 a82, v168\n"
"v_accvgpr_write_b32 a83, v169\n"
"v_cvt_pk_bf16_f32 v172, v172, v173\n"
"v_mfma_f32_16x16x32_bf16 v[120:123], a[44:47], v[20:23], v[120:123]\n"
"ds_read_b64_tr_b16 a[2:3], v178 offset:38912\n"
"v_cvt_pk_bf16_f32 v173, v174, v175\n"
"v_accvgpr_write_b32 a124, v172\n"
"v_cvt_pk_bf16_f32 v240, v248, v249\n"
"v_mfma_f32_16x16x32_bf16 v[124:127], a[24:27], v[56:59], 0\n"
"ds_read_b64_tr_b16 a[4:5], v178 offset:40960\n"
"v_accvgpr_write_b32 a125, v173\n"
"v_mov_b32 v241, 0\n"
"v_accvgpr_write_b32 a154, v232\n"
"v_mfma_f32_16x16x32_bf16 v[124:127], a[28:31], v[60:63], v[124:127]\n"
"s_add_u32 s69, s77, 4096\n"
"ds_read_b64_tr_b16 a[6:7], v178 offset:43008\n"
"buffer_store_dwordx4 a[76:79], v188, s[44:47], s69 offen sc1\n"
"v_accvgpr_write_b32 a155, v233\n"
"v_mfma_f32_16x16x32_bf16 v[124:127], a[32:35], v[64:67], v[124:127]\n"
"s_add_u32 s69, s77, 22528\n"
"ds_read_b64_tr_b16 a[8:9], v178 offset:45056\n"
"v_add_u32 v176, 49152, v176\n"
"v_accvgpr_write_b32 a156, v234\n"
"v_accvgpr_write_b32 a157, v235\n"
"v_mfma_f32_16x16x32_bf16 v[124:127], a[36:39], v[68:71], v[124:127]\n"
"ds_read_b64_tr_b16 a[10:11], v178 offset:47104\n"
"v_accvgpr_write_b32 a158, v236\n"
"v_accvgpr_write_b32 a159, v237\n"
"v_accvgpr_write_b32 a160, v238\n"
"v_mfma_f32_16x16x32_bf16 v[124:127], a[40:43], v[72:75], v[124:127]\n"
"109:\n"
"s_cbranch_execz 113f\n"
"ds_read_b64_tr_b16 a[12:13], v179 offset:24576\n"
"buffer_store_dwordx4 a[118:121], v188, s[44:47], s69 offen sc1\n"
"v_accvgpr_write_b32 a161, v239\n"
"v_mfma_f32_16x16x32_bf16 v[124:127], a[44:47], v[76:79], v[124:127]\n"
"s_waitcnt lgkmcnt(9)\n"
"ds_read_b64_tr_b16 a[14:15], v179 offset:26624\n"
"v_accvgpr_write_b32 a162, v240\n"
"v_accvgpr_write_b32 a163, v241\n"
"v_mfma_f32_16x16x32_bf16 v[128:131], a[48:51], v[0:3], 0\n"
"s_add_u32 s69, s77, 5120\n"
"ds_read_b64_tr_b16 a[16:17], v179 offset:28672\n"
"ds_read_b64_tr_b16 a[18:19], v179 offset:30720\n"
"v_mfma_f32_16x16x32_bf16 v[128:131], a[52:55], v[4:7], v[128:131]\n"
"s_waitcnt lgkmcnt(8)\n"
"ds_read_b64_tr_b16 a[20:21], v179 offset:32768\n"
"ds_read_b64_tr_b16 a[22:23], v179 offset:34816\n"
"v_mfma_f32_16x16x32_bf16 v[128:131], a[56:59], v[8:11], v[128:131]\n"
"s_nop 0\n"
"ds_read_b64_tr_b16 a[24:25], v179 offset:36864\n"
"ds_read_b64_tr_b16 a[26:27], v179 offset:38912\n"
"v_mfma_f32_16x16x32_bf16 v[128:131], a[0:3], v[12:15], v[128:131]\n"
"s_waitcnt lgkmcnt(8)\n"
"ds_read_b64_tr_b16 a[28:29], v179 offset:40960\n"
"ds_read_b64_tr_b16 a[30:31], v179 offset:43008\n"
"v_mfma_f32_16x16x32_bf16 v[128:131], a[4:7], v[16:19], v[128:131]\n"
"s_nop 0\n"
"ds_read_b64_tr_b16 a[32:33], v179 offset:45056\n"
"ds_read_b64_tr_b16 a[34:35], v179 offset:47104\n"
"v_mfma_f32_16x16x32_bf16 v[128:131], a[8:11], v[20:23], v[128:131]\n"
"s_nop 0\n"
"buffer_store_dwordx4 a[80:83], v188, s[44:47], s69 offen sc1\n"
"v_mfma_f32_16x16x32_bf16 v[132:135], a[48:51], v[56:59], 0\n"
"s_add_u32 s69, s77, 23552\n"
"v_add_u32 v177, 49152, v177\n"
"v_mfma_f32_16x16x32_bf16 v[132:135], a[52:55], v[60:63], v[132:135]\n"
"v_mfma_f32_16x16x32_bf16 v[132:135], a[56:59], v[64:67], v[132:135]\n"
"v_mfma_f32_16x16x32_bf16 v[132:135], a[0:3], v[68:71], v[132:135]\n"
"s_waitcnt lgkmcnt(0)\n"
"v_mfma_f32_16x16x32_bf16 v[132:135], a[4:7], v[72:75], v[132:135]\n"
"s_barrier\n"
"ds_read_b64_tr_b16 a[36:37], v176 offset:0\n"
"ds_read_b64_tr_b16 a[38:39], v176 offset:2048\n"
"v_mfma_f32_16x16x32_bf16 v[132:135], a[8:11], v[76:79], v[132:135]\n"
"s_nop 0\n"
"ds_read_b64_tr_b16 a[40:41], v176 offset:4096\n"
"ds_read_b64_tr_b16 a[42:43], v176 offset:6144\n"
"v_mfma_f32_16x16x32_bf16 v[136:139], a[12:15], v[0:3], 0\n"
"s_nop 0\n"
"ds_read_b64_tr_b16 a[44:45], v176 offset:8192\n"
"ds_read_b64_tr_b16 a[46:47], v176 offset:10240\n"
"v_mfma_f32_16x16x32_bf16 v[136:139], a[16:19], v[4:7], v[136:139]\n"
"s_nop 0\n"
"buffer_store_dwordx4 a[122:125], v188, s[44:47], s69 offen sc1\n"
"v_mfma_f32_16x16x32_bf16 v[136:139], a[20:23], v[8:11], v[136:139]\n"
"s_nop 0\n"
"ds_read_b64_tr_b16 a[48:49], v176 offset:12288\n"
"ds_read_b64_tr_b16 a[50:51], v176 offset:14336\n"
"v_mfma_f32_16x16x32_bf16 v[136:139], a[24:27], v[12:15], v[136:139]\n"
"s_nop 0\n"
"ds_read_b64_tr_b16 a[52:53], v176 offset:16384\n"
"ds_read_b64_tr_b16 a[54:55], v176 offset:18432\n"
"v_mfma_f32_16x16x32_bf16 v[136:139], a[28:31], v[16:19], v[136:139]\n"
"s_add_u32 m0, s55, 49152\n"
"s_mov_b32 s56, 3539328\n"
"ds_read_b64_tr_b16 a[56:57], v176 offset:20480\n"
"v_mfma_f32_16x16x32_bf16 v[136:139], a[32:35], v[20:23], v[136:139]\n"
"s_nop 0\n"
"buffer_load_dwordx4 v180, s[28:31], s56 offen lds\n"
"v_mfma_f32_16x16x32_bf16 v[140:143], a[12:15], v[56:59], 0\n"
"v_add_u32 v178, 49152, v178\n"
"v_mfma_f32_16x16x32_bf16 v[140:143], a[16:19], v[60:63], v[140:143]\n"
"s_nop 0\n"
"ds_read_b64_tr_b16 a[58:59], v176 offset:22528\n"
"ds_read_b64_tr_b16 a[0:1], v177 offset:0\n"
"v_mfma_f32_16x16x32_bf16 v[140:143], a[20:23], v[64:67], v[140:143]\n"
"s_nop 0\n"
"ds_read_b64_tr_b16 a[2:3], v177 offset:2048\n"
"ds_read_b64_tr_b16 a[4:5], v177 offset:4096\n"
"v_mfma_f32_16x16x32_bf16 v[140:143], a[24:27], v[68:71], v[140:143]\n"
"s_addk_i32 m0, 4096\n"
"s_add_u32 s56, s56, s62\n"
"ds_read_b64_tr_b16 a[6:7], v177 offset:6144\n"
"v_mfma_f32_16x16x32_bf16 v[140:143], a[28:31], v[72:75], v[140:143]\n"
"s_nop 0\n"
"buffer_load_dwordx4 v180, s[28:31], s56 offen lds\n"
"v_mfma_f32_16x16x32_bf16 v[140:143], a[32:35], v[76:79], v[140:143]\n"
"s_waitcnt lgkmcnt(12)\n"
"ds_read_b64_tr_b16 a[8:9], v177 offset:8192\n"
"ds_read_b64_tr_b16 a[10:11], v177 offset:10240\n"
"v_mfma_f32_16x16x32_bf16 v[112:115], a[36:39], v[24:27], v[112:115]\n"
"s_nop 0\n"
"ds_read_b64_tr_b16 a[12:13], v177 offset:12288\n"
"ds_read_b64_tr_b16 a[14:15], v177 offset:14336\n"
"v_mfma_f32_16x16x32_bf16 v[112:115], a[40:43], v[28:31], v[112:115]\n"
"s_waitcnt lgkmcnt(12)\n"
"ds_read_b64_tr_b16 a[16:17], v177 offset:16384\n"
"ds_read_b64_tr_b16 a[18:19], v177 offset:18432\n"
"v_mfma_f32_16x16x32_bf16 v[112:115], a[44:47], v[32:35], v[112:115]\n"
"s_addk_i32 m0, 4096\n"
"s_add_u32 s56, s56, s62\n"
"ds_read_b64_tr_b16 a[20:21], v177 offset:20480\n"
"v_mfma_f32_16x16x32_bf16 v[112:115], a[48:51], v[36:39], v[112:115]\n"
"s_waitcnt lgkmcnt(11)\n"
"buffer_load_dwordx4 v180, s[28:31], s56 offen lds\n"
"v_mfma_f32_16x16x32_bf16 v[112:115], a[52:55], v[40:43], v[112:115]\n"
"v_add_u32 v179, 49152, v179\n"
"v_mfma_f32_16x16x32_bf16 v[112:115], a[56:59], v[44:47], v[112:115]\n"
"s_nop 0\n"
"ds_read_b64_tr_b16 a[22:23], v177 offset:22528\n"
"ds_read_b64_tr_b16 a[24:25], v178 offset:0\n"
"v_mfma_f32_16x16x32_bf16 v[116:119], a[36:39], v[80:83], v[116:119]\n"
"s_nop 0\n"
"ds_read_b64_tr_b16 a[26:27], v178 offset:2048\n"
"ds_read_b64_tr_b16 a[28:29], v178 offset:4096\n"
"v_mfma_f32_16x16x32_bf16 v[116:119], a[40:43], v[84:87], v[116:119]\n"
"s_addk_i32 m0, 4096\n"
"s_add_u32 s56, s56, s62\n"
"ds_read_b64_tr_b16 a[30:31], v178 offset:6144\n"
"v_mfma_f32_16x16x32_bf16 v[116:119], a[44:47], v[88:91], v[116:119]\n"
"s_nop 0\n"
"buffer_load_dwordx4 v180, s[28:31], s56 offen lds\n"
"v_mfma_f32_16x16x32_bf16 v[116:119], a[48:51], v[92:95], v[116:119]\n"
"s_nop 0\n"
"ds_read_b64_tr_b16 a[32:33], v178 offset:8192\n"
"ds_read_b64_tr_b16 a[34:35], v178 offset:10240\n"
"v_mfma_f32_16x16x32_bf16 v[116:119], a[52:55], v[96:99], v[116:119]\n"
"s_nop 0\n"
"ds_read_b64_tr_b16 a[36:37], v178 offset:12288\n"
"ds_read_b64_tr_b16 a[38:39], v178 offset:14336\n"
"v_mfma_f32_16x16x32_bf16 v[116:119], a[56:59], v[100:103], v[116:119]\n"
"s_waitcnt lgkmcnt(15)\n"
"ds_read_b64_tr_b16 a[40:41], v178 offset:16384\n"
"ds_read_b64_tr_b16 a[42:43], v178 offset:18432\n"
"v_mfma_f32_16x16x32_bf16 v[120:123], a[0:3], v[24:27], v[120:123]\n"
"s_addk_i32 m0, 4096\n"
"s_add_u32 s56, s56, s62\n"
"ds_read_b64_tr_b16 a[44:45], v178 offset:20480\n"
"v_mfma_f32_16x16x32_bf16 v[120:123], a[4:7], v[28:31], v[120:123]\n"
"s_waitcnt lgkmcnt(15)\n"
"buffer_load_dwordx4 v180, s[28:31], s56 offen lds\n"
"v_mfma_f32_16x16x32_bf16 v[120:123], a[8:11], v[32:35], v[120:123]\n"
"s_nop 0\n"
"ds_read_b64_tr_b16 a[46:47], v178 offset:22528\n"
"ds_read_b64_tr_b16 a[48:49], v179 offset:0\n"
"v_mfma_f32_16x16x32_bf16 v[120:123], a[12:15], v[36:39], v[120:123]\n"
"s_waitcnt lgkmcnt(13)\n"
"ds_read_b64_tr_b16 a[50:51], v179 offset:2048\n"
"v_mfma_f32_16x16x32_bf16 v[120:123], a[16:19], v[40:43], v[120:123]\n"
"ds_read_b64_tr_b16 a[52:53], v179 offset:4096\n"
"v_mfma_f32_16x16x32_bf16 v[120:123], a[20:23], v[44:47], v[120:123]\n"
"s_addk_i32 m0, 4096\n"
"s_add_u32 s56, s56, s62\n"
"ds_read_b64_tr_b16 a[54:55], v179 offset:6144\n"
"v_mfma_f32_16x16x32_bf16 v[124:127], a[0:3], v[80:83], v[124:127]\n"
"s_nop 0\n"
"buffer_load_dwordx4 v180, s[28:31], s56 offen lds\n"
"v_mfma_f32_16x16x32_bf16 v[124:127], a[4:7], v[84:87], v[124:127]\n"
"v_accvgpr_read_b32 v183, a254\n"
"v_accvgpr_read_b32 v180, a243\n"
"v_mfma_f32_16x16x32_bf16 v[124:127], a[8:11], v[88:91], v[124:127]\n"
"v_accvgpr_read_b32 v181, a244\n"
"v_mfma_f32_16x16x32_bf16 v[124:127], a[12:15], v[92:95], v[124:127]\n"
"s_nop 0\n"
"ds_read_b64_tr_b16 a[56:57], v179 offset:8192\n"
"ds_read_b64_tr_b16 a[58:59], v179 offset:10240\n"
"v_mfma_f32_16x16x32_bf16 v[124:127], a[16:19], v[96:99], v[124:127]\n"
"s_addk_i32 m0, 4096\n"
"s_add_u32 s56, s70, 0\n"
"ds_read_b64_tr_b16 a[0:1], v179 offset:12288\n"
"v_mfma_f32_16x16x32_bf16 v[124:127], a[20:23], v[100:103], v[124:127]\n"
"s_waitcnt lgkmcnt(15)\n"
"buffer_load_dwordx4 v183, s[36:39], s56 offen lds\n"
"v_mfma_f32_16x16x32_bf16 v[128:131], a[24:27], v[24:27], v[128:131]\n"
"s_nop 0\n"
"ds_read_b64_tr_b16 a[2:3], v179 offset:14336\n"
"ds_read_b64_tr_b16 a[4:5], v179 offset:16384\n"
"v_mfma_f32_16x16x32_bf16 v[128:131], a[28:31], v[28:31], v[128:131]\n"
"s_waitcnt lgkmcnt(13)\n"
"ds_read_b64_tr_b16 a[6:7], v179 offset:18432\n"
"ds_read_b64_tr_b16 a[8:9], v179 offset:20480\n"
"v_mfma_f32_16x16x32_bf16 v[128:131], a[32:35], v[32:35], v[128:131]\n"
"s_nop 0\n"
"ds_read_b64_tr_b16 a[10:11], v179 offset:22528\n"
"ds_read_b64_tr_b16 a[12:13], v176 offset:24576\n"
"v_mfma_f32_16x16x32_bf16 v[128:131], a[36:39], v[36:39], v[128:131]\n"
"s_waitcnt lgkmcnt(13)\n"
"s_addk_i32 m0, 4096\n"
"s_add_u32 s56, s70, 36864\n"
"v_mfma_f32_16x16x32_bf16 v[128:131], a[40:43], v[40:43], v[128:131]\n"
"s_nop 0\n"
"buffer_load_dwordx4 v183, s[36:39], s56 offen lds\n"
"v_mfma_f32_16x16x32_bf16 v[128:131], a[44:47], v[44:47], v[128:131]\n"
"s_nop 0\n"
"ds_read_b64_tr_b16 a[14:15], v176 offset:26624\n"
"ds_read_b64_tr_b16 a[16:17], v176 offset:28672\n"
"v_mfma_f32_16x16x32_bf16 v[132:135], a[24:27], v[80:83], v[132:135]\n"
"s_nop 0\n"
"ds_read_b64_tr_b16 a[18:19], v176 offset:30720\n"
"ds_read_b64_tr_b16 a[20:21], v176 offset:32768\n"
"v_mfma_f32_16x16x32_bf16 v[132:135], a[28:31], v[84:87], v[132:135]\n"
"ds_read_b64_tr_b16 a[22:23], v176 offset:34816\n"
"v_mfma_f32_16x16x32_bf16 v[132:135], a[32:35], v[88:91], v[132:135]\n"
"s_addk_i32 m0, 4096\n"
"s_add_u32 s56, s70, 73728\n"
"ds_read_b64_tr_b16 a[24:25], v176 offset:36864\n"
"v_mfma_f32_16x16x32_bf16 v[132:135], a[36:39], v[92:95], v[132:135]\n"
"ds_read_b64_tr_b16 a[26:27], v176 offset:38912\n"
"v_mfma_f32_16x16x32_bf16 v[132:135], a[40:43], v[96:99], v[132:135]\n"
"ds_read_b64_tr_b16 a[28:29], v176 offset:40960\n"
"v_mfma_f32_16x16x32_bf16 v[132:135], a[44:47], v[100:103], v[132:135]\n"
"s_waitcnt lgkmcnt(15)\n"
"ds_read_b64_tr_b16 a[30:31], v176 offset:43008\n"
"v_mfma_f32_16x16x32_bf16 v[136:139], a[48:51], v[24:27], v[136:139]\n"
"ds_read_b64_tr_b16 a[32:33], v176 offset:45056\n"
"v_mfma_f32_16x16x32_bf16 v[136:139], a[52:55], v[28:31], v[136:139]\n"
"s_waitcnt lgkmcnt(15)\n"
"ds_read_b64_tr_b16 a[34:35], v176 offset:47104\n"
"v_mfma_f32_16x16x32_bf16 v[136:139], a[56:59], v[32:35], v[136:139]\n"
"ds_read_b64_tr_b16 a[36:37], v177 offset:24576\n"
"v_mfma_f32_16x16x32_bf16 v[136:139], a[0:3], v[36:39], v[136:139]\n"
"s_waitcnt lgkmcnt(13)\n"
"ds_read_b64_tr_b16 a[38:39], v177 offset:26624\n"
"v_mfma_f32_16x16x32_bf16 v[136:139], a[4:7], v[40:43], v[136:139]\n"
"ds_read_b64_tr_b16 a[40:41], v177 offset:28672\n"
"v_mfma_f32_16x16x32_bf16 v[136:139], a[8:11], v[44:47], v[136:139]\n"
"ds_read_b64_tr_b16 a[42:43], v177 offset:30720\n"
"v_mfma_f32_16x16x32_bf16 v[140:143], a[48:51], v[80:83], v[140:143]\n"
"ds_read_b64_tr_b16 a[44:45], v177 offset:32768\n"
"v_mfma_f32_16x16x32_bf16 v[140:143], a[52:55], v[84:87], v[140:143]\n"
"s_nop 0\n"
"buffer_load_dwordx2 v[192:193], v191, s[44:47], s59 offen sc1\n"
"v_mfma_f32_16x16x32_bf16 v[140:143], a[56:59], v[88:91], v[140:143]\n"
"v_mul_f32 v185, v185, v185\n"
"v_mul_f32 v186, v186, v186\n"
"v_mfma_f32_16x16x32_bf16 v[140:143], a[0:3], v[92:95], v[140:143]\n"
"s_nop 0\n"
"ds_write_b32 v189, v185\n"
"v_mfma_f32_16x16x32_bf16 v[140:143], a[4:7], v[96:99], v[140:143]\n"
"s_nop 0\n"
"ds_write_b32 v190, v186\n"
"v_mfma_f32_16x16x32_bf16 v[140:143], a[8:11], v[100:103], v[140:143]\n"
"s_waitcnt lgkmcnt(15)\n"
"v_lshlrev_b32 v194, 4, v195\n"
"v_lshlrev_b32 v195, 3, v195\n"
"v_mfma_f32_16x16x32_bf16 v[112:115], a[12:15], v[48:51], v[112:115]\n"
"s_lshl_b32 s58, s53, 12\n"
"buffer_load_dwordx4 v183, s[36:39], s56 offen lds\n"
"v_mfma_f32_16x16x32_bf16 v[112:115], a[16:19], v[52:55], v[112:115]\n"
"s_waitcnt lgkmcnt(11)\n"
"s_add_u32 s58, s58, 299008\n"
"v_bfe_u32 v189, v187, 4, 1\n"
"v_mfma_f32_16x16x32_bf16 v[112:115], a[20:23], a[172:175], v[112:115]\n"
"s_addk_i32 m0, 4096\n"
"v_add_u32 v189, -1, v189\n"
"v_lshlrev_b32 v190, 2, v187\n"
"v_mfma_f32_16x16x32_bf16 v[112:115], a[24:27], a[176:179], v[112:115]\n"
"s_waitcnt lgkmcnt(7)\n"
"ds_read_b64_tr_b16 a[46:47], v177 offset:34816\n"
"s_add_u32 s56, s70, 110592\n"
"v_mfma_f32_16x16x32_bf16 v[112:115], a[28:31], a[180:183], v[112:115]\n"
"s_nop 0\n"
"ds_read_b64_tr_b16 a[48:49], v177 offset:36864\n"
"ds_read_b64_tr_b16 a[50:51], v177 offset:38912\n"
"v_mfma_f32_16x16x32_bf16 v[112:115], a[32:35], a[184:187], v[112:115]\n"
"s_waitcnt vmcnt(10)\n"
"s_add_u32 s79, s77, 37376\n"
"ds_read_b64_tr_b16 a[52:53], v177 offset:40960\n"
"v_mfma_f32_16x16x32_bf16 v[116:119], a[12:15], v[104:107], v[116:119]\n"
"s_nop 0\n"
"v_add_u32_e64 v199, s65, 0\n"
"buffer_store_dwordx2 v[198:199], v197, s[44:47], s79 offen offset:0 sc1\n"
"v_mfma_f32_16x16x32_bf16 v[116:119], a[16:19], v[108:111], v[116:119]\n"
"s_add_u32 s80, s78, 37376\n"
"ds_read_b64_tr_b16 a[54:55], v177 offset:43008\n"
"ds_read_b64_tr_b16 a[56:57], v177 offset:45056\n"
"v_mfma_f32_16x16x32_bf16 v[116:119], a[20:23], a[188:191], v[116:119]\n"
"s_nop 0\n"
"buffer_load_dwordx4 v183, s[36:39], s56 offen lds\n"
"v_mfma_f32_16x16x32_bf16 v[116:119], a[24:27], a[192:195], v[116:119]\n"
"s_addk_i32 m0, 4096\n"
"ds_read_b64_tr_b16 a[58:59], v177 offset:47104\n"
"v_max_f32 v112, 0, v112\n"
"v_mfma_f32_16x16x32_bf16 v[116:119], a[28:31], a[196:199], v[116:119]\n"
"s_add_u32 s56, s70, 147456\n"
"ds_read_b64_tr_b16 a[0:1], v178 offset:24576\n"
"ds_read_b64_tr_b16 a[2:3], v178 offset:26624\n"
"v_mfma_f32_16x16x32_bf16 v[116:119], a[32:35], a[200:203], v[116:119]\n"
"s_waitcnt lgkmcnt(12)\n"
"buffer_load_dwordx4 v183, s[36:39], s56 offen lds\n"
"v_mfma_f32_16x16x32_bf16 v[120:123], a[36:39], v[48:51], v[120:123]\n"
"s_addk_i32 m0, 4096\n"
"ds_read_b64_tr_b16 a[4:5], v178 offset:28672\n"
"v_max_f32 v113, 0, v113\n"
"v_mfma_f32_16x16x32_bf16 v[120:123], a[40:43], v[52:55], v[120:123]\n"
"s_waitcnt lgkmcnt(7)\n"
"ds_read_b64_tr_b16 a[6:7], v178 offset:30720\n"
"v_max_f32 v114, 0, v114\n"
"v_mfma_f32_16x16x32_bf16 v[120:123], a[44:47], a[172:175], v[120:123]\n"
"s_add_u32 s56, s70, 184320\n"
"ds_read_b64_tr_b16 a[8:9], v178 offset:32768\n"
"ds_read_b64_tr_b16 a[10:11], v178 offset:34816\n"
"v_mfma_f32_16x16x32_bf16 v[120:123], a[48:51], a[176:179], v[120:123]\n"
"s_waitcnt lgkmcnt(6)\n"
"buffer_load_dwordx4 v183, s[36:39], s56 offen lds\n"
"v_mfma_f32_16x16x32_bf16 v[120:123], a[52:55], a[180:183], v[120:123]\n"
"ds_read_b64_tr_b16 a[12:13], v178 offset:36864\n"
"v_max_f32 v115, 0, v115\n"
"v_mfma_f32_16x16x32_bf16 v[120:123], a[56:59], a[184:187], v[120:123]\n"
"ds_read_b64_tr_b16 a[14:15], v178 offset:38912\n"
"v_max_f32 v116, 0, v116\n"
"v_mfma_f32_16x16x32_bf16 v[124:127], a[36:39], v[104:107], v[124:127]\n"
"ds_read_b64_tr_b16 a[16:17], v178 offset:40960\n"
"v_max_f32 v117, 0, v117\n"
"v_mfma_f32_16x16x32_bf16 v[124:127], a[40:43], v[108:111], v[124:127]\n"
"ds_read_b64_tr_b16 a[18:19], v178 offset:43008\n"
"v_max_f32 v118, 0, v118\n"
"v_mfma_f32_16x16x32_bf16 v[124:127], a[44:47], a[188:191], v[124:127]\n"
"ds_read_b64_tr_b16 a[20:21], v178 offset:45056\n"
"v_add_u32 v176, 4294868992, v176\n"
"v_mfma_f32_16x16x32_bf16 v[124:127], a[48:51], a[192:195], v[124:127]\n"
"ds_read_b64_tr_b16 a[22:23], v178 offset:47104\n"
"v_max_f32 v119, 0, v119\n"
"v_mfma_f32_16x16x32_bf16 v[124:127], a[52:55], a[196:199], v[124:127]\n"
"ds_read_b64_tr_b16 a[24:25], v179 offset:24576\n"
"v_max_f32 v120, 0, v120\n"
"v_mfma_f32_16x16x32_bf16 v[124:127], a[56:59], a[200:203], v[124:127]\n"
"s_waitcnt lgkmcnt(9)\n"
"ds_read_b64_tr_b16 a[26:27], v179 offset:26624\n"
"v_max_f32 v121, 0, v121\n"
"v_mfma_f32_16x16x32_bf16 v[128:131], a[0:3], v[48:51], v[128:131]\n"
"s_nop 0\n"
"ds_read_b64_tr_b16 a[28:29], v179 offset:28672\n"
"ds_read_b64_tr_b16 a[30:31], v179 offset:30720\n"
"v_mfma_f32_16x16x32_bf16 v[128:131], a[4:7], v[52:55], v[128:131]\n"
"s_waitcnt lgkmcnt(8)\n"
"ds_read_b64_tr_b16 a[32:33], v179 offset:32768\n"
"ds_read_b64_tr_b16 a[34:35], v179 offset:34816\n"
"v_mfma_f32_16x16x32_bf16 v[128:131], a[8:11], a[172:175], v[128:131]\n"
"s_nop 0\n"
"ds_read_b64_tr_b16 a[36:37], v179 offset:36864\n"
"ds_read_b64_tr_b16 a[38:39], v179 offset:38912\n"
"v_mfma_f32_16x16x32_bf16 v[128:131], a[12:15], a[176:179], v[128:131]\n"
"s_waitcnt lgkmcnt(8)\n"
"ds_read_b64_tr_b16 a[40:41], v179 offset:40960\n"
"ds_read_b64_tr_b16 a[42:43], v179 offset:43008\n"
"v_mfma_f32_16x16x32_bf16 v[128:131], a[16:19], a[180:183], v[128:131]\n"
"s_nop 0\n"
"ds_read_b64_tr_b16 a[44:45], v179 offset:45056\n"
"ds_read_b64_tr_b16 a[46:47], v179 offset:47104\n"
"v_mfma_f32_16x16x32_bf16 v[128:131], a[20:23], a[184:187], v[128:131]\n"
"v_add_u32 v177, 4294868992, v177\n"
"v_max_f32 v122, 0, v122\n"
"v_mfma_f32_16x16x32_bf16 v[132:135], a[0:3], v[104:107], v[132:135]\n"
"v_max_f32 v123, 0, v123\n"
"v_max_f32 v124, 0, v124\n"
"v_mfma_f32_16x16x32_bf16 v[132:135], a[4:7], v[108:111], v[132:135]\n"
"v_max_f32 v125, 0, v125\n"
"v_max_f32 v126, 0, v126\n"
"v_mfma_f32_16x16x32_bf16 v[132:135], a[8:11], a[188:191], v[132:135]\n"
"v_max_f32 v127, 0, v127\n"
"v_max_f32 v128, 0, v128\n"
"v_mfma_f32_16x16x32_bf16 v[132:135], a[12:15], a[192:195], v[132:135]\n"
"s_waitcnt lgkmcnt(0)\n"
"v_max_f32 v129, 0, v129\n"
"v_max_f32 v130, 0, v130\n"
"v_mfma_f32_16x16x32_bf16 v[132:135], a[16:19], a[196:199], v[132:135]\n"
"s_barrier\n"
"ds_read_b64_tr_b16 a[48:49], v176 offset:0\n"
"v_max_f32 v131, 0, v131\n"
"v_mfma_f32_16x16x32_bf16 v[132:135], a[20:23], a[200:203], v[132:135]\n"
"s_nop 0\n"
"ds_read_b64_tr_b16 a[50:51], v176 offset:2048\n"
"ds_read_b64_tr_b16 a[52:53], v176 offset:4096\n"
"v_mfma_f32_16x16x32_bf16 v[136:139], a[24:27], v[48:51], v[136:139]\n"
"s_nop 0\n"
"ds_read_b64_tr_b16 a[54:55], v176 offset:6144\n"
"ds_read_b64_tr_b16 a[56:57], v176 offset:8192\n"
"v_mfma_f32_16x16x32_bf16 v[136:139], a[28:31], v[52:55], v[136:139]\n"
"v_max_f32 v132, 0, v132\n"
"v_max_f32 v133, 0, v133\n"
"v_mfma_f32_16x16x32_bf16 v[136:139], a[32:35], a[172:175], v[136:139]\n"
"v_max_f32 v134, 0, v134\n"
"v_max_f32 v135, 0, v135\n"
"v_mfma_f32_16x16x32_bf16 v[136:139], a[36:39], a[176:179], v[136:139]\n"
"s_nop 0\n"
"ds_read_b64_tr_b16 a[58:59], v176 offset:10240\n"
"ds_read_b64_tr_b16 a[0:1], v176 offset:12288\n"
"v_mfma_f32_16x16x32_bf16 v[136:139], a[40:43], a[180:183], v[136:139]\n"
"s_addk_i32 m0, 4096\n"
"s_add_u32 s56, s71, 0\n"
"ds_read_b64_tr_b16 a[2:3], v176 offset:14336\n"
"v_mfma_f32_16x16x32_bf16 v[136:139], a[44:47], a[184:187], v[136:139]\n"
"s_nop 0\n"
"buffer_load_dwordx4 v183, s[36:39], s56 offen lds\n"
"v_mfma_f32_16x16x32_bf16 v[140:143], a[24:27], v[104:107], v[140:143]\n"
"s_nop 0\n"
"ds_read_b64_tr_b16 a[4:5], v176 offset:16384\n"
"ds_read_b64_tr_b16 a[6:7], v176 offset:18432\n"
"v_mfma_f32_16x16x32_bf16 v[140:143], a[28:31], v[108:111], v[140:143]\n"
"ds_read_b64_tr_b16 a[8:9], v176 offset:20480\n"
"v_add_u32 v178, 4294868992, v178\n"
"v_mfma_f32_16x16x32_bf16 v[140:143], a[32:35], a[188:191], v[140:143]\n"
"v_max_f32 v136, 0, v136\n"
"v_max_f32 v137, 0, v137\n"
"v_mfma_f32_16x16x32_bf16 v[140:143], a[36:39], a[192:195], v[140:143]\n"
"s_addk_i32 m0, 4096\n"
"ds_read_b64_tr_b16 a[10:11], v176 offset:22528\n"
"v_max_f32 v138, 0, v138\n"
"v_mfma_f32_16x16x32_bf16 v[140:143], a[40:43], a[196:199], v[140:143]\n"
"s_add_u32 s56, s70, 221184\n"
"ds_read_b64_tr_b16 a[12:13], v177 offset:0\n"
"ds_read_b64_tr_b16 a[14:15], v177 offset:2048\n"
"v_mfma_f32_16x16x32_bf16 v[140:143], a[44:47], a[200:203], v[140:143]\n"
"s_waitcnt lgkmcnt(10)\n"
"buffer_load_dwordx4 v183, s[36:39], s56 offen lds\n"
"v_mfma_f32_16x16x32_bf16 v[144:147], a[48:51], v[0:3], 0\n"
"ds_read_b64_tr_b16 a[16:17], v177 offset:4096\n"
"v_max_f32 v139, 0, v139\n"
"v_mfma_f32_16x16x32_bf16 v[144:147], a[52:55], v[4:7], v[144:147]\n"
"s_waitcnt lgkmcnt(7)\n"
"ds_read_b64_tr_b16 a[18:19], v177 offset:6144\n"
"v_max_f32 v140, 0, v140\n"
"v_mfma_f32_16x16x32_bf16 v[144:147], a[56:59], v[8:11], v[144:147]\n"
"s_addk_i32 m0, 4096\n"
"ds_read_b64_tr_b16 a[20:21], v177 offset:8192\n"
"v_max_f32 v141, 0, v141\n"
"v_mfma_f32_16x16x32_bf16 v[144:147], a[0:3], v[12:15], v[144:147]\n"
"s_waitcnt lgkmcnt(5)\n"
"ds_read_b64_tr_b16 a[22:23], v177 offset:10240\n"
"v_max_f32 v142, 0, v142\n"
"v_mfma_f32_16x16x32_bf16 v[144:147], a[4:7], v[16:19], v[144:147]\n"
"s_add_u32 s56, s70, 258048\n"
"ds_read_b64_tr_b16 a[24:25], v177 offset:12288\n"
"ds_read_b64_tr_b16 a[26:27], v177 offset:14336\n"
"v_mfma_f32_16x16x32_bf16 v[144:147], a[8:11], v[20:23], v[144:147]\n"
"s_nop 0\n"
"buffer_load_dwordx4 v183, s[36:39], s56 offen lds\n"
"v_mfma_f32_16x16x32_bf16 v[148:151], a[48:51], v[56:59], 0\n"
"ds_read_b64_tr_b16 a[28:29], v177 offset:16384\n"
"v_max_f32 v143, 0, v143\n"
"v_mfma_f32_16x16x32_bf16 v[148:151], a[52:55], v[60:63], v[148:151]\n"
"s_addk_i32 m0, 4096\n"
"ds_read_b64_tr_b16 a[30:31], v177 offset:18432\n"
"s_add_u32 s56, s70, 128\n"
"v_mfma_f32_16x16x32_bf16 v[148:151], a[56:59], v[64:67], v[148:151]\n"
"s_nop 0\n"
"ds_read_b64_tr_b16 a[32:33], v177 offset:20480\n"
"ds_read_b64_tr_b16 a[34:35], v177 offset:22528\n"
"v_mfma_f32_16x16x32_bf16 v[148:151], a[0:3], v[68:71], v[148:151]\n"
"s_nop 0\n"
"buffer_load_dwordx4 v183, s[36:39], s56 offen lds\n"
"v_mfma_f32_16x16x32_bf16 v[148:151], a[4:7], v[72:75], v[148:151]\n"
"ds_read_b64_tr_b16 a[36:37], v178 offset:0\n"
"v_add_u32 v179, 4294868992, v179\n"
"v_mfma_f32_16x16x32_bf16 v[148:151], a[8:11], v[76:79], v[148:151]\n"
"s_waitcnt lgkmcnt(9)\n"
"ds_read_b64_tr_b16 a[38:39], v178 offset:2048\n"
"ds_read_b64_tr_b16 a[40:41], v178 offset:4096\n"
"v_mfma_f32_16x16x32_bf16 v[152:155], a[12:15], v[0:3], 0\n"
"s_addk_i32 m0, 4096\n"
"s_add_u32 s56, s70, 36992\n"
"ds_read_b64_tr_b16 a[42:43], v178 offset:6144\n"
"v_mfma_f32_16x16x32_bf16 v[152:155], a[16:19], v[4:7], v[152:155]\n"
"s_waitcnt lgkmcnt(8)\n"
"buffer_load_dwordx4 v183, s[36:39], s56 offen lds\n"
"v_mfma_f32_16x16x32_bf16 v[152:155], a[20:23], v[8:11], v[152:155]\n"
"s_waitcnt vmcnt(10)\n"
"ds_read_b64_tr_b16 a[44:45], v178 offset:8192\n"
"ds_read_b64_tr_b16 a[46:47], v178 offset:10240\n"
"v_mfma_f32_16x16x32_bf16 v[152:155], a[24:27], v[12:15], v[152:155]\n"
"s_waitcnt lgkmcnt(6)\n"
"v_cmp_ne_u64 vcc, s[66:67], v[192:193]\n"
"s_nop 1\n"
"s_cbranch_vccnz 56f\n"
"57:\n"
"v_mfma_f32_16x16x32_bf16 v[152:155], a[28:31], v[16:19], v[152:155]\n"
"s_nop 0\n"
"ds_read_b64_tr_b16 a[48:49], v178 offset:12288\n"
"ds_read_b64_tr_b16 a[50:51], v178 offset:14336\n"
"v_mfma_f32_16x16x32_bf16 v[152:155], a[32:35], v[20:23], v[152:155]\n"
"s_addk_i32 m0, 4096\n"
"buffer_load_dwordx4 a[204:207], v194, s[44:47], s58 offen offset:0 sc1\n"
"v_mfma_f32_16x16x32_bf16 v[156:159], a[12:15], v[56:59], 0\n"
"s_add_u32 s56, s70, 73856\n"
"ds_read_b64_tr_b16 a[52:53], v178 offset:16384\n"
"ds_read_b64_tr_b16 a[54:55], v178 offset:18432\n"
"v_mfma_f32_16x16x32_bf16 v[156:159], a[16:19], v[60:63], v[156:159]\n"
"s_nop 0\n"
"buffer_load_dwordx2 a[252:253], off, s[44:47], s80 sc1\n"
"v_mfma_f32_16x16x32_bf16 v[156:159], a[20:23], v[64:67], v[156:159]\n"
"s_nop 0\n"
"ds_read_b64_tr_b16 a[56:57], v178 offset:20480\n"
"ds_read_b64_tr_b16 a[58:59], v178 offset:22528\n"
"v_mfma_f32_16x16x32_bf16 v[156:159], a[24:27], v[68:71], v[156:159]\n"
"s_nop 0\n"
"buffer_load_dwordx4 a[214:217], v194, s[44:47], s58 offen offset:1024 sc1\n"
"v_mfma_f32_16x16x32_bf16 v[156:159], a[28:31], v[72:75], v[156:159]\n"
"s_nop 0\n"
"ds_read_b64_tr_b16 a[0:1], v179 offset:0\n"
"ds_read_b64_tr_b16 a[2:3], v179 offset:2048\n"
"v_mfma_f32_16x16x32_bf16 v[156:159], a[32:35], v[76:79], v[156:159]\n"
"s_waitcnt lgkmcnt(10)\n"
"buffer_load_dwordx4 a[224:227], v194, s[44:47], s58 offen offset:2048 sc1\n"
"v_mfma_f32_16x16x32_bf16 v[160:163], a[36:39], v[0:3], 0\n"
"s_nop 0\n"
"ds_read_b64_tr_b16 a[4:5], v179 offset:4096\n"
"ds_read_b64_tr_b16 a[6:7], v179 offset:6144\n"
"v_mfma_f32_16x16x32_bf16 v[160:163], a[40:43], v[4:7], v[160:163]\n"
"s_waitcnt lgkmcnt(8)\n"
"buffer_load_dwordx4 a[208:211], v194, s[44:47], s58 offen offset:512 sc1\n"
"v_mfma_f32_16x16x32_bf16 v[160:163], a[44:47], v[8:11], v[160:163]\n"
"s_nop 0\n"
"ds_read_b64_tr_b16 a[8:9], v179 offset:8192\n"
"ds_read_b64_tr_b16 a[10:11], v179 offset:10240\n"
"v_mfma_f32_16x16x32_bf16 v[160:163], a[48:51], v[12:15], v[160:163]\n"
"s_waitcnt lgkmcnt(6)\n"
"buffer_load_dwordx4 a[218:221], v194, s[44:47], s58 offen offset:1536 sc1\n"
"v_mfma_f32_16x16x32_bf16 v[160:163], a[52:55], v[16:19], v[160:163]\n"
"s_nop 0\n"
"ds_read_b64_tr_b16 a[12:13], v179 offset:12288\n"
"ds_read_b64_tr_b16 a[14:15], v179 offset:14336\n"
"v_mfma_f32_16x16x32_bf16 v[160:163], a[56:59], v[20:23], v[160:163]\n"
"s_nop 0\n"
"ds_read_b64_tr_b16 a[16:17], v179 offset:16384\n"
"ds_read_b64_tr_b16 a[18:19], v179 offset:18432\n"
"v_mfma_f32_16x16x32_bf16 v[164:167], a[36:39], v[56:59], 0\n"
"s_nop 0\n"
"ds_read_b64_tr_b16 a[20:21], v179 offset:20480\n"
"ds_read_b64_tr_b16 a[22:23], v179 offset:22528\n"
"v_mfma_f32_16x16x32_bf16 v[164:167], a[40:43], v[60:63], v[164:167]\n"
"s_nop 0\n"
"buffer_load_dwordx4 a[228:231], v194, s[44:47], s58 offen offset:2560 sc1\n"
"v_mfma_f32_16x16x32_bf16 v[164:167], a[44:47], v[64:67], v[164:167]\n"
"s_nop 0\n"
"ds_read_b64_tr_b16 a[24:25], v176 offset:24576\n"
"ds_read_b64_tr_b16 a[26:27], v176 offset:26624\n"
"v_mfma_f32_16x16x32_bf16 v[164:167], a[48:51], v[68:71], v[164:167]\n"
"s_nop 0\n"
"ds_read_b64_tr_b16 a[28:29], v176 offset:28672\n"
"ds_read_b64_tr_b16 a[30:31], v176 offset:30720\n"
"v_mfma_f32_16x16x32_bf16 v[164:167], a[52:55], v[72:75], v[164:167]\n"
"s_nop 0\n"
"ds_read_b64_tr_b16 a[32:33], v176 offset:32768\n"
"ds_read_b64_tr_b16 a[34:35], v176 offset:34816\n"
"v_mfma_f32_16x16x32_bf16 v[164:167], a[56:59], v[76:79], v[164:167]\n"
"s_waitcnt lgkmcnt(14)\n"
"buffer_load_dwordx2 a[212:213], v195, s[44:47], s58 offen offset:3072 sc1\n"
"v_mfma_f32_16x16x32_bf16 v[168:171], a[0:3], v[0:3], 0\n"
"s_nop 0\n"
"ds_read_b64_tr_b16 a[36:37], v176 offset:36864\n"
"ds_read_b64_tr_b16 a[38:39], v176 offset:38912\n"
"v_mfma_f32_16x16x32_bf16 v[168:171], a[4:7], v[4:7], v[168:171]\n"
"s_waitcnt lgkmcnt(12)\n"
"ds_read_b64_tr_b16 a[40:41], v176 offset:40960\n"
"ds_read_b64_tr_b16 a[42:43], v176 offset:43008\n"
"v_mfma_f32_16x16x32_bf16 v[168:171], a[8:11], v[8:11], v[168:171]\n"
"s_nop 0\n"
"ds_read_b64_tr_b16 a[44:45], v176 offset:45056\n"
"ds_read_b64_tr_b16 a[46:47], v176 offset:47104\n"
"v_mfma_f32_16x16x32_bf16 v[168:171], a[12:15], v[12:15], v[168:171]\n"
"s_waitcnt lgkmcnt(12)\n"
"buffer_load_dwordx2 a[222:223], v195, s[44:47], s58 offen offset:3328 sc1\n"
"v_mfma_f32_16x16x32_bf16 v[168:171], a[16:19], v[16:19], v[168:171]\n"
"s_nop 0\n"
"ds_read_b64_tr_b16 a[48:49], v177 offset:24576\n"
"ds_read_b64_tr_b16 a[50:51], v177 offset:26624\n"
"v_mfma_f32_16x16x32_bf16 v[168:171], a[20:23], v[20:23], v[168:171]\n"
"s_nop 0\n"
"ds_read_b64_tr_b16 a[52:53], v177 offset:28672\n"
"ds_read_b64_tr_b16 a[54:55], v177 offset:30720\n"
"v_mfma_f32_16x16x32_bf16 v[172:175], a[0:3], v[56:59], 0\n"
"ds_read_b64_tr_b16 a[56:57], v177 offset:32768\n"
"v_mfma_f32_16x16x32_bf16 v[172:175], a[4:7], v[60:63], v[172:175]\n"
"s_nop 0\n"
"buffer_load_dwordx2 a[232:233], v195, s[44:47], s58 offen offset:3584 sc1\n"
"v_mfma_f32_16x16x32_bf16 v[172:175], a[8:11], v[64:67], v[172:175]\n"
"s_nop 0\n"
"ds_read_b64_tr_b16 a[58:59], v177 offset:34816\n"
"ds_read_b64_tr_b16 a[0:1], v177 offset:36864\n"
"v_mfma_f32_16x16x32_bf16 v[172:175], a[12:15], v[68:71], v[172:175]\n"
"ds_read_b64_tr_b16 a[2:3], v177 offset:38912\n"
"v_mfma_f32_16x16x32_bf16 v[172:175], a[16:19], v[72:75], v[172:175]\n"
"ds_read_b64_tr_b16 a[4:5], v177 offset:40960\n"
"v_mfma_f32_16x16x32_bf16 v[172:175], a[20:23], v[76:79], v[172:175]\n"
"s_waitcnt lgkmcnt(15)\n"
"buffer_load_dwordx4 v183, s[36:39], s56 offen lds\n"
"v_mfma_f32_16x16x32_bf16 v[144:147], a[24:27], v[24:27], v[144:147]\n"
"s_addk_i32 m0, 4096\n"
"s_add_u32 s56, s70, 110720\n"
"ds_read_b64_tr_b16 a[6:7], v177 offset:43008\n"
"v_mfma_f32_16x16x32_bf16 v[144:147], a[28:31], v[28:31], v[144:147]\n"
"s_waitcnt lgkmcnt(14)\n"
"ds_read_b64_tr_b16 a[8:9], v177 offset:45056\n"
"ds_read_b64_tr_b16 a[10:11], v177 offset:47104\n"
"v_mfma_f32_16x16x32_bf16 v[144:147], a[32:35], v[32:35], v[144:147]\n"
"ds_read_b64_tr_b16 a[12:13], v178 offset:24576\n"
"v_mfma_f32_16x16x32_bf16 v[144:147], a[36:39], v[36:39], v[144:147]\n"
"s_waitcnt lgkmcnt(13)\n"
"buffer_load_dwordx4 v183, s[36:39], s56 offen lds\n"
"v_mfma_f32_16x16x32_bf16 v[144:147], a[40:43], v[40:43], v[144:147]\n"
"110:\n"
"s_cbranch_execz 114f\n"
"s_addk_i32 m0, 4096\n"
"s_add_u32 s56, s70, 147584\n"
"ds_read_b64_tr_b16 a[14:15], v178 offset:26624\n"
"v_mfma_f32_16x16x32_bf16 v[144:147], a[44:47], v[44:47], v[144:147]\n"
"s_nop 0\n"
"ds_read_b64_tr_b16 a[16:17], v178 offset:28672\n"
"ds_read_b64_tr_b16 a[18:19], v178 offset:30720\n"
"v_mfma_f32_16x16x32_bf16 v[148:151], a[24:27], v[80:83], v[148:151]\n"
"ds_read_b64_tr_b16 a[20:21], v178 offset:32768\n"
"v_mfma_f32_16x16x32_bf16 v[148:151], a[28:31], v[84:87], v[148:151]\n"
"s_nop 0\n"
"buffer_load_dwordx4 v183, s[36:39], s56 offen lds\n"
"v_mfma_f32_16x16x32_bf16 v[148:151], a[32:35], v[88:91], v[148:151]\n"
"s_addk_i32 m0, 4096\n"
"s_add_u32 s56, s70, 184448\n"
"ds_read_b64_tr_b16 a[22:23], v178 offset:34816\n"
"v_mfma_f32_16x16x32_bf16 v[148:151], a[36:39], v[92:95], v[148:151]\n"
"s_nop 0\n"
"ds_read_b64_tr_b16 a[24:25], v178 offset:36864\n"
"ds_read_b64_tr_b16 a[26:27], v178 offset:38912\n"
"v_mfma_f32_16x16x32_bf16 v[148:151], a[40:43], v[96:99], v[148:151]\n"
"ds_read_b64_tr_b16 a[28:29], v178 offset:40960\n"
"v_mfma_f32_16x16x32_bf16 v[148:151], a[44:47], v[100:103], v[148:151]\n"
"s_waitcnt lgkmcnt(15)\n"
"buffer_load_dwordx4 v183, s[36:39], s56 offen lds\n"
"v_mfma_f32_16x16x32_bf16 v[152:155], a[48:51], v[24:27], v[152:155]\n"
"s_addk_i32 m0, 4096\n"
"s_add_u32 s56, s71, 128\n"
"ds_read_b64_tr_b16 a[30:31], v178 offset:43008\n"
"v_mfma_f32_16x16x32_bf16 v[152:155], a[52:55], v[28:31], v[152:155]\n"
"s_waitcnt lgkmcnt(14)\n"
"ds_read_b64_tr_b16 a[32:33], v178 offset:45056\n"
"v_add_u32 v176, 49152, v176\n"
"v_mfma_f32_16x16x32_bf16 v[152:155], a[56:59], v[32:35], v[152:155]\n"
"s_nop 0\n"
"ds_read_b64_tr_b16 a[34:35], v178 offset:47104\n"
"ds_read_b64_tr_b16 a[36:37], v179 offset:24576\n"
"v_mfma_f32_16x16x32_bf16 v[152:155], a[0:3], v[36:39], v[152:155]\n"
"s_waitcnt lgkmcnt(13)\n"
"buffer_load_dwordx4 v183, s[36:39], s56 offen lds\n"
"v_mfma_f32_16x16x32_bf16 v[152:155], a[4:7], v[40:43], v[152:155]\n"
"s_addk_i32 m0, 4096\n"
"s_add_u32 s56, s70, 221312\n"
"ds_read_b64_tr_b16 a[38:39], v179 offset:26624\n"
"v_mfma_f32_16x16x32_bf16 v[152:155], a[8:11], v[44:47], v[152:155]\n"
"s_nop 0\n"
"ds_read_b64_tr_b16 a[40:41], v179 offset:28672\n"
"ds_read_b64_tr_b16 a[42:43], v179 offset:30720\n"
"v_mfma_f32_16x16x32_bf16 v[156:159], a[48:51], v[80:83], v[156:159]\n"
"s_nop 0\n"
"ds_read_b64_tr_b16 a[44:45], v179 offset:32768\n"
"ds_read_b64_tr_b16 a[46:47], v179 offset:34816\n"
"v_mfma_f32_16x16x32_bf16 v[156:159], a[52:55], v[84:87], v[156:159]\n"
"s_nop 0\n"
"ds_read_b64_tr_b16 a[48:49], v179 offset:36864\n"
"ds_read_b64_tr_b16 a[50:51], v179 offset:38912\n"
"v_mfma_f32_16x16x32_bf16 v[156:159], a[56:59], v[88:91], v[156:159]\n"
"s_nop 0\n"
"ds_read_b64_tr_b16 a[52:53], v179 offset:40960\n"
"ds_read_b64_tr_b16 a[54:55], v179 offset:43008\n"
"v_mfma_f32_16x16x32_bf16 v[156:159], a[0:3], v[92:95], v[156:159]\n"
"ds_read_b64_tr_b16 a[56:57], v179 offset:45056\n"
"v_add_u32 v177, 49152, v177\n"
"v_mfma_f32_16x16x32_bf16 v[156:159], a[4:7], v[96:99], v[156:159]\n"
"ds_read_b64_tr_b16 a[58:59], v179 offset:47104\n"
"v_mfma_f32_16x16x32_bf16 v[156:159], a[8:11], v[100:103], v[156:159]\n"
"s_waitcnt lgkmcnt(15)\n"
"buffer_load_dwordx4 v183, s[36:39], s56 offen lds\n"
"v_mfma_f32_16x16x32_bf16 v[160:163], a[12:15], v[24:27], v[160:163]\n"
"s_addk_i32 m0, 4096\n"
"s_add_u32 s56, s70, 258176\n"
"v_mfma_f32_16x16x32_bf16 v[160:163], a[16:19], v[28:31], v[160:163]\n"
"v_mfma_f32_16x16x32_bf16 v[160:163], a[20:23], v[32:35], v[160:163]\n"
"v_mfma_f32_16x16x32_bf16 v[160:163], a[24:27], v[36:39], v[160:163]\n"
"s_waitcnt lgkmcnt(12)\n"
"v_mfma_f32_16x16x32_bf16 v[160:163], a[28:31], v[40:43], v[160:163]\n"
"v_mfma_f32_16x16x32_bf16 v[160:163], a[32:35], v[44:47], v[160:163]\n"
"v_mfma_f32_16x16x32_bf16 v[164:167], a[12:15], v[80:83], v[164:167]\n"
"s_nop 0\n"
"buffer_load_dwordx4 v183, s[36:39], s56 offen lds\n"
"v_mfma_f32_16x16x32_bf16 v[164:167], a[16:19], v[84:87], v[164:167]\n"
"v_mfma_f32_16x16x32_bf16 v[164:167], a[20:23], v[88:91], v[164:167]\n"
"v_mfma_f32_16x16x32_bf16 v[164:167], a[24:27], v[92:95], v[164:167]\n"
"s_waitcnt vmcnt(23) lgkmcnt(0)\n"
"s_waitcnt vmcnt(15)\n"
"v_mfma_f32_16x16x32_bf16 v[164:167], a[28:31], v[96:99], v[164:167]\n"
"s_barrier\n"
"ds_read_b64_tr_b16 a[0:1], v176 offset:0\n"
"ds_read_b64_tr_b16 a[2:3], v176 offset:2048\n"
"v_mfma_f32_16x16x32_bf16 v[164:167], a[32:35], v[100:103], v[164:167]\n"
"s_nop 0\n"
"v_accvgpr_read_b32 v203, a252\n"
"v_accvgpr_read_b32 v199, a253\n"
"v_xor_b32 v203, s64, v203\n"
"v_xor_b32 v199, s65, v199\n"
"v_or_b32 v203, v203, v199\n"
"v_cmp_ne_u32 vcc, 0, v203\n"
"s_nop 1\n"
"s_cbranch_vccnz 59f\n"
"60:\n"
"v_mfma_f32_16x16x32_bf16 v[168:171], a[36:39], v[24:27], v[168:171]\n"
"s_add_u32 s6, s78, 18432\n"
"ds_read_b64_tr_b16 a[4:5], v176 offset:4096\n"
"ds_read_b64_tr_b16 a[6:7], v176 offset:6144\n"
"v_mfma_f32_16x16x32_bf16 v[168:171], a[40:43], v[28:31], v[168:171]\n"
"s_nop 0\n"
"buffer_load_dwordx4 v[0:3], v188, s[44:47], s6 offen sc1\n"
"v_mfma_f32_16x16x32_bf16 v[168:171], a[44:47], v[32:35], v[168:171]\n"
"s_add_u32 s6, s78, 0\n"
"ds_read_b64_tr_b16 a[8:9], v176 offset:8192\n"
"ds_read_b64_tr_b16 a[10:11], v176 offset:10240\n"
"v_mfma_f32_16x16x32_bf16 v[168:171], a[48:51], v[36:39], v[168:171]\n"
"s_nop 0\n"
"ds_read_b64_tr_b16 a[12:13], v176 offset:12288\n"
"ds_read_b64_tr_b16 a[14:15], v176 offset:14336\n"
"v_mfma_f32_16x16x32_bf16 v[168:171], a[52:55], v[40:43], v[168:171]\n"
"s_add_u32 m0, s55, 0\n"
"s_add_u32 s56, s72, 0\n"
"ds_read_b64_tr_b16 a[16:17], v176 offset:16384\n"
"v_mfma_f32_16x16x32_bf16 v[168:171], a[56:59], v[44:47], v[168:171]\n"
"s_nop 0\n"
"ds_read_b64_tr_b16 a[18:19], v176 offset:18432\n"
"ds_read_b64_tr_b16 a[20:21], v176 offset:20480\n"
"v_mfma_f32_16x16x32_bf16 v[172:175], a[36:39], v[80:83], v[172:175]\n"
"v_add_u32 v178, 49152, v178\n"
"v_mfma_f32_16x16x32_bf16 v[172:175], a[40:43], v[84:87], v[172:175]\n"
"s_nop 0\n"
"ds_read_b64_tr_b16 a[22:23], v176 offset:22528\n"
"ds_read_b64_tr_b16 a[24:25], v177 offset:0\n"
"v_mfma_f32_16x16x32_bf16 v[172:175], a[44:47], v[88:91], v[172:175]\n"
"s_nop 0\n"
"ds_read_b64_tr_b16 a[26:27], v177 offset:2048\n"
"ds_read_b64_tr_b16 a[28:29], v177 offset:4096\n"
"v_mfma_f32_16x16x32_bf16 v[172:175], a[48:51], v[92:95], v[172:175]\n"
"s_nop 0\n"
"ds_read_b64_tr_b16 a[30:31], v177 offset:6144\n"
"ds_read_b64_tr_b16 a[32:33], v177 offset:8192\n"
"v_mfma_f32_16x16x32_bf16 v[172:175], a[52:55], v[96:99], v[172:175]\n"
"s_nop 0\n"
"ds_read_b64_tr_b16 a[34:35], v177 offset:10240\n"
"ds_read_b64_tr_b16 a[36:37], v177 offset:12288\n"
"v_mfma_f32_16x16x32_bf16 v[172:175], a[56:59], v[100:103], v[172:175]\n"
"s_waitcnt lgkmcnt(15)\n"
"ds_read_b64_tr_b16 a[38:39], v177 offset:14336\n"
"ds_read_b64_tr_b16 a[40:41], v177 offset:16384\n"
"v_mfma_f32_16x16x32_bf16 v[144:147], a[0:3], v[48:51], v[144:147]\n"
"v_accvgpr_read_b32 v196, a245\n"
"v_mfma_f32_16x16x32_bf16 v[144:147], a[4:7], v[52:55], v[144:147]\n"
"s_waitcnt lgkmcnt(13)\n"
"ds_read_b64_tr_b16 a[42:43], v177 offset:18432\n"
"ds_read_b64_tr_b16 a[44:45], v177 offset:20480\n"
"v_mfma_f32_16x16x32_bf16 v[144:147], a[8:11], a[172:175], v[144:147]\n"
"v_add_u32 v179, 49152, v179\n"
"v_mfma_f32_16x16x32_bf16 v[144:147], a[12:15], a[176:179], v[144:147]\n"
"s_waitcnt lgkmcnt(11)\n"
"buffer_load_dwordx4 v[56:59], v188, s[44:47], s6 offen sc1\n"
"v_mfma_f32_16x16x32_bf16 v[144:147], a[16:19], a[180:183], v[144:147]\n"
"s_add_u32 s6, s78, 19456\n"
"ds_read_b64_tr_b16 a[46:47], v177 offset:22528\n"
"ds_read_b64_tr_b16 a[48:49], v178 offset:0\n"
"v_mfma_f32_16x16x32_bf16 v[144:147], a[20:23], a[184:187], v[144:147]\n"
"s_nop 0\n"
"ds_read_b64_tr_b16 a[50:51], v178 offset:2048\n"
"ds_read_b64_tr_b16 a[52:53], v178 offset:4096\n"
"v_mfma_f32_16x16x32_bf16 v[148:151], a[0:3], v[104:107], v[148:151]\n"
"s_nop 0\n"
"ds_read_b64_tr_b16 a[54:55], v178 offset:6144\n"
"ds_read_b64_tr_b16 a[56:57], v178 offset:8192\n"
"v_mfma_f32_16x16x32_bf16 v[148:151], a[4:7], v[108:111], v[148:151]\n"
"v_mul_f32 v144, v112, v144\n"
"v_mul_f32 v145, v113, v145\n"
"v_mul_f32 v146, v114, v146\n"
"v_mfma_f32_16x16x32_bf16 v[148:151], a[8:11], a[188:191], v[148:151]\n"
"s_nop 0\n"
"buffer_load_dwordx4 v[4:7], v188, s[44:47], s6 offen sc1\n"
"v_mfma_f32_16x16x32_bf16 v[148:151], a[12:15], a[192:195], v[148:151]\n"
"s_add_u32 s6, s78, 1024\n"
"v_mul_f32 v147, v115, v147\n"
"v_cvt_pk_bf16_f32 v144, v144, v145\n"
"v_mfma_f32_16x16x32_bf16 v[148:151], a[16:19], a[196:199], v[148:151]\n"
"v_cvt_pk_bf16_f32 v145, v146, v147\n"
"v_accvgpr_write_b32 a84, v144\n"
"v_mfma_f32_16x16x32_bf16 v[148:151], a[20:23], a[200:203], v[148:151]\n"
"s_waitcnt lgkmcnt(13)\n"
"v_accvgpr_write_b32 a85, v145\n"
"v_mfma_f32_16x16x32_bf16 v[152:155], a[24:27], v[48:51], v[152:155]\n"
"s_nop 0\n"
"buffer_load_dwordx4 v[60:63], v188, s[44:47], s6 offen sc1\n"
"v_mfma_f32_16x16x32_bf16 v[152:155], a[28:31], v[52:55], v[152:155]\n"
"s_waitcnt lgkmcnt(9)\n"
"s_waitcnt vmcnt(11)\n"
"v_mul_f32 v148, v116, v148\n"
"v_mul_f32 v149, v117, v149\n"
"v_mfma_f32_16x16x32_bf16 v[152:155], a[32:35], a[172:175], v[152:155]\n"
"s_add_u32 s6, s78, 20480\n"
"v_mul_f32 v150, v118, v150\n"
"v_mul_f32 v151, v119, v151\n"
"v_mfma_f32_16x16x32_bf16 v[152:155], a[36:39], a[176:179], v[152:155]\n"
"s_waitcnt lgkmcnt(5)\n"
"ds_read_b64_tr_b16 a[58:59], v178 offset:10240\n"
"v_cvt_pk_bf16_f32 v148, v148, v149\n"
"v_mfma_f32_16x16x32_bf16 v[152:155], a[40:43], a[180:183], v[152:155]\n"
"ds_read_b64_tr_b16 a[0:1], v178 offset:12288\n"
"v_cvt_pk_bf16_f32 v149, v150, v151\n"
"v_mfma_f32_16x16x32_bf16 v[152:155], a[44:47], a[184:187], v[152:155]\n"
"ds_read_b64_tr_b16 a[2:3], v178 offset:14336\n"
"v_accvgpr_write_b32 a126, v148\n"
"v_accvgpr_write_b32 a127, v149\n"
"v_mfma_f32_16x16x32_bf16 v[216:219], a[204:207], a[144:147], 0\n"
"s_nop 0\n"
"buffer_load_dwordx4 v[8:11], v188, s[44:47], s6 offen sc1\n"
"v_mfma_f32_16x16x32_bf16 v[220:223], a[214:217], a[144:147], 0\n"
"s_add_u32 s6, s78, 2048\n"
"v_mul_f32 v152, v120, v152\n"
"v_mul_f32 v153, v121, v153\n"
"v_mul_f32 v154, v122, v154\n"
"v_mfma_f32_16x16x32_bf16 v[224:227], a[224:227], a[144:147], 0\n"
"v_mul_f32 v155, v123, v155\n"
"v_cvt_pk_bf16_f32 v152, v152, v153\n"
"v_cvt_pk_bf16_f32 v153, v154, v155\n"
"v_mfma_f32_16x16x32_bf16 v[228:231], a[204:207], a[154:157], 0\n"
"v_accvgpr_write_b32 a86, v152\n"
"v_accvgpr_write_b32 a87, v153\n"
"v_mfma_f32_16x16x32_bf16 v[232:235], a[214:217], a[154:157], 0\n"
"s_add_u32 s69, s77, 6144\n"
"buffer_store_dwordx4 a[84:87], v188, s[44:47], s69 offen sc1\n"
"v_mfma_f32_16x16x32_bf16 v[236:239], a[224:227], a[154:157], 0\n"
"s_nop 0\n"
"buffer_load_dwordx4 v[64:67], v188, s[44:47], s6 offen sc1\n"
"v_mfma_f32_16x16x32_bf16 v[156:159], a[24:27], v[104:107], v[156:159]\n"
"s_add_u32 s6, s78, 21504\n"
"ds_read_b64_tr_b16 a[4:5], v178 offset:16384\n"
"ds_read_b64_tr_b16 a[6:7], v178 offset:18432\n"
"v_mfma_f32_16x16x32_bf16 v[156:159], a[28:31], v[108:111], v[156:159]\n"
"s_nop 0\n"
"ds_read_b64_tr_b16 a[8:9], v178 offset:20480\n"
"ds_read_b64_tr_b16 a[10:11], v178 offset:22528\n"
"v_mfma_f32_16x16x32_bf16 v[156:159], a[32:35], a[188:191], v[156:159]\n"
"s_nop 0\n"
"ds_read_b64_tr_b16 a[12:13], v179 offset:0\n"
"ds_read_b64_tr_b16 a[14:15], v179 offset:2048\n"
"v_mfma_f32_16x16x32_bf16 v[156:159], a[36:39], a[192:195], v[156:159]\n"
"s_nop 0\n"
"ds_read_b64_tr_b16 a[16:17], v179 offset:4096\n"
"ds_read_b64_tr_b16 a[18:19], v179 offset:6144\n"
"v_mfma_f32_16x16x32_bf16 v[156:159], a[40:43], a[196:199], v[156:159]\n"
"s_nop 0\n"
"ds_read_b64_tr_b16 a[20:21], v179 offset:8192\n"
"ds_read_b64_tr_b16 a[22:23], v179 offset:10240\n"
"v_mfma_f32_16x16x32_bf16 v[156:159], a[44:47], a[200:203], v[156:159]\n"
"s_waitcnt lgkmcnt(14)\n"
"ds_read_b64_tr_b16 a[24:25], v179 offset:12288\n"
"ds_read_b64_tr_b16 a[26:27], v179 offset:14336\n"
"v_mfma_f32_16x16x32_bf16 v[216:219], a[208:211], a[148:151], v[216:219]\n"
"s_nop 0\n"
"ds_read_b64_tr_b16 a[28:29], v179 offset:16384\n"
"ds_read_b64_tr_b16 a[30:31], v179 offset:18432\n"
"v_mfma_f32_16x16x32_bf16 v[220:223], a[218:221], a[148:151], v[220:223]\n"
"v_mul_f32 v156, v124, v156\n"
"v_mul_f32 v157, v125, v157\n"
"v_mul_f32 v158, v126, v158\n"
"v_mfma_f32_16x16x32_bf16 v[224:227], a[228:231], a[148:151], v[224:227]\n"
"v_mul_f32 v159, v127, v159\n"
"v_cvt_pk_bf16_f32 v156, v156, v157\n"
"v_cvt_pk_bf16_f32 v157, v158, v159\n"
"v_mfma_f32_16x16x32_bf16 v[228:231], a[208:211], a[158:161], v[228:231]\n"
"v_accvgpr_write_b32 a128, v156\n"
"v_accvgpr_write_b32 a129, v157\n"
"v_mfma_f32_16x16x32_bf16 v[232:235], a[218:221], a[158:161], v[232:235]\n"
"s_add_u32 s69, s77, 32768\n"
"v_mfma_f32_16x16x32_bf16 v[236:239], a[228:231], a[158:161], v[236:239]\n"
"v_mfma_f32_16x16x32_bf16 v[160:163], a[48:51], v[48:51], v[160:163]\n"
"ds_read_b64_tr_b16 a[32:33], v179 offset:20480\n"
"v_mfma_f32_16x16x32_bf16 v[160:163], a[52:55], v[52:55], v[160:163]\n"
"s_waitcnt lgkmcnt(15)\n"
"ds_read_b64_tr_b16 a[34:35], v179 offset:22528\n"
"v_mfma_f32_16x16x32_bf16 v[160:163], a[56:59], a[172:175], v[160:163]\n"
"ds_read_b64_tr_b16 a[36:37], v176 offset:24576\n"
"v_mfma_f32_16x16x32_bf16 v[160:163], a[0:3], a[176:179], v[160:163]\n"
"s_waitcnt lgkmcnt(13)\n"
"ds_read_b64_tr_b16 a[38:39], v176 offset:26624\n"
"v_mfma_f32_16x16x32_bf16 v[160:163], a[4:7], a[180:183], v[160:163]\n"
"ds_read_b64_tr_b16 a[40:41], v176 offset:28672\n"
"v_mfma_f32_16x16x32_bf16 v[160:163], a[8:11], a[184:187], v[160:163]\n"
"ds_read_b64_tr_b16 a[42:43], v176 offset:30720\n"
"v_mfma_f32_16x16x16_bf16 v[216:219], a[212:213], a[152:153], v[216:219]\n"
"s_nop 0\n"
"buffer_store_dwordx4 a[126:129], v188, s[44:47], s69 offen sc1\n"
"v_mfma_f32_16x16x16_bf16 v[220:223], a[222:223], a[152:153], v[220:223]\n"
"v_mfma_f32_16x16x16_bf16 v[224:227], a[232:233], a[152:153], v[224:227]\n"
"v_mfma_f32_16x16x16_bf16 v[228:231], a[212:213], a[162:163], v[228:231]\n"
"s_nop 0\n"
"buffer_load_dwordx4 v[12:15], v188, s[44:47], s6 offen sc1\n"
"v_mfma_f32_16x16x16_bf16 v[232:235], a[222:223], a[162:163], v[232:235]\n"
"s_add_u32 s6, s78, 3072\n"
"v_mul_f32 v160, v128, v160\n"
"v_mul_f32 v161, v129, v161\n"
"v_mul_f32 v162, v130, v162\n"
"v_mfma_f32_16x16x16_bf16 v[236:239], a[232:233], a[162:163], v[236:239]\n"
"v_add_f32 v226, v226, v227\n"
"v_add_f32 v226, 0x358637bd, v226\n"
"v_mfma_f32_16x16x32_bf16 v[164:167], a[48:51], v[104:107], v[164:167]\n"
"v_mul_f32 v226, v226, v185\n"
"v_mfma_f32_16x16x32_bf16 v[164:167], a[52:55], v[108:111], v[164:167]\n"
"s_nop 0\n"
"v_rcp_f32 v226, v226\n"
"v_mfma_f32_16x16x32_bf16 v[164:167], a[56:59], a[188:191], v[164:167]\n"
"v_mul_f32 v216, v216, v226\n"
"v_mul_f32 v217, v217, v226\n"
"v_mfma_f32_16x16x32_bf16 v[164:167], a[0:3], a[192:195], v[164:167]\n"
"v_mul_f32 v218, v218, v226\n"
"v_mul_f32 v219, v219, v226\n"
"v_mfma_f32_16x16x32_bf16 v[164:167], a[4:7], a[196:199], v[164:167]\n"
"v_mul_f32 v220, v220, v226\n"
"v_mul_f32 v221, v221, v226\n"
"v_mfma_f32_16x16x32_bf16 v[164:167], a[8:11], a[200:203], v[164:167]\n"
"s_waitcnt lgkmcnt(12)\n"
"v_mul_f32 v222, v222, v226\n"
"v_mul_f32 v223, v223, v226\n"
"v_mul_f32 v224, v224, v226\n"
"v_mfma_f32_16x16x32_bf16 v[168:171], a[12:15], v[48:51], v[168:171]\n"
"v_mul_f32 v225, v225, v226\n"
"v_cvt_pk_bf16_f32 v216, v216, v217\n"
"v_cvt_pk_bf16_f32 v217, v218, v219\n"
"v_mfma_f32_16x16x32_bf16 v[168:171], a[16:19], v[52:55], v[168:171]\n"
"s_waitcnt lgkmcnt(8)\n"
"v_cvt_pk_bf16_f32 v218, v220, v221\n"
"v_cvt_pk_bf16_f32 v219, v222, v223\n"
"v_cvt_pk_bf16_f32 v220, v224, v225\n"
"v_mfma_f32_16x16x32_bf16 v[168:171], a[20:23], a[172:175], v[168:171]\n"
"ds_read_b64_tr_b16 a[44:45], v176 offset:32768\n"
"v_and_b32 v220, v189, v220\n"
"s_add_u32 s58, s77, 17408\n"
"v_mfma_f32_16x16x32_bf16 v[168:171], a[24:27], a[176:179], v[168:171]\n"
"s_waitcnt lgkmcnt(5)\n"
"ds_read_b64_tr_b16 a[46:47], v176 offset:34816\n"
"s_add_u32 s59, s77, 36864\n"
"v_mfma_f32_16x16x32_bf16 v[168:171], a[28:31], a[180:183], v[168:171]\n"
"ds_read_b64_tr_b16 a[48:49], v176 offset:36864\n"
"buffer_store_dwordx4 v[216:219], v188, s[44:47], s58 offen sc1\n"
"v_mfma_f32_16x16x32_bf16 v[168:171], a[32:35], a[184:187], v[168:171]\n"
"ds_read_b64_tr_b16 a[50:51], v176 offset:38912\n"
"buffer_store_dword v220, v190, s[44:47], s59 offen sc1\n"
"v_mfma_f32_16x16x32_bf16 v[172:175], a[12:15], v[104:107], v[172:175]\n"
"ds_read_b64_tr_b16 a[52:53], v176 offset:40960\n"
"v_accvgpr_write_b32 a96, v216\n"
"v_accvgpr_write_b32 a97, v217\n"
"v_mfma_f32_16x16x32_bf16 v[172:175], a[16:19], v[108:111], v[172:175]\n"
"ds_read_b64_tr_b16 a[54:55], v176 offset:43008\n"
"v_accvgpr_write_b32 a98, v218\n"
"v_accvgpr_write_b32 a99, v219\n"
"v_mfma_f32_16x16x32_bf16 v[172:175], a[20:23], a[188:191], v[172:175]\n"
"ds_read_b64_tr_b16 a[56:57], v177 offset:24576\n"
"v_accvgpr_write_b32 a100, v220\n"
"v_add_f32 v238, v238, v239\n"
"v_mfma_f32_16x16x32_bf16 v[172:175], a[24:27], a[192:195], v[172:175]\n"
"ds_read_b64_tr_b16 a[58:59], v177 offset:26624\n"
"v_add_f32 v238, 0x358637bd, v238\n"
"v_mul_f32 v238, v238, v186\n"
"v_mfma_f32_16x16x32_bf16 v[172:175], a[28:31], a[196:199], v[172:175]\n"
"ds_read_b64_tr_b16 a[0:1], v177 offset:28672\n"
"v_rcp_f32 v238, v238\n"
"v_mfma_f32_16x16x32_bf16 v[172:175], a[32:35], a[200:203], v[172:175]\n"
"s_waitcnt lgkmcnt(9)\n"
"ds_read_b64_tr_b16 a[2:3], v177 offset:30720\n"
"buffer_load_dwordx4 v[68:71], v188, s[44:47], s6 offen sc1\n"
"v_mfma_f32_16x16x32_bf16 v[112:115], a[36:39], a[60:63], 0\n"
"s_add_u32 s6, s78, 22528\n"
"ds_read_b64_tr_b16 a[4:5], v177 offset:32768\n"
"v_accvgpr_read_b32 v182, a248\n"
"v_mul_f32 v228, v228, v238\n"
"v_mfma_f32_16x16x32_bf16 v[112:115], a[40:43], a[64:67], v[112:115]\n"
"s_waitcnt lgkmcnt(7)\n"
"ds_read_b64_tr_b16 a[6:7], v177 offset:34816\n"
"buffer_load_dwordx4 v[16:19], v188, s[44:47], s6 offen sc1\n"
"v_mfma_f32_16x16x32_bf16 v[112:115], a[44:47], a[68:71], v[112:115]\n"
"s_add_u32 s6, s78, 4096\n"
"ds_read_b64_tr_b16 a[8:9], v177 offset:36864\n"
"v_mul_f32 v229, v229, v238\n"
"v_mul_f32 v230, v230, v238\n"
"v_mfma_f32_16x16x32_bf16 v[112:115], a[48:51], a[72:75], v[112:115]\n"
"s_waitcnt lgkmcnt(7)\n"
"ds_read_b64_tr_b16 a[10:11], v177 offset:38912\n"
"v_mul_f32 v231, v231, v238\n"
"v_mul_f32 v232, v232, v238\n"
"v_mfma_f32_16x16x32_bf16 v[112:115], a[52:55], a[76:79], v[112:115]\n"
"ds_read_b64_tr_b16 a[12:13], v177 offset:40960\n"
"v_mul_f32 v233, v233, v238\n"
"v_mul_f32 v234, v234, v238\n"
"v_mfma_f32_16x16x32_bf16 v[116:119], a[36:39], a[102:105], 0\n"
"ds_read_b64_tr_b16 a[14:15], v177 offset:43008\n"
"v_mul_f32 v235, v235, v238\n"
"v_mul_f32 v236, v236, v238\n"
"v_mfma_f32_16x16x32_bf16 v[116:119], a[40:43], a[106:109], v[116:119]\n"
"ds_read_b64_tr_b16 a[16:17], v178 offset:24576\n"
"v_mul_f32 v237, v237, v238\n"
"v_cvt_pk_bf16_f32 v228, v228, v229\n"
"v_mfma_f32_16x16x32_bf16 v[116:119], a[44:47], a[110:113], v[116:119]\n"
"ds_read_b64_tr_b16 a[18:19], v178 offset:26624\n"
"v_cvt_pk_bf16_f32 v229, v230, v231\n"
"v_cvt_pk_bf16_f32 v230, v232, v233\n"
"v_mfma_f32_16x16x32_bf16 v[116:119], a[48:51], a[114:117], v[116:119]\n"
"ds_read_b64_tr_b16 a[20:21], v178 offset:28672\n"
"v_cvt_pk_bf16_f32 v231, v234, v235\n"
"v_cvt_pk_bf16_f32 v232, v236, v237\n"
"v_mfma_f32_16x16x32_bf16 v[116:119], a[52:55], a[118:121], v[116:119]\n"
"s_waitcnt lgkmcnt(9)\n"
"ds_read_b64_tr_b16 a[22:23], v178 offset:30720\n"
"v_and_b32 v232, v189, v232\n"
"s_add_u32 s58, s77, 35840\n"
"v_mfma_f32_16x16x32_bf16 v[120:123], a[56:59], a[60:63], 0\n"
"s_add_u32 s59, s77, 37120\n"
"ds_read_b64_tr_b16 a[24:25], v178 offset:32768\n"
"buffer_store_dwordx4 v[228:231], v188, s[44:47], s58 offen sc1\n"
"v_mfma_f32_16x16x32_bf16 v[120:123], a[0:3], a[64:67], v[120:123]\n"
"s_waitcnt lgkmcnt(7)\n"
"ds_read_b64_tr_b16 a[26:27], v178 offset:34816\n"
"buffer_store_dword v232, v190, s[44:47], s59 offen sc1\n"
"v_mfma_f32_16x16x32_bf16 v[120:123], a[4:7], a[68:71], v[120:123]\n"
"ds_read_b64_tr_b16 a[28:29], v178 offset:36864\n"
"v_accvgpr_write_b32 a138, v228\n"
"v_accvgpr_write_b32 a139, v229\n"
"v_mfma_f32_16x16x32_bf16 v[120:123], a[8:11], a[72:75], v[120:123]\n"
"s_waitcnt lgkmcnt(7)\n"
"ds_read_b64_tr_b16 a[30:31], v178 offset:38912\n"
"buffer_load_dwordx4 v[72:75], v188, s[44:47], s6 offen sc1\n"
"v_mfma_f32_16x16x32_bf16 v[120:123], a[12:15], a[76:79], v[120:123]\n"
"s_add_u32 s6, s78, 23552\n"
"ds_read_b64_tr_b16 a[32:33], v178 offset:40960\n"
"v_accvgpr_write_b32 a140, v230\n"
"v_accvgpr_write_b32 a141, v231\n"
"v_mfma_f32_16x16x32_bf16 v[124:127], a[56:59], a[102:105], 0\n"
"ds_read_b64_tr_b16 a[34:35], v178 offset:43008\n"
"v_accvgpr_write_b32 a142, v232\n"
"v_mfma_f32_16x16x32_bf16 v[124:127], a[0:3], a[106:109], v[124:127]\n"
"s_nop 0\n"
"ds_read_b64_tr_b16 a[36:37], v179 offset:24576\n"
"ds_read_b64_tr_b16 a[38:39], v179 offset:26624\n"
"v_mfma_f32_16x16x32_bf16 v[124:127], a[4:7], a[110:113], v[124:127]\n"
"s_nop 0\n"
"ds_read_b64_tr_b16 a[40:41], v179 offset:28672\n"
"ds_read_b64_tr_b16 a[42:43], v179 offset:30720\n"
"v_mfma_f32_16x16x32_bf16 v[124:127], a[8:11], a[114:117], v[124:127]\n"
"s_nop 0\n"
"ds_read_b64_tr_b16 a[44:45], v179 offset:32768\n"
"ds_read_b64_tr_b16 a[46:47], v179 offset:34816\n"
"v_mfma_f32_16x16x32_bf16 v[124:127], a[12:15], a[118:121], v[124:127]\n"
"s_waitcnt lgkmcnt(12)\n"
"ds_read_b64_tr_b16 a[48:49], v179 offset:36864\n"
"ds_read_b64_tr_b16 a[50:51], v179 offset:38912\n"
"v_mul_f32 v163, v131, v163\n"
"v_mfma_f32_16x16x32_bf16 v[128:131], a[16:19], a[60:63], 0\n"
"s_nop 0\n"
"ds_read_b64_tr_b16 a[52:53], v179 offset:40960\n"
"ds_read_b64_tr_b16 a[54:55], v179 offset:43008\n"
"v_mfma_f32_16x16x32_bf16 v[128:131], a[20:23], a[64:67], v[128:131]\n"
"s_waitcnt lgkmcnt(12)\n"
"buffer_load_dwordx4 v183, s[36:39], s56 offen lds\n"
"v_mfma_f32_16x16x32_bf16 v[128:131], a[24:27], a[68:71], v[128:131]\n"
"s_addk_i32 m0, 4096\n"
"buffer_load_dwordx4 v[20:23], v188, s[44:47], s6 offen sc1\n"
"v_mfma_f32_16x16x32_bf16 v[128:131], a[28:31], a[72:75], v[128:131]\n"
"s_waitcnt lgkmcnt(10)\n"
"v_cvt_pk_bf16_f32 v160, v160, v161\n"
"v_cvt_pk_bf16_f32 v161, v162, v163\n"
"v_accvgpr_write_b32 a88, v160\n"
"v_mfma_f32_16x16x32_bf16 v[128:131], a[32:35], a[76:79], v[128:131]\n"
"s_add_u32 s56, s72, 36864\n"
"buffer_load_dwordx4 v183, s[36:39], s56 offen lds\n"
"v_accvgpr_write_b32 a89, v161\n"
"v_mul_f32 v164, v132, v164\n"
"v_mul_f32 v165, v133, v165\n"
"v_mul_f32 v166, v134, v166\n"
"v_mul_f32 v167, v135, v167\n"
"v_mfma_f32_16x16x32_bf16 v[132:135], a[16:19], a[102:105], 0\n"
"s_waitcnt vmcnt(23) lgkmcnt(0)\n"
"v_cvt_pk_bf16_f32 v164, v164, v165\n"
"v_cvt_pk_bf16_f32 v165, v166, v167\n"
"v_accvgpr_write_b32 a130, v164\n"
"v_mfma_f32_16x16x32_bf16 v[132:135], a[20:23], a[106:109], v[132:135]\n"
"s_barrier\n"
"ds_read_b64_tr_b16 a[56:57], v176 offset:45056\n"
"v_accvgpr_write_b32 a131, v165\n"
"v_mul_f32 v168, v136, v168\n"
"v_mfma_f32_16x16x32_bf16 v[132:135], a[24:27], a[110:113], v[132:135]\n"
"s_addk_i32 m0, 4096\n"
"ds_read_b64_tr_b16 a[58:59], v176 offset:47104\n"
"v_mul_f32 v169, v137, v169\n"
"v_mul_f32 v170, v138, v170\n"
"v_mfma_f32_16x16x32_bf16 v[132:135], a[28:31], a[114:117], v[132:135]\n"
"s_add_u32 s56, s72, 73728\n"
"ds_read_b64_tr_b16 a[0:1], v176 offset:49152\n"
"ds_read_b64_tr_b16 a[2:3], v176 offset:51200\n"
"v_mul_f32 v171, v139, v171\n"
"v_mfma_f32_16x16x32_bf16 v[132:135], a[32:35], a[118:121], v[132:135]\n"
"s_add_u32 s6, s78, 5120\n"
"buffer_load_dwordx4 v183, s[36:39], s56 offen lds\n"
"v_mfma_f32_16x16x32_bf16 v[136:139], a[36:39], a[60:63], 0\n"
"s_addk_i32 m0, 4096\n"
"ds_read_b64_tr_b16 a[4:5], v176 offset:53248\n"
"buffer_load_dwordx4 v[76:79], v188, s[44:47], s6 offen sc1\n"
"v_mfma_f32_16x16x32_bf16 v[136:139], a[40:43], a[64:67], v[136:139]\n"
"s_add_u32 s56, s72, 110592\n"
"ds_read_b64_tr_b16 a[6:7], v176 offset:55296\n"
"v_cvt_pk_bf16_f32 v168, v168, v169\n"
"v_cvt_pk_bf16_f32 v169, v170, v171\n"
"v_mfma_f32_16x16x32_bf16 v[136:139], a[44:47], a[68:71], v[136:139]\n"
"s_add_u32 s6, s78, 34816\n"
"ds_read_b64_tr_b16 a[8:9], v176 offset:57344\n"
"v_accvgpr_write_b32 a90, v168\n"
"v_accvgpr_write_b32 a91, v169\n"
"v_mfma_f32_16x16x32_bf16 v[136:139], a[48:51], a[72:75], v[136:139]\n"
"s_add_u32 s69, s77, 7168\n"
"ds_read_b64_tr_b16 a[10:11], v176 offset:59392\n"
"v_and_b32 v204, 15, v187\n"
"v_lshrrev_b32 v205, 4, v187\n"
"v_mfma_f32_16x16x32_bf16 v[136:139], a[52:55], a[76:79], v[136:139]\n"
"s_lshr_b32 s60, s5, s57\n"
"ds_read_b64_tr_b16 a[12:13], v177 offset:45056\n"
"v_lshlrev_b32 v189, 1, v205\n"
"v_lshrrev_b32 v190, v189, s60\n"
"buffer_store_dwordx4 a[88:91], v188, s[44:47], s69 offen sc1\n"
"v_mul_f32 v172, v140, v172\n"
"v_mul_f32 v173, v141, v173\n"
"v_mul_f32 v174, v142, v174\n"
"v_mul_f32 v175, v143, v175\n"
"v_mfma_f32_16x16x32_bf16 v[140:143], a[36:39], a[102:105], 0\n"
"ds_read_b64_tr_b16 a[14:15], v177 offset:47104\n"
"v_cvt_pk_bf16_f32 v172, v172, v173\n"
"v_cvt_pk_bf16_f32 v173, v174, v175\n"
"v_mfma_f32_16x16x32_bf16 v[140:143], a[40:43], a[106:109], v[140:143]\n"
"ds_read_b64_tr_b16 a[16:17], v177 offset:49152\n"
"v_accvgpr_write_b32 a132, v172\n"
"v_mfma_f32_16x16x32_bf16 v[140:143], a[44:47], a[110:113], v[140:143]\n"
"s_nop 0\n"
"ds_read_b64_tr_b16 a[18:19], v177 offset:51200\n"
"ds_read_b64_tr_b16 a[20:21], v177 offset:53248\n"
"v_mfma_f32_16x16x32_bf16 v[140:143], a[48:51], a[114:117], v[140:143]\n"
"s_nop 0\n"
"buffer_load_dwordx4 v183, s[36:39], s56 offen lds\n"
"v_mfma_f32_16x16x32_bf16 v[140:143], a[52:55], a[118:121], v[140:143]\n"
"s_waitcnt lgkmcnt(9)\n"
"ds_read_b64_tr_b16 a[22:23], v177 offset:55296\n"
"v_accvgpr_write_b32 a133, v173\n"
"v_mfma_f32_16x16x32_bf16 v[112:115], a[56:59], a[80:83], v[112:115]\n"
"s_addk_i32 m0, 4096\n"
"ds_read_b64_tr_b16 a[24:25], v177 offset:57344\n"
"s_add_u32 s69, s77, 33792\n"
"v_mfma_f32_16x16x32_bf16 v[112:115], a[0:3], a[92:95], v[112:115]\n"
"s_waitcnt lgkmcnt(7)\n"
"ds_read_b64_tr_b16 a[26:27], v177 offset:59392\n"
"v_bfe_u32 v226, v190, 8, 1\n"
"v_mfma_f32_16x16x32_bf16 v[112:115], a[4:7], a[84:87], v[112:115]\n"
"s_add_u32 s56, s72, 147456\n"
"ds_read_b64_tr_b16 a[28:29], v178 offset:45056\n"
"v_and_b32 v189, 1, v190\n"
"v_mfma_f32_16x16x32_bf16 v[112:115], a[8:11], a[88:91], v[112:115]\n"
"ds_read_b64_tr_b16 a[30:31], v178 offset:47104\n"
"v_and_b32 v191, 1, v204\n"
"v_mfma_f32_16x16x32_bf16 v[116:119], a[56:59], a[122:125], v[116:119]\n"
"ds_read_b64_tr_b16 a[32:33], v178 offset:49152\n"
"v_add_u32 v176, 61440, v176\n"
"v_mfma_f32_16x16x32_bf16 v[116:119], a[0:3], a[134:137], v[116:119]\n"
"ds_read_b64_tr_b16 a[34:35], v178 offset:51200\n"
"v_lshlrev_b32 v193, 3, v191\n"
"v_mfma_f32_16x16x32_bf16 v[116:119], a[4:7], a[126:129], v[116:119]\n"
"ds_read_b64_tr_b16 a[36:37], v178 offset:53248\n"
"v_bfe_u32 v190, v190, v193, 1\n"
"v_mfma_f32_16x16x32_bf16 v[116:119], a[8:11], a[130:133], v[116:119]\n"
"s_waitcnt lgkmcnt(9)\n"
"ds_read_b64_tr_b16 a[38:39], v178 offset:55296\n"
"ds_read_b64_tr_b16 a[40:41], v178 offset:57344\n"
"v_mfma_f32_16x16x32_bf16 v[120:123], a[12:15], a[80:83], v[120:123]\n"
"s_nop 0\n"
"buffer_load_dwordx4 v183, s[36:39], s56 offen lds\n"
"v_mfma_f32_16x16x32_bf16 v[120:123], a[16:19], a[92:95], v[120:123]\n"
"s_waitcnt lgkmcnt(7)\n"
"ds_read_b64_tr_b16 a[42:43], v178 offset:59392\n"
"v_lshlrev_b32 v193, 20, v205\n"
"v_mfma_f32_16x16x32_bf16 v[120:123], a[20:23], a[84:87], v[120:123]\n"
"s_addk_i32 m0, 4096\n"
"ds_read_b64_tr_b16 a[44:45], v179 offset:45056\n"
"v_lshl_add_u32 v191, v191, 22, v193\n"
"v_mfma_f32_16x16x32_bf16 v[120:123], a[24:27], a[88:91], v[120:123]\n"
"s_add_u32 s56, s72, 184320\n"
"ds_read_b64_tr_b16 a[46:47], v179 offset:47104\n"
"ds_read_b64_tr_b16 a[48:49], v179 offset:49152\n"
"v_mfma_f32_16x16x32_bf16 v[124:127], a[12:15], a[122:125], v[124:127]\n"
"s_nop 0\n"
"buffer_load_dwordx4 v183, s[36:39], s56 offen lds\n"
"v_mfma_f32_16x16x32_bf16 v[124:127], a[16:19], a[134:137], v[124:127]\n"
"s_addk_i32 m0, 4096\n"
"ds_read_b64_tr_b16 a[50:51], v179 offset:51200\n"
"v_lshl_add_u32 v191, v190, 13, v191\n"
"v_mfma_f32_16x16x32_bf16 v[124:127], a[20:23], a[126:129], v[124:127]\n"
"111:\n"
"s_cbranch_execz 115f\n"
"s_add_u32 s56, s70, 0\n"
"ds_read_b64_tr_b16 a[52:53], v179 offset:53248\n"
"v_add_u32 v177, 61440, v177\n"
"v_mfma_f32_16x16x32_bf16 v[124:127], a[24:27], a[130:133], v[124:127]\n"
"s_waitcnt lgkmcnt(9)\n"
"ds_read_b64_tr_b16 a[54:55], v179 offset:55296\n"
"v_lshl_add_u32 v193, v204, 4, v193\n"
"v_mfma_f32_16x16x32_bf16 v[128:131], a[28:31], a[80:83], v[128:131]\n"
"ds_read_b64_tr_b16 a[56:57], v179 offset:57344\n"
"v_lshl_add_u32 v189, v189, 13, v193\n"
"v_mfma_f32_16x16x32_bf16 v[128:131], a[32:35], a[92:95], v[128:131]\n"
"s_waitcnt lgkmcnt(7)\n"
"ds_read_b64_tr_b16 a[58:59], v179 offset:59392\n"
"v_lshl_add_u32 v226, v226, 13, v193\n"
"v_mfma_f32_16x16x32_bf16 v[128:131], a[36:39], a[84:87], v[128:131]\n"
"s_lshr_b32 s61, s53, 1\n"
"ds_read_b64_tr_b16 a[0:1], v176 offset:0\n"
"s_lshl_b32 s60, s52, 4\n"
"v_mfma_f32_16x16x32_bf16 v[128:131], a[40:43], a[88:91], v[128:131]\n"
"s_add_u32 s60, s60, s57\n"
"ds_read_b64_tr_b16 a[2:3], v176 offset:2048\n"
"s_lshl_b32 s60, s60, 19\n"
"v_mfma_f32_16x16x32_bf16 v[132:135], a[28:31], a[122:125], v[132:135]\n"
"s_lshr_b32 s58, s53, 2\n"
"ds_read_b64_tr_b16 a[4:5], v176 offset:4096\n"
"s_mul_i32 s58, s58, 81920\n"
"v_mfma_f32_16x16x32_bf16 v[132:135], a[32:35], a[134:137], v[132:135]\n"
"s_add_u32 s60, s60, s58\n"
"ds_read_b64_tr_b16 a[6:7], v176 offset:6144\n"
"ds_read_b64_tr_b16 a[8:9], v176 offset:8192\n"
"v_mfma_f32_16x16x32_bf16 v[132:135], a[36:39], a[126:129], v[132:135]\n"
"s_add_u32 s66, s60, 4231936\n"
"buffer_store_dwordx4 a[130:133], v188, s[44:47], s69 offen sc1\n"
"v_mfma_f32_16x16x32_bf16 v[132:135], a[40:43], a[130:133], v[132:135]\n"
"s_waitcnt lgkmcnt(9)\n"
"ds_read_b64_tr_b16 a[10:11], v176 offset:10240\n"
"s_lshl_b32 s59, s54, 8\n"
"v_mfma_f32_16x16x32_bf16 v[136:139], a[44:47], a[80:83], v[136:139]\n"
"s_add_u32 s60, s60, s59\n"
"ds_read_b64_tr_b16 a[12:13], v176 offset:12288\n"
"ds_read_b64_tr_b16 a[14:15], v176 offset:14336\n"
"v_mfma_f32_16x16x32_bf16 v[136:139], a[48:51], a[92:95], v[136:139]\n"
"s_waitcnt lgkmcnt(8)\n"
"buffer_load_dwordx4 v[32:35], v188, s[44:47], s6 offen sc1\n"
"v_mfma_f32_16x16x32_bf16 v[136:139], a[52:55], a[84:87], v[136:139]\n"
"s_xor_b32 s59, s61, s57\n"
"ds_read_b64_tr_b16 a[16:17], v176 offset:16384\n"
"v_add_u32 v178, 61440, v178\n"
"v_mfma_f32_16x16x32_bf16 v[136:139], a[56:59], a[88:91], v[136:139]\n"
"s_and_b32 s59, s59, 1\n"
"ds_read_b64_tr_b16 a[18:19], v176 offset:18432\n"
"s_lshl_b32 s81, s59, 10\n"
"v_mfma_f32_16x16x32_bf16 v[140:143], a[44:47], a[122:125], v[140:143]\n"
"s_add_u32 s81, s81, s60\n"
"ds_read_b64_tr_b16 a[20:21], v177 offset:0\n"
"s_add_u32 s81, s81, 4232192\n"
"v_mfma_f32_16x16x32_bf16 v[140:143], a[48:51], a[134:137], v[140:143]\n"
"s_add_u32 s82, s81, 4194304\n"
"ds_read_b64_tr_b16 a[22:23], v177 offset:2048\n"
"s_mul_i32 s58, s59, 16384\n"
"v_mfma_f32_16x16x32_bf16 v[140:143], a[52:55], a[126:129], v[140:143]\n"
"s_add_u32 s58, s58, s60\n"
"ds_read_b64_tr_b16 a[24:25], v177 offset:4096\n"
"s_add_u32 s58, s58, 4243456\n"
"v_mfma_f32_16x16x32_bf16 v[140:143], a[56:59], a[130:133], v[140:143]\n"
"s_waitcnt lgkmcnt(9)\n"
"ds_read_b64_tr_b16 a[26:27], v177 offset:6144\n"
"s_add_u32 s59, s58, 4194304\n"
"v_mfma_f32_16x16x32_bf16 v[144:147], a[0:3], a[60:63], 0\n"
"s_mov_b32 s68, 28\n"
"ds_read_b64_tr_b16 a[28:29], v177 offset:8192\n"
"v_lshrrev_b32 v193, 2, v204\n"
"v_mfma_f32_16x16x32_bf16 v[144:147], a[4:7], a[64:67], v[144:147]\n"
"s_waitcnt lgkmcnt(7)\n"
"ds_read_b64_tr_b16 a[30:31], v177 offset:10240\n"
"v_mul_u32_u24 v192, 1152, v193\n"
"v_mfma_f32_16x16x32_bf16 v[144:147], a[8:11], a[68:71], v[144:147]\n"
"s_add_u32 s6, s78, 16384\n"
"ds_read_b64_tr_b16 a[32:33], v177 offset:12288\n"
"ds_read_b64_tr_b16 a[34:35], v177 offset:14336\n"
"v_mfma_f32_16x16x32_bf16 v[144:147], a[12:15], a[72:75], v[144:147]\n"
"s_waitcnt lgkmcnt(8)\n"
"buffer_load_dwordx4 v[88:91], v188, s[44:47], s6 offen sc1\n"
"v_mfma_f32_16x16x32_bf16 v[144:147], a[16:19], a[76:79], v[144:147]\n"
"s_nop 0\n"
"ds_read_b64_tr_b16 a[36:37], v177 offset:16384\n"
"ds_read_b64_tr_b16 a[38:39], v177 offset:18432\n"
"v_mfma_f32_16x16x32_bf16 v[148:151], a[0:3], a[102:105], 0\n"
"s_nop 0\n"
"buffer_load_dwordx4 v180, s[36:39], s56 offen lds\n"
"v_mfma_f32_16x16x32_bf16 v[148:151], a[4:7], a[106:109], v[148:151]\n"
"s_addk_i32 m0, 4096\n"
"ds_read_b64_tr_b16 a[40:41], v178 offset:0\n"
"v_add_u32 v179, 61440, v179\n"
"v_mfma_f32_16x16x32_bf16 v[148:151], a[8:11], a[110:113], v[148:151]\n"
"s_add_u32 s56, s70, 73728\n"
"ds_read_b64_tr_b16 a[42:43], v178 offset:2048\n"
"v_and_b32 v204, 3, v204\n"
"v_mfma_f32_16x16x32_bf16 v[148:151], a[12:15], a[114:117], v[148:151]\n"
"ds_read_b64_tr_b16 a[44:45], v178 offset:4096\n"
"v_lshl_add_u32 v192, v204, 3, v192\n"
"v_mfma_f32_16x16x32_bf16 v[148:151], a[16:19], a[118:121], v[148:151]\n"
"s_waitcnt lgkmcnt(9)\n"
"ds_read_b64_tr_b16 a[46:47], v178 offset:6144\n"
"v_lshl_add_u32 v192, v205, 5, v192\n"
"v_mfma_f32_16x16x32_bf16 v[152:155], a[20:23], a[60:63], 0\n"
"ds_read_b64_tr_b16 a[48:49], v178 offset:8192\n"
"v_lshl_add_u32 v195, v204, 2, v193\n"
"v_mfma_f32_16x16x32_bf16 v[152:155], a[24:27], a[64:67], v[152:155]\n"
"s_waitcnt lgkmcnt(7)\n"
"ds_read_b64_tr_b16 a[50:51], v178 offset:10240\n"
"ds_read_b64_tr_b16 a[52:53], v178 offset:12288\n"
"v_mfma_f32_16x16x32_bf16 v[152:155], a[28:31], a[68:71], v[152:155]\n"
"s_nop 0\n"
"buffer_load_dwordx4 v180, s[36:39], s56 offen lds\n"
"v_mfma_f32_16x16x32_bf16 v[152:155], a[32:35], a[72:75], v[152:155]\n"
"s_waitcnt lgkmcnt(7)\n"
"ds_read_b64_tr_b16 a[54:55], v178 offset:14336\n"
"v_lshl_add_u32 v195, v205, 4, v195\n"
"v_mfma_f32_16x16x32_bf16 v[152:155], a[36:39], a[76:79], v[152:155]\n"
"s_addk_i32 m0, 4096\n"
"ds_read_b64_tr_b16 a[56:57], v178 offset:16384\n"
"v_lshlrev_b32 v195, 2, v195\n"
"v_mfma_f32_16x16x32_bf16 v[156:159], a[20:23], a[102:105], 0\n"
"s_add_u32 s56, s70, 147456\n"
"ds_read_b64_tr_b16 a[58:59], v178 offset:18432\n"
"v_lshrrev_b32 v205, 1, v205\n"
"v_mfma_f32_16x16x32_bf16 v[156:159], a[24:27], a[106:109], v[156:159]\n"
"s_nop 0\n"
"ds_read_b64_tr_b16 a[0:1], v179 offset:0\n"
"ds_read_b64_tr_b16 a[2:3], v179 offset:2048\n"
"v_mfma_f32_16x16x32_bf16 v[156:159], a[28:31], a[110:113], v[156:159]\n"
"s_nop 0\n"
"ds_read_b64_tr_b16 a[4:5], v179 offset:4096\n"
"ds_read_b64_tr_b16 a[6:7], v179 offset:6144\n"
"v_mfma_f32_16x16x32_bf16 v[156:159], a[32:35], a[114:117], v[156:159]\n"
"s_nop 0\n"
"ds_read_b64_tr_b16 a[8:9], v179 offset:8192\n"
"ds_read_b64_tr_b16 a[10:11], v179 offset:10240\n"
"v_mfma_f32_16x16x32_bf16 v[156:159], a[36:39], a[118:121], v[156:159]\n"
"s_waitcnt lgkmcnt(12)\n"
"ds_read_b64_tr_b16 a[12:13], v179 offset:12288\n"
"ds_read_b64_tr_b16 a[14:15], v179 offset:14336\n"
"v_mfma_f32_16x16x32_bf16 v[160:163], a[40:43], a[60:63], 0\n"
"s_nop 0\n"
"ds_read_b64_tr_b16 a[16:17], v179 offset:16384\n"
"ds_read_b64_tr_b16 a[18:19], v179 offset:18432\n"
"v_mfma_f32_16x16x32_bf16 v[160:163], a[44:47], a[64:67], v[160:163]\n"
"s_waitcnt lgkmcnt(12)\n"
"buffer_load_dwordx4 v180, s[36:39], s56 offen lds\n"
"v_mfma_f32_16x16x32_bf16 v[160:163], a[48:51], a[68:71], v[160:163]\n"
"s_addk_i32 m0, 4096\n"
"v_lshl_add_u32 v194, v205, 30, v192\n"
"s_lshl_b32 s60, s57, 6\n"
"v_mfma_f32_16x16x32_bf16 v[160:163], a[52:55], a[72:75], v[160:163]\n"
"s_waitcnt lgkmcnt(10)\n"
"v_add_u32 v194, s60, v194\n"
"s_mul_i32 s60, s57, 640\n"
"v_mfma_f32_16x16x32_bf16 v[160:163], a[56:59], a[76:79], v[160:163]\n"
"s_add_u32 s56, s70, 221184\n"
"v_add_u32 v192, s60, v192\n"
"s_lshl_b32 s61, s61, 2\n"
"v_mfma_f32_16x16x32_bf16 v[164:167], a[40:43], a[102:105], 0\n"
"s_waitcnt vmcnt(6) lgkmcnt(0)\n"
"buffer_load_dwordx4 v180, s[36:39], s56 offen lds\n"
"v_mfma_f32_16x16x32_bf16 v[164:167], a[44:47], a[106:109], v[164:167]\n"
"s_barrier\n"
"ds_read_b64_tr_b16 a[20:21], v176 offset:20480\n"
"s_add_u32 s61, s61, s54\n"
"v_mfma_f32_16x16x32_bf16 v[164:167], a[48:51], a[110:113], v[164:167]\n"
"s_addk_i32 m0, 4096\n"
"ds_read_b64_tr_b16 a[22:23], v176 offset:22528\n"
"s_lshl_b32 s60, s52, 5\n"
"v_mfma_f32_16x16x32_bf16 v[164:167], a[52:55], a[114:117], v[164:167]\n"
"s_add_u32 s56, s72, 128\n"
"ds_read_b64_tr_b16 a[24:25], v176 offset:24576\n"
"s_add_u32 s60, s60, s61\n"
"v_mfma_f32_16x16x32_bf16 v[164:167], a[56:59], a[118:121], v[164:167]\n"
"s_mul_i32 s67, s60, 4608\n"
"ds_read_b64_tr_b16 a[26:27], v176 offset:26624\n"
"s_lshl_b32 s61, s61, 4\n"
"v_mfma_f32_16x16x32_bf16 v[168:171], a[0:3], a[60:63], 0\n"
"s_add_u32 s61, s61, 159744\n"
"ds_read_b64_tr_b16 a[28:29], v176 offset:28672\n"
"v_lshl_add_u32 v193, v193, 2, s61\n"
"v_mfma_f32_16x16x32_bf16 v[168:171], a[4:7], a[64:67], v[168:171]\n"
"s_nop 0\n"
"ds_read_b64_tr_b16 a[30:31], v176 offset:30720\n"
"ds_read_b64_tr_b16 a[32:33], v176 offset:32768\n"
"v_mfma_f32_16x16x32_bf16 v[168:171], a[8:11], a[68:71], v[168:171]\n"
"s_nop 0\n"
"buffer_load_dwordx4 v183, s[36:39], s56 offen lds\n"
"v_mfma_f32_16x16x32_bf16 v[168:171], a[12:15], a[72:75], v[168:171]\n"
"s_addk_i32 m0, 4096\n"
"ds_read_b64_tr_b16 a[34:35], v176 offset:34816\n"
"ds_read_b32 v193, v193\n"
"v_mfma_f32_16x16x32_bf16 v[168:171], a[16:19], a[76:79], v[168:171]\n"
"s_add_u32 s56, s72, 36992\n"
"ds_read_b64_tr_b16 a[36:37], v177 offset:20480\n"
"ds_read_b64_tr_b16 a[38:39], v177 offset:22528\n"
"v_mfma_f32_16x16x32_bf16 v[172:175], a[0:3], a[102:105], 0\n"
"s_nop 0\n"
"ds_read_b64_tr_b16 a[40:41], v177 offset:24576\n"
"ds_read_b64_tr_b16 a[42:43], v177 offset:26624\n"
"v_mfma_f32_16x16x32_bf16 v[172:175], a[4:7], a[106:109], v[172:175]\n"
"ds_read_b64_tr_b16 a[44:45], v177 offset:28672\n"
"v_mfma_f32_16x16x32_bf16 v[172:175], a[8:11], a[110:113], v[172:175]\n"
"ds_read_b64_tr_b16 a[46:47], v177 offset:30720\n"
"v_mfma_f32_16x16x32_bf16 v[172:175], a[12:15], a[114:117], v[172:175]\n"
"ds_read_b64_tr_b16 a[48:49], v177 offset:32768\n"
"v_mfma_f32_16x16x32_bf16 v[172:175], a[16:19], a[118:121], v[172:175]\n"
"s_waitcnt lgkmcnt(12)\n"
"ds_read_b64_tr_b16 a[50:51], v177 offset:34816\n"
"v_mfma_f32_16x16x32_bf16 v[144:147], a[20:23], a[80:83], v[144:147]\n"
"ds_read_b64_tr_b16 a[52:53], v178 offset:20480\n"
"v_mfma_f32_16x16x32_bf16 v[144:147], a[24:27], a[92:95], v[144:147]\n"
"s_waitcnt lgkmcnt(10)\n"
"s_waitcnt vmcnt(7)\n"
"s_add_u32 s79, s77, 37504\n"
"v_mfma_f32_16x16x32_bf16 v[144:147], a[28:31], a[84:87], v[144:147]\n"
"s_nop 0\n"
"v_add_u32_e64 v199, s65, 0\n"
"buffer_store_dwordx2 v[198:199], v197, s[44:47], s79 offen offset:0 sc1\n"
"v_mfma_f32_16x16x32_bf16 v[144:147], a[32:35], a[88:91], v[144:147]\n"
"s_add_u32 s80, s78, 37504\n"
"ds_read_b64_tr_b16 a[54:55], v178 offset:22528\n"
"ds_read_b64_tr_b16 a[56:57], v178 offset:24576\n"
"v_mfma_f32_16x16x32_bf16 v[148:151], a[20:23], a[122:125], v[148:151]\n"
"v_add_u32 v176, 4294856704, v176\n"
"v_mfma_f32_16x16x32_bf16 v[148:151], a[24:27], a[134:137], v[148:151]\n"
"s_nop 0\n"
"ds_read_b64_tr_b16 a[58:59], v178 offset:26624\n"
"ds_read_b64_tr_b16 a[0:1], v178 offset:28672\n"
"v_mfma_f32_16x16x32_bf16 v[148:151], a[28:31], a[126:129], v[148:151]\n"
"s_nop 0\n"
"ds_read_b64_tr_b16 a[2:3], v178 offset:30720\n"
"ds_read_b64_tr_b16 a[4:5], v178 offset:32768\n"
"v_mfma_f32_16x16x32_bf16 v[148:151], a[32:35], a[130:133], v[148:151]\n"
"s_waitcnt lgkmcnt(11)\n"
"ds_read_b64_tr_b16 a[6:7], v178 offset:34816\n"
"v_mfma_f32_16x16x32_bf16 v[152:155], a[36:39], a[80:83], v[152:155]\n"
"ds_read_b64_tr_b16 a[8:9], v179 offset:20480\n"
"v_mfma_f32_16x16x32_bf16 v[152:155], a[40:43], a[92:95], v[152:155]\n"
"s_waitcnt lgkmcnt(9)\n"
"ds_read_b64_tr_b16 a[10:11], v179 offset:22528\n"
"v_mfma_f32_16x16x32_bf16 v[152:155], a[44:47], a[84:87], v[152:155]\n"
"ds_read_b64_tr_b16 a[12:13], v179 offset:24576\n"
"v_mfma_f32_16x16x32_bf16 v[152:155], a[48:51], a[88:91], v[152:155]\n"
"ds_read_b64_tr_b16 a[14:15], v179 offset:26624\n"
"v_mfma_f32_16x16x32_bf16 v[156:159], a[36:39], a[122:125], v[156:159]\n"
"ds_read_b64_tr_b16 a[16:17], v179 offset:28672\n"
"v_mfma_f32_16x16x32_bf16 v[156:159], a[40:43], a[134:137], v[156:159]\n"
"ds_read_b64_tr_b16 a[18:19], v179 offset:30720\n"
"v_mfma_f32_16x16x32_bf16 v[156:159], a[44:47], a[126:129], v[156:159]\n"
"ds_read_b64_tr_b16 a[20:21], v179 offset:32768\n"
"v_add_u32 v177, 4294856704, v177\n"
"v_mfma_f32_16x16x32_bf16 v[156:159], a[48:51], a[130:133], v[156:159]\n"
"s_waitcnt lgkmcnt(11)\n"
"ds_read_b64_tr_b16 a[22:23], v179 offset:34816\n"
"v_mfma_f32_16x16x32_bf16 v[160:163], a[52:55], a[80:83], v[160:163]\n"
"ds_read_b64_tr_b16 a[24:25], v176 offset:0\n"
"v_mfma_f32_16x16x32_bf16 v[160:163], a[56:59], a[92:95], v[160:163]\n"
"s_waitcnt lgkmcnt(9)\n"
"ds_read_b64_tr_b16 a[26:27], v176 offset:2048\n"
"v_mfma_f32_16x16x32_bf16 v[160:163], a[0:3], a[84:87], v[160:163]\n"
"s_nop 0\n"
"buffer_load_dwordx4 v183, s[36:39], s56 offen lds\n"
"v_mfma_f32_16x16x32_bf16 v[160:163], a[4:7], a[88:91], v[160:163]\n"
"s_addk_i32 m0, 4096\n"
"s_add_u32 s56, s72, 73856\n"
"ds_read_b64_tr_b16 a[28:29], v176 offset:4096\n"
"v_mfma_f32_16x16x32_bf16 v[164:167], a[52:55], a[122:125], v[164:167]\n"
"s_nop 0\n"
"ds_read_b64_tr_b16 a[30:31], v176 offset:6144\n"
"ds_read_b64_tr_b16 a[32:33], v176 offset:8192\n"
"v_mfma_f32_16x16x32_bf16 v[164:167], a[56:59], a[134:137], v[164:167]\n"
"ds_read_b64_tr_b16 a[34:35], v176 offset:10240\n"
"v_mfma_f32_16x16x32_bf16 v[164:167], a[0:3], a[126:129], v[164:167]\n"
"s_nop 0\n"
"buffer_load_dwordx4 v183, s[36:39], s56 offen lds\n"
"v_mfma_f32_16x16x32_bf16 v[164:167], a[4:7], a[130:133], v[164:167]\n"
"s_waitcnt lgkmcnt(10)\n"
"s_addk_i32 m0, 4096\n"
"s_add_u32 s56, s72, 110720\n"
"v_mfma_f32_16x16x32_bf16 v[168:171], a[8:11], a[80:83], v[168:171]\n"
"s_nop 0\n"
"ds_read_b64_tr_b16 a[36:37], v176 offset:12288\n"
"ds_read_b64_tr_b16 a[38:39], v176 offset:14336\n"
"v_mfma_f32_16x16x32_bf16 v[168:171], a[12:15], a[92:95], v[168:171]\n"
"s_waitcnt lgkmcnt(8)\n"
"buffer_load_dwordx4 v183, s[36:39], s56 offen lds\n"
"v_mfma_f32_16x16x32_bf16 v[168:171], a[16:19], a[84:87], v[168:171]\n"
"s_addk_i32 m0, 4096\n"
"ds_read_b64_tr_b16 a[40:41], v176 offset:16384\n"
"s_add_u32 s56, s72, 147584\n"
"v_mfma_f32_16x16x32_bf16 v[168:171], a[20:23], a[88:91], v[168:171]\n"
"s_nop 0\n"
"ds_read_b64_tr_b16 a[42:43], v176 offset:18432\n"
"ds_read_b64_tr_b16 a[44:45], v176 offset:20480\n"
"v_mfma_f32_16x16x32_bf16 v[172:175], a[8:11], a[122:125], v[172:175]\n"
"s_nop 0\n"
"buffer_load_dwordx4 v183, s[36:39], s56 offen lds\n"
"v_mfma_f32_16x16x32_bf16 v[172:175], a[12:15], a[134:137], v[172:175]\n"
"s_addk_i32 m0, 4096\n"
"ds_read_b64_tr_b16 a[46:47], v176 offset:22528\n"
"v_add_u32 v178, 4294856704, v178\n"
"v_mfma_f32_16x16x32_bf16 v[172:175], a[16:19], a[126:129], v[172:175]\n"
"s_add_u32 s56, s72, 184448\n"
"ds_read_b64_tr_b16 a[48:49], v177 offset:0\n"
"ds_read_b64_tr_b16 a[50:51], v177 offset:2048\n"
"v_mfma_f32_16x16x32_bf16 v[172:175], a[20:23], a[130:133], v[172:175]\n"
"s_waitcnt lgkmcnt(10)\n"
"ds_read_b64_tr_b16 a[52:53], v177 offset:4096\n"
"ds_read_b64_tr_b16 a[54:55], v177 offset:6144\n"
"v_mfma_f32_16x16x32_bf16 v[112:115], a[24:27], v[0:3], v[112:115]\n"
"s_nop 0\n"
"buffer_load_dwordx4 v183, s[36:39], s56 offen lds\n"
"v_mfma_f32_16x16x32_bf16 v[112:115], a[28:31], v[4:7], v[112:115]\n"
"s_waitcnt lgkmcnt(8)\n"
"s_addk_i32 m0, 4096\n"
"s_add_u32 s56, s73, 0\n"
"v_mfma_f32_16x16x32_bf16 v[112:115], a[32:35], v[8:11], v[112:115]\n"
"s_nop 0\n"
"ds_read_b64_tr_b16 a[56:57], v177 offset:8192\n"
"ds_read_b64_tr_b16 a[58:59], v177 offset:10240\n"
"v_mfma_f32_16x16x32_bf16 v[112:115], a[36:39], v[12:15], v[112:115]\n"
"s_waitcnt lgkmcnt(6)\n"
"ds_read_b64_tr_b16 a[0:1], v177 offset:12288\n"
"ds_read_b64_tr_b16 a[2:3], v177 offset:14336\n"
"v_mfma_f32_16x16x32_bf16 v[112:115], a[40:43], v[16:19], v[112:115]\n"
"s_nop 0\n"
"buffer_load_dwordx4 v183, s[36:39], s56 offen lds\n"
"v_mfma_f32_16x16x32_bf16 v[112:115], a[44:47], v[20:23], v[112:115]\n"
"s_addk_i32 m0, 4096\n"
"s_add_u32 s56, s72, 221184\n"
"ds_read_b64_tr_b16 a[4:5], v177 offset:16384\n"
"v_mfma_f32_16x16x32_bf16 v[116:119], a[24:27], v[56:59], v[116:119]\n"
"s_nop 0\n"
"ds_read_b64_tr_b16 a[6:7], v177 offset:18432\n"
"ds_read_b64_tr_b16 a[8:9], v177 offset:20480\n"
"v_mfma_f32_16x16x32_bf16 v[116:119], a[28:31], v[60:63], v[116:119]\n"
"v_add_u32 v179, 4294856704, v179\n"
"v_mfma_f32_16x16x32_bf16 v[116:119], a[32:35], v[64:67], v[116:119]\n"
"s_nop 0\n"
"buffer_load_dwordx4 v183, s[36:39], s56 offen lds\n"
"v_mfma_f32_16x16x32_bf16 v[116:119], a[36:39], v[68:71], v[116:119]\n"
"s_addk_i32 m0, 4096\n"
"ds_read_b64_tr_b16 a[10:11], v177 offset:22528\n"
"s_add_u32 s56, s72, 258048\n"
"v_mfma_f32_16x16x32_bf16 v[116:119], a[40:43], v[72:75], v[116:119]\n"
"s_nop 0\n"
"ds_read_b64_tr_b16 a[12:13], v178 offset:0\n"
"ds_read_b64_tr_b16 a[14:15], v178 offset:2048\n"
"v_mfma_f32_16x16x32_bf16 v[116:119], a[44:47], v[76:79], v[116:119]\n"
"s_waitcnt lgkmcnt(10)\n"
"ds_read_b64_tr_b16 a[16:17], v178 offset:4096\n"
"ds_read_b64_tr_b16 a[18:19], v178 offset:6144\n"
"v_mfma_f32_16x16x32_bf16 v[120:123], a[48:51], v[0:3], v[120:123]\n"
"s_nop 0\n"
"buffer_load_dwordx4 v183, s[36:39], s56 offen lds\n"
"v_mfma_f32_16x16x32_bf16 v[120:123], a[52:55], v[4:7], v[120:123]\n"
"s_waitcnt lgkmcnt(8)\n"
"s_addk_i32 m0, 4096\n"
"s_add_u32 s56, s74, 0\n"
"v_mfma_f32_16x16x32_bf16 v[120:123], a[56:59], v[8:11], v[120:123]\n"
"s_nop 0\n"
"ds_read_b64_tr_b16 a[20:21], v178 offset:8192\n"
"ds_read_b64_tr_b16 a[22:23], v178 offset:10240\n"
"v_mfma_f32_16x16x32_bf16 v[120:123], a[0:3], v[12:15], v[120:123]\n"
"s_waitcnt lgkmcnt(6)\n"
"ds_read_b64_tr_b16 a[24:25], v178 offset:12288\n"
"ds_read_b64_tr_b16 a[26:27], v178 offset:14336\n"
"v_mfma_f32_16x16x32_bf16 v[120:123], a[4:7], v[16:19], v[120:123]\n"
"s_nop 0\n"
"buffer_load_dwordx4 v184, s[20:23], s56 offen lds\n"
"v_mfma_f32_16x16x32_bf16 v[120:123], a[8:11], v[20:23], v[120:123]\n"
"s_addk_i32 m0, 4096\n"
"s_add_u32 s56, s75, 0\n"
"ds_read_b64_tr_b16 a[28:29], v178 offset:16384\n"
"v_mfma_f32_16x16x32_bf16 v[124:127], a[48:51], v[56:59], v[124:127]\n"
"s_nop 0\n"
"ds_read_b64_tr_b16 a[30:31], v178 offset:18432\n"
"ds_read_b64_tr_b16 a[32:33], v178 offset:20480\n"
"v_mfma_f32_16x16x32_bf16 v[124:127], a[52:55], v[60:63], v[124:127]\n"
"v_add_u32 v200, 4294942720, v200\n"
"v_mfma_f32_16x16x32_bf16 v[124:127], a[56:59], v[64:67], v[124:127]\n"
"s_nop 0\n"
"ds_read_b64_tr_b16 a[34:35], v178 offset:22528\n"
"ds_read_b64_tr_b16 a[36:37], v179 offset:0\n"
"v_mfma_f32_16x16x32_bf16 v[124:127], a[0:3], v[68:71], v[124:127]\n"
"s_nop 0\n"
"buffer_load_dwordx2 a[252:253], off, s[44:47], s80 sc1\n"
"v_mfma_f32_16x16x32_bf16 v[124:127], a[4:7], v[72:75], v[124:127]\n"
"s_nop 0\n"
"ds_read_b64_tr_b16 a[38:39], v179 offset:2048\n"
"ds_read_b64_tr_b16 a[40:41], v179 offset:4096\n"
"v_mfma_f32_16x16x32_bf16 v[124:127], a[8:11], v[76:79], v[124:127]\n"
"s_waitcnt lgkmcnt(11)\n"
"ds_read_b64_tr_b16 a[42:43], v179 offset:6144\n"
"ds_read_b64_tr_b16 a[44:45], v179 offset:8192\n"
"v_mfma_f32_16x16x32_bf16 v[128:131], a[12:15], v[0:3], v[128:131]\n"
"s_nop 0\n"
"ds_read_b64_tr_b16 a[46:47], v179 offset:10240\n"
"ds_read_b64_tr_b16 a[48:49], v179 offset:12288\n"
"v_mfma_f32_16x16x32_bf16 v[128:131], a[16:19], v[4:7], v[128:131]\n"
"s_waitcnt lgkmcnt(11)\n"
"v_add_u32 v201, 4294942720, v201\n"
"v_mfma_f32_16x16x32_bf16 v[128:131], a[20:23], v[8:11], v[128:131]\n"
"s_nop 0\n"
"ds_read_b64_tr_b16 a[50:51], v179 offset:14336\n"
"ds_read_b64_tr_b16 a[52:53], v179 offset:16384\n"
"v_mfma_f32_16x16x32_bf16 v[128:131], a[24:27], v[12:15], v[128:131]\n"
"s_waitcnt lgkmcnt(9)\n"
"ds_read_b64_tr_b16 a[54:55], v179 offset:18432\n"
"ds_read_b64_tr_b16 a[56:57], v179 offset:20480\n"
"v_mfma_f32_16x16x32_bf16 v[128:131], a[28:31], v[16:19], v[128:131]\n"
"ds_read_b64_tr_b16 a[58:59], v179 offset:22528\n"
"v_mfma_f32_16x16x32_bf16 v[128:131], a[32:35], v[20:23], v[128:131]\n"
"v_mfma_f32_16x16x32_bf16 v[132:135], a[12:15], v[56:59], v[132:135]\n"
"v_mfma_f32_16x16x32_bf16 v[132:135], a[16:19], v[60:63], v[132:135]\n"
"v_mfma_f32_16x16x32_bf16 v[132:135], a[20:23], v[64:67], v[132:135]\n"
"v_mfma_f32_16x16x32_bf16 v[132:135], a[24:27], v[68:71], v[132:135]\n"
"s_waitcnt vmcnt(5) lgkmcnt(0)\n"
"buffer_load_dwordx4 v184, s[20:23], s56 offen lds\n"
"v_mfma_f32_16x16x32_bf16 v[132:135], a[28:31], v[72:75], v[132:135]\n"
"s_barrier\n"
"ds_read_b64_tr_b16 a[0:1], v200 offset:0\n"
"s_addk_i32 m0, 4096\n"
"v_mfma_f32_16x16x32_bf16 v[132:135], a[32:35], v[76:79], v[132:135]\n"
"s_add_u32 s56, s73, 128\n"
"buffer_load_dwordx4 v183, s[36:39], s56 offen lds\n"
"v_mfma_f32_16x16x32_bf16 v[136:139], a[36:39], v[0:3], v[136:139]\n"
"s_addk_i32 m0, 4096\n"
"ds_read_b64_tr_b16 a[2:3], v200 offset:1024\n"
"s_add_u32 s56, s72, 221312\n"
"v_mfma_f32_16x16x32_bf16 v[136:139], a[40:43], v[4:7], v[136:139]\n"
"s_nop 0\n"
"ds_read_b64_tr_b16 a[4:5], v200 offset:2048\n"
"ds_read_b64_tr_b16 a[6:7], v200 offset:3072\n"
"v_mfma_f32_16x16x32_bf16 v[136:139], a[44:47], v[8:11], v[136:139]\n"
"s_nop 0\n"
"buffer_load_dwordx4 v183, s[36:39], s56 offen lds\n"
"v_mfma_f32_16x16x32_bf16 v[136:139], a[48:51], v[12:15], v[136:139]\n"
"s_addk_i32 m0, 4096\n"
"ds_read_b64_tr_b16 a[8:9], v200 offset:4096\n"
"s_add_u32 s56, s72, 258176\n"
"v_mfma_f32_16x16x32_bf16 v[136:139], a[52:55], v[16:19], v[136:139]\n"
"s_nop 0\n"
"ds_read_b64_tr_b16 a[10:11], v200 offset:5120\n"
"ds_read_b64_tr_b16 a[12:13], v200 offset:6144\n"
"v_mfma_f32_16x16x32_bf16 v[136:139], a[56:59], v[20:23], v[136:139]\n"
"s_nop 0\n"
"buffer_load_dwordx4 v183, s[36:39], s56 offen lds\n"
"v_mfma_f32_16x16x32_bf16 v[140:143], a[36:39], v[56:59], v[140:143]\n"
"s_addk_i32 m0, 4096\n"
"ds_read_b64_tr_b16 a[14:15], v200 offset:7168\n"
"s_add_u32 s56, s74, 128\n"
"v_mfma_f32_16x16x32_bf16 v[140:143], a[40:43], v[60:63], v[140:143]\n"
"s_nop 0\n"
"ds_read_b64_tr_b16 a[16:17], v201 offset:0\n"
"ds_read_b64_tr_b16 a[18:19], v201 offset:1024\n"
"v_mfma_f32_16x16x32_bf16 v[140:143], a[44:47], v[64:67], v[140:143]\n"
"s_nop 0\n"
"buffer_load_dwordx4 v184, s[20:23], s56 offen lds\n"
"v_mfma_f32_16x16x32_bf16 v[140:143], a[48:51], v[68:71], v[140:143]\n"
"s_addk_i32 m0, 4096\n"
"ds_read_b64_tr_b16 a[20:21], v201 offset:2048\n"
"s_add_u32 s56, s75, 128\n"
"v_mfma_f32_16x16x32_bf16 v[140:143], a[52:55], v[72:75], v[140:143]\n"
"s_nop 0\n"
"ds_read_b64_tr_b16 a[22:23], v201 offset:3072\n"
"ds_read_b64_tr_b16 a[24:25], v201 offset:4096\n"
"v_mfma_f32_16x16x32_bf16 v[140:143], a[56:59], v[76:79], v[140:143]\n"
"s_waitcnt lgkmcnt(9)\n"
"buffer_load_dwordx4 v184, s[20:23], s56 offen lds\n"
"v_mfma_f32_16x16x32_bf16 v[240:243], a[0:3], a[60:63], 0\n"
"s_addk_i32 m0, 4096\n"
"ds_read_b64_tr_b16 a[26:27], v201 offset:5120\n"
"s_add_u32 s56, s70, 256\n"
"v_mfma_f32_16x16x32_bf16 v[240:243], a[4:7], a[64:67], v[240:243]\n"
"s_waitcnt lgkmcnt(6)\n"
"ds_read_b64_tr_b16 a[28:29], v201 offset:6144\n"
"ds_read_b64_tr_b16 a[30:31], v201 offset:7168\n"
"v_mfma_f32_16x16x32_bf16 v[240:243], a[8:11], a[68:71], v[240:243]\n"
"s_nop 0\n"
"buffer_load_dwordx4 v183, s[36:39], s56 offen lds\n"
"v_mfma_f32_16x16x32_bf16 v[240:243], a[12:15], a[72:75], v[240:243]\n"
"s_addk_i32 m0, 4096\n"
"ds_read_b64_tr_b16 a[32:33], v200 offset:8192\n"
"s_add_u32 s56, s70, 37120\n"
"v_mfma_f32_16x16x32_bf16 v[244:247], a[0:3], a[102:105], 0\n"
"s_nop 0\n"
"ds_read_b64_tr_b16 a[34:35], v200 offset:9216\n"
"ds_read_b64_tr_b16 a[36:37], v200 offset:10240\n"
"v_mfma_f32_16x16x32_bf16 v[244:247], a[4:7], a[106:109], v[244:247]\n"
"s_nop 0\n"
"buffer_load_dwordx4 v183, s[36:39], s56 offen lds\n"
"v_mfma_f32_16x16x32_bf16 v[244:247], a[8:11], a[110:113], v[244:247]\n"
"s_addk_i32 m0, 4096\n"
"ds_read_b64_tr_b16 a[38:39], v200 offset:11264\n"
"s_add_u32 s56, s70, 73984\n"
"v_mfma_f32_16x16x32_bf16 v[244:247], a[12:15], a[114:117], v[244:247]\n"
"s_waitcnt lgkmcnt(8)\n"
"ds_read_b64_tr_b16 a[40:41], v200 offset:12288\n"
"ds_read_b64_tr_b16 a[42:43], v200 offset:13312\n"
"v_mfma_f32_16x16x32_bf16 v[248:251], a[16:19], a[60:63], 0\n"
"s_nop 0\n"
"ds_read_b64_tr_b16 a[44:45], v200 offset:14336\n"
"ds_read_b64_tr_b16 a[46:47], v200 offset:15360\n"
"v_mfma_f32_16x16x32_bf16 v[248:251], a[20:23], a[64:67], v[248:251]\n"
"s_waitcnt lgkmcnt(8)\n"
"ds_read_b64_tr_b16 a[48:49], v201 offset:8192\n"
"v_mfma_f32_16x16x32_bf16 v[248:251], a[24:27], a[68:71], v[248:251]\n"
"s_nop 0\n"
"buffer_load_dwordx4 v183, s[36:39], s56 offen lds\n"
"v_mfma_f32_16x16x32_bf16 v[248:251], a[28:31], a[72:75], v[248:251]\n"
"s_addk_i32 m0, 4096\n"
"s_add_u32 s56, s70, 110848\n"
"ds_read_b64_tr_b16 a[50:51], v201 offset:9216\n"
"v_mfma_f32_16x16x32_bf16 v[252:255], a[16:19], a[102:105], 0\n"
"s_nop 0\n"
"ds_read_b64_tr_b16 a[52:53], v201 offset:10240\n"
"ds_read_b64_tr_b16 a[54:55], v201 offset:11264\n"
"v_mfma_f32_16x16x32_bf16 v[252:255], a[20:23], a[106:109], v[252:255]\n"
"ds_read_b64_tr_b16 a[56:57], v201 offset:12288\n"
"v_mfma_f32_16x16x32_bf16 v[252:255], a[24:27], a[110:113], v[252:255]\n"
"ds_read_b64_tr_b16 a[58:59], v201 offset:13312\n"
"v_mfma_f32_16x16x32_bf16 v[252:255], a[28:31], a[114:117], v[252:255]\n"
"s_waitcnt lgkmcnt(10)\n"
"ds_read_b64_tr_b16 a[0:1], v201 offset:14336\n"
"v_mfma_f32_16x16x32_bf16 v[240:243], a[32:35], a[76:79], v[240:243]\n"
"ds_read_b64_tr_b16 a[2:3], v201 offset:15360\n"
"v_mfma_f32_16x16x32_bf16 v[240:243], a[36:39], a[80:83], v[240:243]\n"
"s_waitcnt lgkmcnt(8)\n"
"ds_read_b64_tr_b16 a[4:5], v176 offset:40960\n"
"v_mfma_f32_16x16x32_bf16 v[240:243], a[40:43], a[84:87], v[240:243]\n"
"ds_read_b64_tr_b16 a[6:7], v176 offset:43008\n"
"v_mfma_f32_16x16x32_bf16 v[240:243], a[44:47], a[88:91], v[240:243]\n"
"ds_read_b64_tr_b16 a[8:9], v176 offset:45056\n"
"v_mfma_f32_16x16x32_bf16 v[244:247], a[32:35], a[118:121], v[244:247]\n"
"ds_read_b64_tr_b16 a[10:11], v176 offset:47104\n"
"v_mfma_f32_16x16x32_bf16 v[244:247], a[36:39], a[122:125], v[244:247]\n"
"s_nop 0\n"
"buffer_load_dwordx4 v183, s[36:39], s56 offen lds\n"
"v_mfma_f32_16x16x32_bf16 v[244:247], a[40:43], a[126:129], v[244:247]\n"
"s_addk_i32 m0, 4096\n"
"s_add_u32 s56, s70, 147712\n"
"ds_read_b64_tr_b16 a[12:13], v176 offset:49152\n"
"v_mfma_f32_16x16x32_bf16 v[244:247], a[44:47], a[130:133], v[244:247]\n"
"s_waitcnt lgkmcnt(9)\n"
"ds_read_b64_tr_b16 a[14:15], v176 offset:51200\n"
"ds_read_b64_tr_b16 a[16:17], v176 offset:53248\n"
"v_mfma_f32_16x16x32_bf16 v[248:251], a[48:51], a[76:79], v[248:251]\n"
"ds_read_b64_tr_b16 a[18:19], v176 offset:55296\n"
"v_mfma_f32_16x16x32_bf16 v[248:251], a[52:55], a[80:83], v[248:251]\n"
"s_waitcnt lgkmcnt(8)\n"
"buffer_load_dwordx4 v183, s[36:39], s56 offen lds\n"
"v_mfma_f32_16x16x32_bf16 v[248:251], a[56:59], a[84:87], v[248:251]\n"
"s_addk_i32 m0, 4096\n"
"s_add_u32 s56, s70, 184576\n"
"ds_read_b64_tr_b16 a[20:21], v176 offset:57344\n"
"v_mfma_f32_16x16x32_bf16 v[248:251], a[0:3], a[88:91], v[248:251]\n"
"s_waitcnt vmcnt(11)\n"
"v_accvgpr_read_b32 v203, a252\n"
"v_accvgpr_read_b32 v199, a253\n"
"v_xor_b32 v203, s64, v203\n"
"v_xor_b32 v199, s65, v199\n"
"v_or_b32 v203, v203, v199\n"
"v_cmp_ne_u32 vcc, 0, v203\n"
"s_nop 1\n"
"s_cbranch_vccnz 62f\n"
"63:\n"
"v_mfma_f32_16x16x32_bf16 v[252:255], a[48:51], a[118:121], v[252:255]\n"
"s_add_u32 s80, s78, 32768\n"
"ds_read_b64_tr_b16 a[22:23], v176 offset:59392\n"
"ds_read_b64_tr_b16 a[24:25], v176 offset:61440\n"
"v_mfma_f32_16x16x32_bf16 v[252:255], a[52:55], a[122:125], v[252:255]\n"
"s_nop 0\n"
"buffer_load_dwordx4 v[24:27], v188, s[44:47], s80 offen sc1\n"
"v_mfma_f32_16x16x32_bf16 v[252:255], a[56:59], a[126:129], v[252:255]\n"
"s_add_u32 s80, s78, 6144\n"
"ds_read_b64_tr_b16 a[26:27], v176 offset:63488\n"
"ds_read_b64_tr_b16 a[28:29], v177 offset:40960\n"
"v_mfma_f32_16x16x32_bf16 v[252:255], a[0:3], a[130:133], v[252:255]\n"
"s_waitcnt lgkmcnt(9)\n"
"ds_read_b64_tr_b16 a[30:31], v177 offset:43008\n"
"ds_read_b64_tr_b16 a[32:33], v177 offset:45056\n"
"v_mfma_f32_16x16x32_bf16 v[144:147], a[4:7], v[0:3], v[144:147]\n"
"112:\n"
"s_cbranch_execz 98b\n"
"s_nop 0\n"
"buffer_load_dwordx4 v[80:83], v188, s[44:47], s80 offen sc1\n"
"v_mfma_f32_16x16x32_bf16 v[144:147], a[8:11], v[4:7], v[144:147]\n"
"s_waitcnt lgkmcnt(7)\n"
"s_add_u32 s80, s78, 33792\n"
"ds_read_b64_tr_b16 a[34:35], v177 offset:47104\n"
"v_mfma_f32_16x16x32_bf16 v[144:147], a[12:15], v[8:11], v[144:147]\n"
"s_nop 0\n"
"ds_read_b64_tr_b16 a[36:37], v177 offset:49152\n"
"ds_read_b64_tr_b16 a[38:39], v177 offset:51200\n"
"v_mfma_f32_16x16x32_bf16 v[144:147], a[16:19], v[12:15], v[144:147]\n"
"s_waitcnt lgkmcnt(6)\n"
"ds_read_b64_tr_b16 a[40:41], v177 offset:53248\n"
"v_mfma_f32_16x16x32_bf16 v[144:147], a[20:23], v[16:19], v[144:147]\n"
"s_nop 0\n"
"buffer_load_dwordx4 v[28:31], v188, s[44:47], s80 offen sc1\n"
"v_mfma_f32_16x16x32_bf16 v[144:147], a[24:27], v[20:23], v[144:147]\n"
"s_add_u32 s80, s78, 7168\n"
"ds_read_b64_tr_b16 a[42:43], v177 offset:55296\n"
"ds_read_b64_tr_b16 a[44:45], v177 offset:57344\n"
"v_mfma_f32_16x16x32_bf16 v[148:151], a[4:7], v[56:59], v[148:151]\n"
"ds_read_b64_tr_b16 a[46:47], v177 offset:59392\n"
"v_mfma_f32_16x16x32_bf16 v[148:151], a[8:11], v[60:63], v[148:151]\n"
"ds_read_b64_tr_b16 a[48:49], v177 offset:61440\n"
"v_mfma_f32_16x16x32_bf16 v[148:151], a[12:15], v[64:67], v[148:151]\n"
"ds_read_b64_tr_b16 a[50:51], v177 offset:63488\n"
"v_mfma_f32_16x16x32_bf16 v[148:151], a[16:19], v[68:71], v[148:151]\n"
"s_nop 0\n"
"buffer_load_dwordx4 v[84:87], v188, s[44:47], s80 offen sc1\n"
"v_mfma_f32_16x16x32_bf16 v[148:151], a[20:23], v[72:75], v[148:151]\n"
"s_add_u32 s80, s78, 35840\n"
"ds_read_b64_tr_b16 a[52:53], v178 offset:40960\n"
"ds_read_b64_tr_b16 a[54:55], v178 offset:43008\n"
"v_mfma_f32_16x16x32_bf16 v[148:151], a[24:27], v[76:79], v[148:151]\n"
"s_waitcnt lgkmcnt(10)\n"
"ds_read_b64_tr_b16 a[56:57], v178 offset:45056\n"
"v_mfma_f32_16x16x32_bf16 v[152:155], a[28:31], v[0:3], v[152:155]\n"
"ds_read_b64_tr_b16 a[58:59], v178 offset:47104\n"
"v_mfma_f32_16x16x32_bf16 v[152:155], a[32:35], v[4:7], v[152:155]\n"
"s_waitcnt lgkmcnt(8)\n"
"buffer_load_dwordx4 v[36:39], v188, s[44:47], s80 offen sc1\n"
"v_mfma_f32_16x16x32_bf16 v[152:155], a[36:39], v[8:11], v[152:155]\n"
"s_add_u32 s80, s78, 17408\n"
"ds_read_b64_tr_b16 a[0:1], v178 offset:49152\n"
"ds_read_b64_tr_b16 a[2:3], v178 offset:51200\n"
"v_mfma_f32_16x16x32_bf16 v[152:155], a[40:43], v[12:15], v[152:155]\n"
"s_waitcnt lgkmcnt(6)\n"
"ds_read_b64_tr_b16 a[4:5], v178 offset:53248\n"
"v_mfma_f32_16x16x32_bf16 v[152:155], a[44:47], v[16:19], v[152:155]\n"
"ds_read_b64_tr_b16 a[6:7], v178 offset:55296\n"
"v_mfma_f32_16x16x32_bf16 v[152:155], a[48:51], v[20:23], v[152:155]\n"
"s_nop 0\n"
"buffer_load_dwordx4 v[92:95], v188, s[44:47], s80 offen sc1\n"
"v_mfma_f32_16x16x32_bf16 v[156:159], a[28:31], v[56:59], v[156:159]\n"
"v_lshlrev_b32 v203, 2, v187\n"
"s_add_u32 s80, s78, 36864\n"
"v_mfma_f32_16x16x32_bf16 v[156:159], a[32:35], v[60:63], v[156:159]\n"
"s_nop 0\n"
"ds_read_b64_tr_b16 a[8:9], v178 offset:57344\n"
"ds_read_b64_tr_b16 a[10:11], v178 offset:59392\n"
"v_mfma_f32_16x16x32_bf16 v[156:159], a[36:39], v[64:67], v[156:159]\n"
"s_nop 0\n"
"buffer_load_dword a101, v203, s[44:47], s80 offen offset:256 sc1\n"
"v_mfma_f32_16x16x32_bf16 v[156:159], a[40:43], v[68:71], v[156:159]\n"
"s_nop 0\n"
"ds_read_b64_tr_b16 a[12:13], v178 offset:61440\n"
"ds_read_b64_tr_b16 a[14:15], v178 offset:63488\n"
"v_mfma_f32_16x16x32_bf16 v[156:159], a[44:47], v[72:75], v[156:159]\n"
"s_nop 0\n"
"buffer_load_dword a143, v203, s[44:47], s80 offen offset:0 sc1\n"
"v_mfma_f32_16x16x32_bf16 v[156:159], a[48:51], v[76:79], v[156:159]\n"
"s_waitcnt lgkmcnt(8)\n"
"ds_read_b64_tr_b16 a[16:17], v179 offset:40960\n"
"v_add_u32 v176, 65536, v176\n"
"v_mfma_f32_16x16x32_bf16 v[160:163], a[52:55], v[0:3], v[160:163]\n"
"s_nop 0\n"
"ds_read_b64_tr_b16 a[18:19], v179 offset:43008\n"
"ds_read_b64_tr_b16 a[20:21], v179 offset:45056\n"
"v_mfma_f32_16x16x32_bf16 v[160:163], a[56:59], v[4:7], v[160:163]\n"
"s_waitcnt lgkmcnt(7)\n"
"ds_read_b64_tr_b16 a[22:23], v179 offset:47104\n"
"ds_read_b64_tr_b16 a[24:25], v179 offset:49152\n"
"v_mfma_f32_16x16x32_bf16 v[160:163], a[0:3], v[8:11], v[160:163]\n"
"s_nop 0\n"
"ds_read_b64_tr_b16 a[26:27], v179 offset:51200\n"
"ds_read_b64_tr_b16 a[28:29], v179 offset:53248\n"
"v_mfma_f32_16x16x32_bf16 v[160:163], a[4:7], v[12:15], v[160:163]\n"
"s_waitcnt lgkmcnt(7)\n"
"ds_read_b64_tr_b16 a[30:31], v179 offset:55296\n"
"ds_read_b64_tr_b16 a[32:33], v179 offset:57344\n"
"v_mfma_f32_16x16x32_bf16 v[160:163], a[8:11], v[16:19], v[160:163]\n"
"ds_read_b64_tr_b16 a[34:35], v179 offset:59392\n"
"v_add_u32 v177, 65536, v177\n"
"v_mfma_f32_16x16x32_bf16 v[160:163], a[12:15], v[20:23], v[160:163]\n"
"s_nop 0\n"
"ds_read_b64_tr_b16 a[36:37], v179 offset:61440\n"
"ds_read_b64_tr_b16 a[38:39], v179 offset:63488\n"
"v_mfma_f32_16x16x32_bf16 v[164:167], a[52:55], v[56:59], v[164:167]\n"
"s_nop 0\n"
"buffer_load_dwordx4 v183, s[36:39], s56 offen lds\n"
"v_mfma_f32_16x16x32_bf16 v[164:167], a[56:59], v[60:63], v[164:167]\n"
"s_addk_i32 m0, 4096\n"
"s_add_u32 s56, s71, 256\n"
"v_mfma_f32_16x16x32_bf16 v[164:167], a[0:3], v[64:67], v[164:167]\n"
"s_nop 0\n"
"buffer_load_dwordx4 v183, s[36:39], s56 offen lds\n"
"v_mfma_f32_16x16x32_bf16 v[164:167], a[4:7], v[68:71], v[164:167]\n"
"s_addk_i32 m0, 4096\n"
"s_add_u32 s56, s70, 221440\n"
"v_mfma_f32_16x16x32_bf16 v[164:167], a[8:11], v[72:75], v[164:167]\n"
"s_nop 0\n"
"buffer_load_dwordx4 v183, s[36:39], s56 offen lds\n"
"v_mfma_f32_16x16x32_bf16 v[164:167], a[12:15], v[76:79], v[164:167]\n"
"s_waitcnt vmcnt(16) lgkmcnt(0)\n"
"s_addk_i32 m0, 4096\n"
"s_add_u32 s56, s70, 258304\n"
"v_mfma_f32_16x16x32_bf16 v[168:171], a[16:19], v[0:3], v[168:171]\n"
"s_barrier\n"
"ds_read_b64_tr_b16 a[40:41], v176 offset:0\n"
"ds_read_b64_tr_b16 a[42:43], v176 offset:2048\n"
"v_mfma_f32_16x16x32_bf16 v[168:171], a[20:23], v[4:7], v[168:171]\n"
"ds_read_b64_tr_b16 a[44:45], v176 offset:4096\n"
"v_mfma_f32_16x16x32_bf16 v[168:171], a[24:27], v[8:11], v[168:171]\n"
"ds_read_b64_tr_b16 a[46:47], v176 offset:6144\n"
"v_mfma_f32_16x16x32_bf16 v[168:171], a[28:31], v[12:15], v[168:171]\n"
"ds_read_b64_tr_b16 a[48:49], v176 offset:8192\n"
"v_mfma_f32_16x16x32_bf16 v[168:171], a[32:35], v[16:19], v[168:171]\n"
"ds_read_b64_tr_b16 a[50:51], v176 offset:10240\n"
"v_mfma_f32_16x16x32_bf16 v[168:171], a[36:39], v[20:23], v[168:171]\n"
"ds_read_b64_tr_b16 a[52:53], v176 offset:12288\n"
"v_mfma_f32_16x16x32_bf16 v[172:175], a[16:19], v[56:59], v[172:175]\n"
"ds_read_b64_tr_b16 a[54:55], v176 offset:14336\n"
"v_mfma_f32_16x16x32_bf16 v[172:175], a[20:23], v[60:63], v[172:175]\n"
"ds_read_b64_tr_b16 a[56:57], v176 offset:16384\n"
"v_add_u32 v178, 65536, v178\n"
"v_mfma_f32_16x16x32_bf16 v[172:175], a[24:27], v[64:67], v[172:175]\n"
"ds_read_b64_tr_b16 a[58:59], v176 offset:18432\n"
"v_mfma_f32_16x16x32_bf16 v[172:175], a[28:31], v[68:71], v[172:175]\n"
"v_mfma_f32_16x16x32_bf16 v[172:175], a[32:35], v[72:75], v[172:175]\n"
"ds_read_b64_tr_b16 a[0:1], v202 offset:0\n"
"v_mfma_f32_16x16x32_bf16 v[172:175], a[36:39], v[76:79], v[172:175]\n"
"s_waitcnt lgkmcnt(7)\n"
"ds_read_b64_tr_b16 a[4:5], v177 offset:0\n"
"v_mfma_f32_16x16x32_bf16 v[112:115], a[40:43], v[32:35], v[112:115]\n"
"s_waitcnt vmcnt(10)\n"
"buffer_load_dwordx4 v183, s[36:39], s56 offen lds\n"
"v_mfma_f32_16x16x32_bf16 v[112:115], a[44:47], v[24:27], v[112:115]\n"
"s_waitcnt vmcnt(9) lgkmcnt(4)\n"
"s_add_u32 m0, s55, 0\n"
"s_add_u32 s56, s72, 256\n"
"v_mfma_f32_16x16x32_bf16 v[112:115], a[48:51], v[28:31], v[112:115]\n"
"s_nop 0\n"
"ds_read_b64_tr_b16 a[6:7], v177 offset:2048\n"
"ds_read_b64_tr_b16 a[8:9], v177 offset:4096\n"
"v_mfma_f32_16x16x32_bf16 v[112:115], a[52:55], a[96:99], v[112:115]\n"
"s_waitcnt vmcnt(7) lgkmcnt(4)\n"
"buffer_load_dwordx4 v183, s[36:39], s56 offen lds\n"
"v_mfma_f32_16x16x32_bf16 v[112:115], a[56:59], v[36:39], v[112:115]\n"
"s_addk_i32 m0, 4096\n"
"ds_read_b64_tr_b16 a[10:11], v177 offset:6144\n"
"s_add_u32 s56, s72, 37120\n"
"v_mfma_f32_16x16x32_bf16 v[116:119], a[40:43], v[88:91], v[116:119]\n"
"s_nop 0\n"
"ds_read_b64_tr_b16 a[12:13], v177 offset:8192\n"
"ds_read_b64_tr_b16 a[14:15], v177 offset:10240\n"
"v_mfma_f32_16x16x32_bf16 v[116:119], a[44:47], v[80:83], v[116:119]\n"
"s_nop 0\n"
"buffer_load_dwordx4 v183, s[36:39], s56 offen lds\n"
"v_mfma_f32_16x16x32_bf16 v[116:119], a[48:51], v[84:87], v[116:119]\n"
"s_addk_i32 m0, 4096\n"
"ds_read_b64_tr_b16 a[16:17], v177 offset:12288\n"
"s_add_u32 s56, s72, 73984\n"
"v_mfma_f32_16x16x32_bf16 v[116:119], a[52:55], a[138:141], v[116:119]\n"
"s_waitcnt vmcnt(8)\n"
"ds_read_b64_tr_b16 a[18:19], v177 offset:14336\n"
"ds_read_b64_tr_b16 a[20:21], v177 offset:16384\n"
"v_mfma_f32_16x16x32_bf16 v[116:119], a[56:59], v[92:95], v[116:119]\n"
"s_waitcnt vmcnt(7) lgkmcnt(9)\n"
"buffer_load_dwordx4 v183, s[36:39], s56 offen lds\n"
"v_mfma_f32_16x16x16_bf16 v[112:115], a[0:1], a[100:101], v[112:115]\n"
"s_waitcnt vmcnt(7)\n"
"ds_read_b64_tr_b16 a[22:23], v177 offset:18432\n"
"v_add_u32 v179, 65536, v179\n"
"v_mfma_f32_16x16x16_bf16 v[116:119], a[0:1], a[142:143], v[116:119]\n"
"s_waitcnt lgkmcnt(6)\n"
"s_addk_i32 m0, 4096\n"
"s_add_u32 s56, s72, 110848\n"
"v_mfma_f32_16x16x32_bf16 v[120:123], a[4:7], v[32:35], v[120:123]\n"
"s_nop 0\n"
"ds_read_b64_tr_b16 a[24:25], v202 offset:32\n"
"ds_read_b64_tr_b16 a[28:29], v178 offset:0\n"
"v_mfma_f32_16x16x32_bf16 v[120:123], a[8:11], v[24:27], v[120:123]\n"
"s_waitcnt lgkmcnt(4)\n"
"buffer_load_dwordx4 v183, s[36:39], s56 offen lds\n"
"v_mfma_f32_16x16x32_bf16 v[120:123], a[12:15], v[28:31], v[120:123]\n"
"s_addk_i32 m0, 4096\n"
"ds_read_b64_tr_b16 a[30:31], v178 offset:2048\n"
"s_add_u32 s56, s72, 147712\n"
"v_mfma_f32_16x16x32_bf16 v[120:123], a[16:19], a[96:99], v[120:123]\n"
"s_waitcnt lgkmcnt(3)\n"
"ds_read_b64_tr_b16 a[32:33], v178 offset:4096\n"
"ds_read_b64_tr_b16 a[34:35], v178 offset:6144\n"
"v_mfma_f32_16x16x32_bf16 v[120:123], a[20:23], v[36:39], v[120:123]\n"
"s_nop 0\n"
"buffer_load_dwordx4 v183, s[36:39], s56 offen lds\n"
"v_mfma_f32_16x16x32_bf16 v[124:127], a[4:7], v[88:91], v[124:127]\n"
"s_addk_i32 m0, 4096\n"
"ds_read_b64_tr_b16 a[36:37], v178 offset:8192\n"
"s_add_u32 s56, s72, 184576\n"
"v_mfma_f32_16x16x32_bf16 v[124:127], a[8:11], v[80:83], v[124:127]\n"
"s_nop 0\n"
"ds_read_b64_tr_b16 a[38:39], v178 offset:10240\n"
"ds_read_b64_tr_b16 a[40:41], v178 offset:12288\n"
"v_mfma_f32_16x16x32_bf16 v[124:127], a[12:15], v[84:87], v[124:127]\n"
"s_nop 0\n"
"buffer_load_dwordx4 v183, s[36:39], s56 offen lds\n"
"v_mfma_f32_16x16x32_bf16 v[124:127], a[16:19], a[138:141], v[124:127]\n"
"s_addk_i32 m0, 4096\n"
"ds_read_b64_tr_b16 a[42:43], v178 offset:14336\n"
"s_add_u32 s56, s73, 256\n"
"v_mfma_f32_16x16x32_bf16 v[124:127], a[20:23], v[92:95], v[124:127]\n"
"s_waitcnt lgkmcnt(8)\n"
"ds_read_b64_tr_b16 a[44:45], v178 offset:16384\n"
"ds_read_b64_tr_b16 a[46:47], v178 offset:18432\n"
"v_mfma_f32_16x16x16_bf16 v[120:123], a[24:25], a[100:101], v[120:123]\n"
"s_nop 0\n"
"buffer_load_dwordx4 v183, s[36:39], s56 offen lds\n"
"v_mfma_f32_16x16x16_bf16 v[124:127], a[24:25], a[142:143], v[124:127]\n"
"s_waitcnt lgkmcnt(6)\n"
"v_cvt_pk_bf16_f32 v112, v112, v113\n"
"v_cvt_pk_bf16_f32 v113, v114, v115\n"
"v_mfma_f32_16x16x32_bf16 v[128:131], a[28:31], v[32:35], v[128:131]\n"
"s_addk_i32 m0, 4096\n"
"ds_read_b64_tr_b16 a[48:49], v202 offset:64\n"
"v_cvt_pk_bf16_f32 v114, v120, v121\n"
"v_mfma_f32_16x16x32_bf16 v[128:131], a[32:35], v[24:27], v[128:131]\n"
"s_waitcnt lgkmcnt(3)\n"
"ds_read_b64_tr_b16 a[52:53], v179 offset:0\n"
"v_cvt_pk_bf16_f32 v115, v122, v123\n"
"v_mfma_f32_16x16x32_bf16 v[128:131], a[36:39], v[28:31], v[128:131]\n"
"s_add_u32 s56, s72, 221440\n"
"ds_read_b64_tr_b16 a[54:55], v179 offset:2048\n"
"s_add_u32 s69, s77, 49152\n"
"v_mfma_f32_16x16x32_bf16 v[128:131], a[40:43], a[96:99], v[128:131]\n"
"s_waitcnt lgkmcnt(3)\n"
"ds_read_b64_tr_b16 a[56:57], v179 offset:4096\n"
"ds_read_b64_tr_b16 a[58:59], v179 offset:6144\n"
"v_mfma_f32_16x16x32_bf16 v[128:131], a[44:47], v[36:39], v[128:131]\n"
"s_nop 0\n"
"buffer_store_dwordx4 v[112:115], v182, s[44:47], s69 offen sc1\n"
"v_mfma_f32_16x16x32_bf16 v[132:135], a[28:31], v[88:91], v[132:135]\n"
"s_nop 0\n"
"ds_read_b64_tr_b16 a[0:1], v179 offset:8192\n"
"ds_read_b64_tr_b16 a[2:3], v179 offset:10240\n"
"v_mfma_f32_16x16x32_bf16 v[132:135], a[32:35], v[80:83], v[132:135]\n"
"s_nop 0\n"
"buffer_load_dwordx4 v183, s[36:39], s56 offen lds\n"
"v_mfma_f32_16x16x32_bf16 v[132:135], a[36:39], v[84:87], v[132:135]\n"
"s_addk_i32 m0, 4096\n"
"ds_read_b64_tr_b16 a[4:5], v179 offset:12288\n"
"v_cvt_pk_bf16_f32 v116, v116, v117\n"
"v_mfma_f32_16x16x32_bf16 v[132:135], a[40:43], a[138:141], v[132:135]\n"
"s_add_u32 s56, s72, 258304\n"
"ds_read_b64_tr_b16 a[6:7], v179 offset:14336\n"
"v_cvt_pk_bf16_f32 v117, v118, v119\n"
"v_mfma_f32_16x16x32_bf16 v[132:135], a[44:47], v[92:95], v[132:135]\n"
"s_waitcnt lgkmcnt(8)\n"
"ds_read_b64_tr_b16 a[8:9], v179 offset:16384\n"
"ds_read_b64_tr_b16 a[10:11], v179 offset:18432\n"
"v_mfma_f32_16x16x16_bf16 v[128:131], a[48:49], a[100:101], v[128:131]\n"
"s_nop 0\n"
"buffer_load_dwordx4 v183, s[36:39], s56 offen lds\n"
"v_mfma_f32_16x16x16_bf16 v[132:135], a[48:49], a[142:143], v[132:135]\n"
"s_waitcnt lgkmcnt(6)\n"
"v_cvt_pk_bf16_f32 v118, v124, v125\n"
"v_cvt_pk_bf16_f32 v119, v126, v127\n"
"v_mfma_f32_16x16x32_bf16 v[136:139], a[52:55], v[32:35], v[136:139]\n"
"s_addk_i32 m0, 4096\n"
"ds_read_b64_tr_b16 a[12:13], v202 offset:96\n"
"s_add_u32 s69, s77, 65536\n"
"v_mfma_f32_16x16x32_bf16 v[136:139], a[56:59], v[24:27], v[136:139]\n"
"s_waitcnt lgkmcnt(3)\n"
"ds_read_b64_tr_b16 a[16:17], v176 offset:20480\n"
"ds_read_b64_tr_b16 a[18:19], v176 offset:22528\n"
"v_mfma_f32_16x16x32_bf16 v[136:139], a[0:3], v[28:31], v[136:139]\n"
"s_add_u32 s56, s74, 256\n"
"buffer_store_dwordx4 v[116:119], v182, s[44:47], s69 offen sc1\n"
"v_mfma_f32_16x16x32_bf16 v[136:139], a[4:7], a[96:99], v[136:139]\n"
"s_waitcnt lgkmcnt(3)\n"
"ds_read_b64_tr_b16 a[20:21], v176 offset:24576\n"
"ds_read_b64_tr_b16 a[22:23], v176 offset:26624\n"
"v_mfma_f32_16x16x32_bf16 v[136:139], a[8:11], v[36:39], v[136:139]\n"
"s_nop 0\n"
"buffer_load_dwordx4 v184, s[20:23], s56 offen lds\n"
"v_mfma_f32_16x16x32_bf16 v[140:143], a[52:55], v[88:91], v[140:143]\n"
"s_addk_i32 m0, 4096\n"
"ds_read_b64_tr_b16 a[24:25], v176 offset:28672\n"
"s_add_u32 s56, s75, 256\n"
"v_mfma_f32_16x16x32_bf16 v[140:143], a[56:59], v[80:83], v[140:143]\n"
"s_nop 0\n"
"ds_read_b64_tr_b16 a[26:27], v176 offset:30720\n"
"ds_read_b64_tr_b16 a[28:29], v176 offset:32768\n"
"v_mfma_f32_16x16x32_bf16 v[140:143], a[0:3], v[84:87], v[140:143]\n"
"s_nop 0\n"
"ds_read_b64_tr_b16 a[30:31], v176 offset:34816\n"
"ds_read_b64_tr_b16 a[32:33], v176 offset:36864\n"
"v_mfma_f32_16x16x32_bf16 v[140:143], a[4:7], a[138:141], v[140:143]\n"
"s_nop 0\n"
"buffer_load_dwordx4 v184, s[20:23], s56 offen lds\n"
"v_mfma_f32_16x16x32_bf16 v[140:143], a[8:11], v[92:95], v[140:143]\n"
"s_waitcnt lgkmcnt(9)\n"
"s_addk_i32 m0, 4096\n"
"s_add_u32 s56, s70, 384\n"
"v_mfma_f32_16x16x16_bf16 v[136:139], a[12:13], a[100:101], v[136:139]\n"
"ds_read_b64_tr_b16 a[34:35], v176 offset:38912\n"
"v_cvt_pk_bf16_f32 v128, v128, v129\n"
"v_mfma_f32_16x16x16_bf16 v[140:143], a[12:13], a[142:143], v[140:143]\n"
"s_waitcnt lgkmcnt(6)\n"
"v_cvt_pk_bf16_f32 v129, v130, v131\n"
"v_mfma_f32_16x16x32_bf16 v[144:147], a[16:19], v[32:35], v[144:147]\n"
"s_nop 0\n"
"ds_read_b64_tr_b16 a[36:37], v202 offset:128\n"
"ds_read_b64_tr_b16 a[40:41], v177 offset:20480\n"
"v_mfma_f32_16x16x32_bf16 v[144:147], a[20:23], v[24:27], v[144:147]\n"
"s_waitcnt lgkmcnt(4)\n"
"buffer_load_dwordx4 v183, s[36:39], s56 offen lds\n"
"v_mfma_f32_16x16x32_bf16 v[144:147], a[24:27], v[28:31], v[144:147]\n"
"s_addk_i32 m0, 4096\n"
"ds_read_b64_tr_b16 a[42:43], v177 offset:22528\n"
"v_cvt_pk_bf16_f32 v130, v136, v137\n"
"v_mfma_f32_16x16x32_bf16 v[144:147], a[28:31], a[96:99], v[144:147]\n"
"s_waitcnt lgkmcnt(3)\n"
"ds_read_b64_tr_b16 a[44:45], v177 offset:24576\n"
"v_cvt_pk_bf16_f32 v131, v138, v139\n"
"v_mfma_f32_16x16x32_bf16 v[144:147], a[32:35], v[36:39], v[144:147]\n"
"s_add_u32 s56, s70, 37248\n"
"ds_read_b64_tr_b16 a[46:47], v177 offset:26624\n"
"s_add_u32 s69, s77, 50176\n"
"v_mfma_f32_16x16x32_bf16 v[148:151], a[16:19], v[88:91], v[148:151]\n"
"s_nop 0\n"
"ds_read_b64_tr_b16 a[48:49], v177 offset:28672\n"
"ds_read_b64_tr_b16 a[50:51], v177 offset:30720\n"
"v_mfma_f32_16x16x32_bf16 v[148:151], a[20:23], v[80:83], v[148:151]\n"
"s_nop 0\n"
"buffer_store_dwordx4 v[128:131], v182, s[44:47], s69 offen sc1\n"
"v_mfma_f32_16x16x32_bf16 v[148:151], a[24:27], v[84:87], v[148:151]\n"
"ds_read_b64_tr_b16 a[52:53], v177 offset:32768\n"
"v_cvt_pk_bf16_f32 v132, v132, v133\n"
"v_mfma_f32_16x16x32_bf16 v[148:151], a[28:31], a[138:141], v[148:151]\n"
"s_nop 0\n"
"ds_read_b64_tr_b16 a[54:55], v177 offset:34816\n"
"ds_read_b64_tr_b16 a[56:57], v177 offset:36864\n"
"v_mfma_f32_16x16x32_bf16 v[148:151], a[32:35], v[92:95], v[148:151]\n"
"s_waitcnt lgkmcnt(9)\n"
"buffer_load_dwordx4 v183, s[36:39], s56 offen lds\n"
"v_mfma_f32_16x16x16_bf16 v[144:147], a[36:37], a[100:101], v[144:147]\n"
"s_addk_i32 m0, 4096\n"
"ds_read_b64_tr_b16 a[58:59], v177 offset:38912\n"
"v_cvt_pk_bf16_f32 v133, v134, v135\n"
"v_mfma_f32_16x16x16_bf16 v[148:151], a[36:37], a[142:143], v[148:151]\n"
"s_waitcnt lgkmcnt(6)\n"
"ds_read_b64_tr_b16 a[0:1], v202 offset:160\n"
"v_cvt_pk_bf16_f32 v134, v140, v141\n"
"v_mfma_f32_16x16x32_bf16 v[152:155], a[40:43], v[32:35], v[152:155]\n"
"s_add_u32 s56, s70, 74112\n"
"ds_read_b64_tr_b16 a[4:5], v178 offset:20480\n"
"v_cvt_pk_bf16_f32 v135, v142, v143\n"
"v_mfma_f32_16x16x32_bf16 v[152:155], a[44:47], v[24:27], v[152:155]\n"
"s_waitcnt lgkmcnt(4)\n"
"ds_read_b64_tr_b16 a[6:7], v178 offset:22528\n"
"s_add_u32 s69, s77, 66560\n"
"v_mfma_f32_16x16x32_bf16 v[152:155], a[48:51], v[28:31], v[152:155]\n"
"s_nop 0\n"
"ds_read_b64_tr_b16 a[8:9], v178 offset:24576\n"
"ds_read_b64_tr_b16 a[10:11], v178 offset:26624\n"
"v_mfma_f32_16x16x32_bf16 v[152:155], a[52:55], a[96:99], v[152:155]\n"
"s_waitcnt lgkmcnt(5)\n"
"ds_read_b64_tr_b16 a[12:13], v178 offset:28672\n"
"ds_read_b64_tr_b16 a[14:15], v178 offset:30720\n"
"v_mfma_f32_16x16x32_bf16 v[152:155], a[56:59], v[36:39], v[152:155]\n"
"ds_read_b64_tr_b16 a[16:17], v178 offset:32768\n"
"v_mfma_f32_16x16x32_bf16 v[156:159], a[40:43], v[88:91], v[156:159]\n"
"ds_read_b64_tr_b16 a[18:19], v178 offset:34816\n"
"v_mfma_f32_16x16x32_bf16 v[156:159], a[44:47], v[80:83], v[156:159]\n"
"ds_read_b64_tr_b16 a[20:21], v178 offset:36864\n"
"v_mfma_f32_16x16x32_bf16 v[156:159], a[48:51], v[84:87], v[156:159]\n"
"ds_read_b64_tr_b16 a[22:23], v178 offset:38912\n"
"v_mfma_f32_16x16x32_bf16 v[156:159], a[52:55], a[138:141], v[156:159]\n"
"ds_read_b64_tr_b16 a[24:25], v202 offset:192\n"
"v_mfma_f32_16x16x32_bf16 v[156:159], a[56:59], v[92:95], v[156:159]\n"
"s_waitcnt lgkmcnt(11)\n"
"ds_read_b64_tr_b16 a[28:29], v179 offset:20480\n"
"v_mfma_f32_16x16x16_bf16 v[152:155], a[0:1], a[100:101], v[152:155]\n"
"s_nop 0\n"
"ds_read_b64_tr_b16 a[30:31], v179 offset:22528\n"
"ds_read_b64_tr_b16 a[32:33], v179 offset:24576\n"
"v_mfma_f32_16x16x16_bf16 v[156:159], a[0:1], a[142:143], v[156:159]\n"
"s_waitcnt lgkmcnt(10)\n"
"ds_read_b64_tr_b16 a[34:35], v179 offset:26624\n"
"ds_read_b64_tr_b16 a[36:37], v179 offset:28672\n"
"v_mfma_f32_16x16x32_bf16 v[160:163], a[4:7], v[32:35], v[160:163]\n"
"s_nop 0\n"
"ds_read_b64_tr_b16 a[38:39], v179 offset:30720\n"
"ds_read_b64_tr_b16 a[40:41], v179 offset:32768\n"
"v_mfma_f32_16x16x32_bf16 v[160:163], a[8:11], v[24:27], v[160:163]\n"
"s_waitcnt lgkmcnt(10)\n"
"ds_read_b64_tr_b16 a[42:43], v179 offset:34816\n"
"ds_read_b64_tr_b16 a[44:45], v179 offset:36864\n"
"v_mfma_f32_16x16x32_bf16 v[160:163], a[12:15], v[28:31], v[160:163]\n"
"s_nop 0\n"
"ds_read_b64_tr_b16 a[46:47], v179 offset:38912\n"
"ds_read_b64_tr_b16 a[48:49], v202 offset:224\n"
"v_mfma_f32_16x16x32_bf16 v[160:163], a[16:19], a[96:99], v[160:163]\n"
"s_waitcnt lgkmcnt(12)\n"
"v_mfma_f32_16x16x32_bf16 v[160:163], a[20:23], v[36:39], v[160:163]\n"
"s_nop 0\n"
"buffer_store_dwordx4 v[132:135], v182, s[44:47], s69 offen sc1\n"
"v_mfma_f32_16x16x32_bf16 v[164:167], a[4:7], v[88:91], v[164:167]\n"
"v_cvt_pk_bf16_f32 v144, v144, v145\n"
"v_cvt_pk_bf16_f32 v145, v146, v147\n"
"v_mfma_f32_16x16x32_bf16 v[164:167], a[8:11], v[80:83], v[164:167]\n"
"v_cvt_pk_bf16_f32 v146, v152, v153\n"
"v_cvt_pk_bf16_f32 v147, v154, v155\n"
"v_mfma_f32_16x16x32_bf16 v[164:167], a[12:15], v[84:87], v[164:167]\n"
"s_add_u32 s69, s77, 51200\n"
"buffer_store_dwordx4 v[144:147], v182, s[44:47], s69 offen sc1\n"
"v_mfma_f32_16x16x32_bf16 v[164:167], a[16:19], a[138:141], v[164:167]\n"
"v_cvt_pk_bf16_f32 v148, v148, v149\n"
"v_cvt_pk_bf16_f32 v149, v150, v151\n"
"v_mfma_f32_16x16x32_bf16 v[164:167], a[20:23], v[92:95], v[164:167]\n"
"s_waitcnt vmcnt(18) lgkmcnt(0)\n"
"v_cvt_pk_bf16_f32 v150, v156, v157\n"
"v_cvt_pk_bf16_f32 v151, v158, v159\n"
"v_mfma_f32_16x16x16_bf16 v[160:163], a[24:25], a[100:101], v[160:163]\n"
"s_barrier\n"
"ds_read_b64_tr_b16 a[52:53], v176 offset:40960\n"
"s_add_u32 s69, s77, 67584\n"
"v_mfma_f32_16x16x16_bf16 v[164:167], a[24:25], a[142:143], v[164:167]\n"
"s_nop 0\n"
"buffer_store_dwordx4 v[148:151], v182, s[44:47], s69 offen sc1\n"
"v_mfma_f32_16x16x32_bf16 v[168:171], a[28:31], v[32:35], v[168:171]\n"
"s_nop 0\n"
"ds_read_b64_tr_b16 a[54:55], v176 offset:43008\n"
"ds_read_b64_tr_b16 a[56:57], v176 offset:45056\n"
"v_mfma_f32_16x16x32_bf16 v[168:171], a[32:35], v[24:27], v[168:171]\n"
"s_nop 0\n"
"buffer_load_dwordx4 v183, s[36:39], s56 offen lds\n"
"v_mfma_f32_16x16x32_bf16 v[168:171], a[36:39], v[28:31], v[168:171]\n"
"s_addk_i32 m0, 4096\n"
"ds_read_b64_tr_b16 a[58:59], v176 offset:47104\n"
"s_add_u32 s56, s70, 110976\n"
"v_mfma_f32_16x16x32_bf16 v[168:171], a[40:43], a[96:99], v[168:171]\n"
"s_nop 0\n"
"ds_read_b64_tr_b16 a[0:1], v176 offset:49152\n"
"ds_read_b64_tr_b16 a[2:3], v176 offset:51200\n"
"v_mfma_f32_16x16x32_bf16 v[168:171], a[44:47], v[36:39], v[168:171]\n"
"s_nop 0\n"
"buffer_load_dwordx4 v183, s[36:39], s56 offen lds\n"
"v_mfma_f32_16x16x32_bf16 v[172:175], a[28:31], v[88:91], v[172:175]\n"
"s_addk_i32 m0, 4096\n"
"ds_read_b64_tr_b16 a[4:5], v176 offset:53248\n"
"s_add_u32 s56, s70, 147840\n"
"v_mfma_f32_16x16x32_bf16 v[172:175], a[32:35], v[80:83], v[172:175]\n"
"s_nop 0\n"
"ds_read_b64_tr_b16 a[6:7], v176 offset:55296\n"
"ds_read_b64_tr_b16 a[8:9], v176 offset:57344\n"
"v_mfma_f32_16x16x32_bf16 v[172:175], a[36:39], v[84:87], v[172:175]\n"
"s_nop 0\n"
"ds_read_b64_tr_b16 a[10:11], v176 offset:59392\n"
"ds_read_b64_tr_b16 a[12:13], v177 offset:40960\n"
"v_mfma_f32_16x16x32_bf16 v[172:175], a[40:43], a[138:141], v[172:175]\n"
"s_nop 0\n"
"buffer_load_dwordx4 v183, s[36:39], s56 offen lds\n"
"v_mfma_f32_16x16x32_bf16 v[172:175], a[44:47], v[92:95], v[172:175]\n"
"s_addk_i32 m0, 4096\n"
"s_add_u32 s56, s70, 184704\n"
"ds_read_b64_tr_b16 a[14:15], v177 offset:43008\n"
"v_mfma_f32_16x16x16_bf16 v[168:171], a[48:49], a[100:101], v[168:171]\n"
"v_cvt_pk_bf16_f32 v160, v160, v161\n"
"v_cvt_pk_bf16_f32 v161, v162, v163\n"
"v_mfma_f32_16x16x16_bf16 v[172:175], a[48:49], a[142:143], v[172:175]\n"
"s_waitcnt lgkmcnt(8)\n"
"ds_read_b64_tr_b16 a[16:17], v177 offset:45056\n"
"ds_read_b64_tr_b16 a[18:19], v177 offset:47104\n"
"v_mfma_f32_16x16x32_bf16 v[112:115], a[52:55], a[60:63], 0\n"
"s_nop 0\n"
"buffer_load_dwordx4 v183, s[36:39], s56 offen lds\n"
"v_mfma_f32_16x16x32_bf16 v[112:115], a[56:59], a[64:67], v[112:115]\n"
"s_waitcnt lgkmcnt(6)\n"
"ds_read_b64_tr_b16 a[20:21], v177 offset:49152\n"
"v_cvt_pk_bf16_f32 v162, v168, v169\n"
"v_mfma_f32_16x16x32_bf16 v[112:115], a[0:3], a[68:71], v[112:115]\n"
"s_addk_i32 m0, 4096\n"
"ds_read_b64_tr_b16 a[22:23], v177 offset:51200\n"
"v_cvt_pk_bf16_f32 v163, v170, v171\n"
"v_mfma_f32_16x16x32_bf16 v[112:115], a[4:7], a[72:75], v[112:115]\n"
"s_waitcnt lgkmcnt(6)\n"
"ds_read_b64_tr_b16 a[24:25], v177 offset:53248\n"
"s_add_u32 s69, s77, 52224\n"
"v_mfma_f32_16x16x32_bf16 v[112:115], a[8:11], a[76:79], v[112:115]\n"
"s_add_u32 s56, s71, 384\n"
"ds_read_b64_tr_b16 a[26:27], v177 offset:55296\n"
"ds_read_b64_tr_b16 a[28:29], v177 offset:57344\n"
"v_mfma_f32_16x16x32_bf16 v[116:119], a[52:55], a[102:105], 0\n"
"s_nop 0\n"
"ds_read_b64_tr_b16 a[30:31], v177 offset:59392\n"
"ds_read_b64_tr_b16 a[32:33], v178 offset:40960\n"
"v_mfma_f32_16x16x32_bf16 v[116:119], a[56:59], a[106:109], v[116:119]\n"
"ds_read_b64_tr_b16 a[34:35], v178 offset:43008\n"
"v_mfma_f32_16x16x32_bf16 v[116:119], a[0:3], a[110:113], v[116:119]\n"
"ds_read_b64_tr_b16 a[36:37], v178 offset:45056\n"
"v_mfma_f32_16x16x32_bf16 v[116:119], a[4:7], a[114:117], v[116:119]\n"
"ds_read_b64_tr_b16 a[38:39], v178 offset:47104\n"
"v_mfma_f32_16x16x32_bf16 v[116:119], a[8:11], a[118:121], v[116:119]\n"
"s_waitcnt lgkmcnt(10)\n"
"ds_read_b64_tr_b16 a[40:41], v178 offset:49152\n"
"v_mfma_f32_16x16x32_bf16 v[120:123], a[12:15], a[60:63], 0\n"
"ds_read_b64_tr_b16 a[42:43], v178 offset:51200\n"
"v_mfma_f32_16x16x32_bf16 v[120:123], a[16:19], a[64:67], v[120:123]\n"
"s_waitcnt lgkmcnt(8)\n"
"ds_read_b64_tr_b16 a[44:45], v178 offset:53248\n"
"v_add_u32 v176, 61440, v176\n"
"v_mfma_f32_16x16x32_bf16 v[120:123], a[20:23], a[68:71], v[120:123]\n"
"ds_read_b64_tr_b16 a[46:47], v178 offset:55296\n"
"v_mfma_f32_16x16x32_bf16 v[120:123], a[24:27], a[72:75], v[120:123]\n"
"s_waitcnt lgkmcnt(8)\n"
"ds_read_b64_tr_b16 a[48:49], v178 offset:57344\n"
"v_mfma_f32_16x16x32_bf16 v[120:123], a[28:31], a[76:79], v[120:123]\n"
"ds_read_b64_tr_b16 a[50:51], v178 offset:59392\n"
"v_mfma_f32_16x16x32_bf16 v[124:127], a[12:15], a[102:105], 0\n"
"ds_read_b64_tr_b16 a[52:53], v179 offset:40960\n"
"v_mfma_f32_16x16x32_bf16 v[124:127], a[16:19], a[106:109], v[124:127]\n"
"ds_read_b64_tr_b16 a[54:55], v179 offset:43008\n"
"v_mfma_f32_16x16x32_bf16 v[124:127], a[20:23], a[110:113], v[124:127]\n"
"ds_read_b64_tr_b16 a[56:57], v179 offset:45056\n"
"v_mfma_f32_16x16x32_bf16 v[124:127], a[24:27], a[114:117], v[124:127]\n"
"ds_read_b64_tr_b16 a[58:59], v179 offset:47104\n"
"v_mfma_f32_16x16x32_bf16 v[124:127], a[28:31], a[118:121], v[124:127]\n"
"s_waitcnt lgkmcnt(10)\n"
"ds_read_b64_tr_b16 a[0:1], v179 offset:49152\n"
"v_add_u32 v177, 61440, v177\n"
"v_mfma_f32_16x16x32_bf16 v[128:131], a[32:35], a[60:63], 0\n"
"s_nop 0\n"
"buffer_store_dwordx4 v[160:163], v182, s[44:47], s69 offen sc1\n"
"v_mfma_f32_16x16x32_bf16 v[128:131], a[36:39], a[64:67], v[128:131]\n"
"s_waitcnt lgkmcnt(7)\n"
"v_cvt_pk_bf16_f32 v164, v164, v165\n"
"v_cvt_pk_bf16_f32 v165, v166, v167\n"
"v_mfma_f32_16x16x32_bf16 v[128:131], a[40:43], a[68:71], v[128:131]\n"
"ds_read_b64_tr_b16 a[2:3], v179 offset:51200\n"
"v_cvt_pk_bf16_f32 v166, v172, v173\n"
"v_mfma_f32_16x16x32_bf16 v[128:131], a[44:47], a[72:75], v[128:131]\n"
"s_waitcnt lgkmcnt(6)\n"
"ds_read_b64_tr_b16 a[4:5], v179 offset:53248\n"
"ds_read_b64_tr_b16 a[6:7], v179 offset:55296\n"
"v_mfma_f32_16x16x32_bf16 v[128:131], a[48:51], a[76:79], v[128:131]\n"
"s_nop 0\n"
"buffer_load_dwordx4 v183, s[36:39], s56 offen lds\n"
"v_mfma_f32_16x16x32_bf16 v[132:135], a[32:35], a[102:105], 0\n"
"s_addk_i32 m0, 4096\n"
"ds_read_b64_tr_b16 a[8:9], v179 offset:57344\n"
"v_cvt_pk_bf16_f32 v167, v174, v175\n"
"v_mfma_f32_16x16x32_bf16 v[132:135], a[36:39], a[106:109], v[132:135]\n"
"s_add_u32 s56, s70, 221568\n"
"ds_read_b64_tr_b16 a[10:11], v179 offset:59392\n"
"s_add_u32 s69, s77, 68608\n"
"v_mfma_f32_16x16x32_bf16 v[132:135], a[40:43], a[110:113], v[132:135]\n"
"s_nop 0\n"
"ds_read_b64_tr_b16 a[12:13], v176 offset:0\n"
"ds_read_b64_tr_b16 a[14:15], v176 offset:2048\n"
"v_mfma_f32_16x16x32_bf16 v[132:135], a[44:47], a[114:117], v[132:135]\n"
"s_nop 0\n"
"buffer_store_dwordx4 v[164:167], v182, s[44:47], s69 offen sc1\n"
"v_mfma_f32_16x16x32_bf16 v[132:135], a[48:51], a[118:121], v[132:135]\n"
"s_waitcnt lgkmcnt(8)\n"
"ds_read_b64_tr_b16 a[16:17], v176 offset:4096\n"
"v_add_u32 v178, 61440, v178\n"
"v_mfma_f32_16x16x32_bf16 v[136:139], a[52:55], a[60:63], 0\n"
"113:\n"
"s_cbranch_execz 98b\n"
"s_nop 0\n"
"ds_read_b64_tr_b16 a[18:19], v176 offset:6144\n"
"ds_read_b64_tr_b16 a[20:21], v176 offset:8192\n"
"v_mfma_f32_16x16x32_bf16 v[136:139], a[56:59], a[64:67], v[136:139]\n"
"s_waitcnt lgkmcnt(7)\n"
"buffer_load_dwordx4 v183, s[36:39], s56 offen lds\n"
"v_mfma_f32_16x16x32_bf16 v[136:139], a[0:3], a[68:71], v[136:139]\n"
"s_addk_i32 m0, 4096\n"
"ds_read_b64_tr_b16 a[22:23], v176 offset:10240\n"
"s_add_u32 s56, s70, 258432\n"
"v_mfma_f32_16x16x32_bf16 v[136:139], a[4:7], a[72:75], v[136:139]\n"
"s_waitcnt lgkmcnt(6)\n"
"ds_read_b64_tr_b16 a[24:25], v176 offset:12288\n"
"ds_read_b64_tr_b16 a[26:27], v176 offset:14336\n"
"v_mfma_f32_16x16x32_bf16 v[136:139], a[8:11], a[76:79], v[136:139]\n"
"s_nop 0\n"
"ds_read_b64_tr_b16 a[28:29], v177 offset:0\n"
"ds_read_b64_tr_b16 a[30:31], v177 offset:2048\n"
"v_mfma_f32_16x16x32_bf16 v[140:143], a[52:55], a[102:105], 0\n"
"s_nop 0\n"
"buffer_load_dwordx4 v183, s[36:39], s56 offen lds\n"
"v_mfma_f32_16x16x32_bf16 v[140:143], a[56:59], a[106:109], v[140:143]\n"
"s_addk_i32 m0, 4096\n"
"s_add_u32 s56, s72, 384\n"
"ds_read_b64_tr_b16 a[32:33], v177 offset:4096\n"
"v_mfma_f32_16x16x32_bf16 v[140:143], a[0:3], a[110:113], v[140:143]\n"
"v_add_u32 v179, 61440, v179\n"
"v_mfma_f32_16x16x32_bf16 v[140:143], a[4:7], a[114:117], v[140:143]\n"
"s_nop 0\n"
"ds_read_b64_tr_b16 a[34:35], v177 offset:6144\n"
"ds_read_b64_tr_b16 a[36:37], v177 offset:8192\n"
"v_mfma_f32_16x16x32_bf16 v[140:143], a[8:11], a[118:121], v[140:143]\n"
"s_waitcnt lgkmcnt(9)\n"
"buffer_load_dwordx4 v183, s[36:39], s56 offen lds\n"
"v_mfma_f32_16x16x32_bf16 v[112:115], a[12:15], a[80:83], v[112:115]\n"
"s_addk_i32 m0, 4096\n"
"ds_read_b64_tr_b16 a[38:39], v177 offset:10240\n"
"s_add_u32 s56, s72, 37248\n"
"v_mfma_f32_16x16x32_bf16 v[112:115], a[16:19], a[92:95], v[112:115]\n"
"s_waitcnt lgkmcnt(6)\n"
"ds_read_b64_tr_b16 a[40:41], v177 offset:12288\n"
"ds_read_b64_tr_b16 a[42:43], v177 offset:14336\n"
"v_mfma_f32_16x16x32_bf16 v[112:115], a[20:23], a[84:87], v[112:115]\n"
"s_nop 0\n"
"ds_read_b64_tr_b16 a[44:45], v178 offset:0\n"
"ds_read_b64_tr_b16 a[46:47], v178 offset:2048\n"
"v_mfma_f32_16x16x32_bf16 v[112:115], a[24:27], a[88:91], v[112:115]\n"
"ds_read_b64_tr_b16 a[48:49], v178 offset:4096\n"
"v_add_u32 v176, 4294840320, v176\n"
"v_mfma_f32_16x16x32_bf16 v[116:119], a[12:15], a[122:125], v[116:119]\n"
"s_nop 0\n"
"buffer_load_dwordx4 v183, s[36:39], s56 offen lds\n"
"v_mfma_f32_16x16x32_bf16 v[116:119], a[16:19], a[134:137], v[116:119]\n"
"s_addk_i32 m0, 4096\n"
"s_add_u32 s56, s72, 74112\n"
"ds_read_b64_tr_b16 a[50:51], v178 offset:6144\n"
"v_mfma_f32_16x16x32_bf16 v[116:119], a[20:23], a[126:129], v[116:119]\n"
"s_nop 0\n"
"ds_read_b64_tr_b16 a[52:53], v178 offset:8192\n"
"ds_read_b64_tr_b16 a[54:55], v178 offset:10240\n"
"v_mfma_f32_16x16x32_bf16 v[116:119], a[24:27], a[130:133], v[116:119]\n"
"s_waitcnt lgkmcnt(10)\n"
"ds_read_b64_tr_b16 a[56:57], v178 offset:12288\n"
"ds_read_b64_tr_b16 a[58:59], v178 offset:14336\n"
"v_mfma_f32_16x16x32_bf16 v[120:123], a[28:31], a[80:83], v[120:123]\n"
"s_nop 0\n"
"buffer_load_dwordx4 v183, s[36:39], s56 offen lds\n"
"v_mfma_f32_16x16x32_bf16 v[120:123], a[32:35], a[92:95], v[120:123]\n"
"s_waitcnt lgkmcnt(8)\n"
"s_addk_i32 m0, 4096\n"
"s_add_u32 s56, s72, 110976\n"
"v_mfma_f32_16x16x32_bf16 v[120:123], a[36:39], a[84:87], v[120:123]\n"
"s_nop 0\n"
"ds_read_b64_tr_b16 a[0:1], v179 offset:0\n"
"ds_read_b64_tr_b16 a[2:3], v179 offset:2048\n"
"v_mfma_f32_16x16x32_bf16 v[120:123], a[40:43], a[88:91], v[120:123]\n"
"s_nop 0\n"
"ds_read_b64_tr_b16 a[4:5], v179 offset:4096\n"
"ds_read_b64_tr_b16 a[6:7], v179 offset:6144\n"
"v_mfma_f32_16x16x32_bf16 v[124:127], a[28:31], a[122:125], v[124:127]\n"
"s_nop 0\n"
"ds_read_b64_tr_b16 a[8:9], v179 offset:8192\n"
"ds_read_b64_tr_b16 a[10:11], v179 offset:10240\n"
"v_mfma_f32_16x16x32_bf16 v[124:127], a[32:35], a[134:137], v[124:127]\n"
"s_nop 0\n"
"ds_read_b64_tr_b16 a[12:13], v179 offset:12288\n"
"ds_read_b64_tr_b16 a[14:15], v179 offset:14336\n"
"v_mfma_f32_16x16x32_bf16 v[124:127], a[36:39], a[126:129], v[124:127]\n"
"s_nop 0\n"
"buffer_load_dwordx4 v183, s[36:39], s56 offen lds\n"
"v_mfma_f32_16x16x32_bf16 v[124:127], a[40:43], a[130:133], v[124:127]\n"
"s_waitcnt lgkmcnt(12)\n"
"v_add_u32 v177, 4294840320, v177\n"
"s_addk_i32 m0, 4096\n"
"v_mfma_f32_16x16x32_bf16 v[128:131], a[44:47], a[80:83], v[128:131]\n"
"s_add_u32 s56, s72, 147840\n"
"v_mfma_f32_16x16x32_bf16 v[128:131], a[48:51], a[92:95], v[128:131]\n"
"s_waitcnt lgkmcnt(8)\n"
"v_mfma_f32_16x16x32_bf16 v[128:131], a[52:55], a[84:87], v[128:131]\n"
"s_waitcnt vmcnt(19) lgkmcnt(0)\n"
"v_mfma_f32_16x16x32_bf16 v[128:131], a[56:59], a[88:91], v[128:131]\n"
"s_barrier\n"
"ds_read_b64_tr_b16 a[16:17], v176 offset:0\n"
"ds_read_b64_tr_b16 a[18:19], v176 offset:2048\n"
"v_mfma_f32_16x16x32_bf16 v[132:135], a[44:47], a[122:125], v[132:135]\n"
"s_nop 0\n"
"ds_read_b64_tr_b16 a[20:21], v176 offset:4096\n"
"ds_read_b64_tr_b16 a[22:23], v176 offset:6144\n"
"v_mfma_f32_16x16x32_bf16 v[132:135], a[48:51], a[134:137], v[132:135]\n"
"ds_read_b64_tr_b16 a[24:25], v176 offset:8192\n"
"v_mfma_f32_16x16x32_bf16 v[132:135], a[52:55], a[126:129], v[132:135]\n"
"ds_read_b64_tr_b16 a[26:27], v176 offset:10240\n"
"v_mfma_f32_16x16x32_bf16 v[132:135], a[56:59], a[130:133], v[132:135]\n"
"ds_read_b64_tr_b16 a[28:29], v176 offset:12288\n"
"v_mfma_f32_16x16x32_bf16 v[136:139], a[0:3], a[80:83], v[136:139]\n"
"ds_read_b64_tr_b16 a[30:31], v176 offset:14336\n"
"v_mfma_f32_16x16x32_bf16 v[136:139], a[4:7], a[92:95], v[136:139]\n"
"ds_read_b64_tr_b16 a[32:33], v176 offset:16384\n"
"v_mfma_f32_16x16x32_bf16 v[136:139], a[8:11], a[84:87], v[136:139]\n"
"ds_read_b64_tr_b16 a[34:35], v176 offset:18432\n"
"v_mfma_f32_16x16x32_bf16 v[136:139], a[12:15], a[88:91], v[136:139]\n"
"ds_read_b64_tr_b16 a[36:37], v176 offset:20480\n"
"v_add_u32 v178, 4294840320, v178\n"
"v_mfma_f32_16x16x32_bf16 v[140:143], a[0:3], a[122:125], v[140:143]\n"
"ds_read_b64_tr_b16 a[38:39], v176 offset:22528\n"
"v_mfma_f32_16x16x32_bf16 v[140:143], a[4:7], a[134:137], v[140:143]\n"
"ds_read_b64_tr_b16 a[40:41], v177 offset:0\n"
"v_mfma_f32_16x16x32_bf16 v[140:143], a[8:11], a[126:129], v[140:143]\n"
"ds_read_b64_tr_b16 a[42:43], v177 offset:2048\n"
"v_mfma_f32_16x16x32_bf16 v[140:143], a[12:15], a[130:133], v[140:143]\n"
"s_waitcnt lgkmcnt(10)\n"
"buffer_load_dwordx4 v183, s[36:39], s56 offen lds\n"
"v_mfma_f32_16x16x32_bf16 v[112:115], a[16:19], v[0:3], v[112:115]\n"
"s_addk_i32 m0, 4096\n"
"s_add_u32 s56, s72, 184704\n"
"ds_read_b64_tr_b16 a[44:45], v177 offset:4096\n"
"v_mfma_f32_16x16x32_bf16 v[112:115], a[20:23], v[4:7], v[112:115]\n"
"s_waitcnt lgkmcnt(7)\n"
"ds_read_b64_tr_b16 a[46:47], v177 offset:6144\n"
"ds_read_b64_tr_b16 a[48:49], v177 offset:8192\n"
"v_mfma_f32_16x16x32_bf16 v[112:115], a[24:27], v[8:11], v[112:115]\n"
"ds_read_b64_tr_b16 a[50:51], v177 offset:10240\n"
"v_mfma_f32_16x16x32_bf16 v[112:115], a[28:31], v[12:15], v[112:115]\n"
"s_waitcnt lgkmcnt(6)\n"
"buffer_load_dwordx4 v183, s[36:39], s56 offen lds\n"
"v_mfma_f32_16x16x32_bf16 v[112:115], a[32:35], v[16:19], v[112:115]\n"
"s_addk_i32 m0, 4096\n"
"s_add_u32 s56, s73, 384\n"
"ds_read_b64_tr_b16 a[52:53], v177 offset:12288\n"
"v_mfma_f32_16x16x32_bf16 v[112:115], a[36:39], v[20:23], v[112:115]\n"
"s_nop 0\n"
"ds_read_b64_tr_b16 a[54:55], v177 offset:14336\n"
"ds_read_b64_tr_b16 a[56:57], v177 offset:16384\n"
"v_mfma_f32_16x16x32_bf16 v[116:119], a[16:19], v[56:59], v[116:119]\n"
"ds_read_b64_tr_b16 a[58:59], v177 offset:18432\n"
"v_mfma_f32_16x16x32_bf16 v[116:119], a[20:23], v[60:63], v[116:119]\n"
"ds_read_b64_tr_b16 a[0:1], v177 offset:20480\n"
"v_add_u32 v179, 4294840320, v179\n"
"v_mfma_f32_16x16x32_bf16 v[116:119], a[24:27], v[64:67], v[116:119]\n"
"s_nop 0\n"
"buffer_load_dwordx4 v183, s[36:39], s56 offen lds\n"
"v_mfma_f32_16x16x32_bf16 v[116:119], a[28:31], v[68:71], v[116:119]\n"
"s_addk_i32 m0, 4096\n"
"s_add_u32 s56, s72, 221568\n"
"ds_read_b64_tr_b16 a[2:3], v177 offset:22528\n"
"v_mfma_f32_16x16x32_bf16 v[116:119], a[32:35], v[72:75], v[116:119]\n"
"s_nop 0\n"
"ds_read_b64_tr_b16 a[4:5], v178 offset:0\n"
"ds_read_b64_tr_b16 a[6:7], v178 offset:2048\n"
"v_mfma_f32_16x16x32_bf16 v[116:119], a[36:39], v[76:79], v[116:119]\n"
"s_waitcnt lgkmcnt(10)\n"
"ds_read_b64_tr_b16 a[8:9], v178 offset:4096\n"
"v_mfma_f32_16x16x32_bf16 v[120:123], a[40:43], v[0:3], v[120:123]\n"
"s_nop 0\n"
"buffer_load_dwordx4 v183, s[36:39], s56 offen lds\n"
"v_mfma_f32_16x16x32_bf16 v[120:123], a[44:47], v[4:7], v[120:123]\n"
"s_waitcnt lgkmcnt(7)\n"
"s_addk_i32 m0, 4096\n"
"s_add_u32 s56, s72, 258432\n"
"v_mfma_f32_16x16x32_bf16 v[120:123], a[48:51], v[8:11], v[120:123]\n"
"s_nop 0\n"
"ds_read_b64_tr_b16 a[10:11], v178 offset:6144\n"
"ds_read_b64_tr_b16 a[12:13], v178 offset:8192\n"
"v_mfma_f32_16x16x32_bf16 v[120:123], a[52:55], v[12:15], v[120:123]\n"
"s_waitcnt lgkmcnt(5)\n"
"ds_read_b64_tr_b16 a[14:15], v178 offset:10240\n"
"ds_read_b64_tr_b16 a[16:17], v178 offset:12288\n"
"v_mfma_f32_16x16x32_bf16 v[120:123], a[56:59], v[16:19], v[120:123]\n"
"s_nop 0\n"
"buffer_load_dwordx4 v183, s[36:39], s56 offen lds\n"
"v_mfma_f32_16x16x32_bf16 v[120:123], a[0:3], v[20:23], v[120:123]\n"
"s_addk_i32 m0, 4096\n"
"s_add_u32 s56, s74, 384\n"
"ds_read_b64_tr_b16 a[18:19], v178 offset:14336\n"
"v_mfma_f32_16x16x32_bf16 v[124:127], a[40:43], v[56:59], v[124:127]\n"
"s_nop 0\n"
"ds_read_b64_tr_b16 a[20:21], v178 offset:16384\n"
"ds_read_b64_tr_b16 a[22:23], v178 offset:18432\n"
"v_mfma_f32_16x16x32_bf16 v[124:127], a[44:47], v[60:63], v[124:127]\n"
"ds_read_b64_tr_b16 a[24:25], v178 offset:20480\n"
"v_mfma_f32_16x16x32_bf16 v[124:127], a[48:51], v[64:67], v[124:127]\n"
"ds_read_b64_tr_b16 a[26:27], v178 offset:22528\n"
"v_mfma_f32_16x16x32_bf16 v[124:127], a[52:55], v[68:71], v[124:127]\n"
"s_nop 0\n"
"buffer_load_dwordx4 v184, s[20:23], s56 offen lds\n"
"v_mfma_f32_16x16x32_bf16 v[124:127], a[56:59], v[72:75], v[124:127]\n"
"s_addk_i32 m0, 4096\n"
"s_add_u32 s56, s75, 384\n"
"ds_read_b64_tr_b16 a[28:29], v179 offset:0\n"
"v_mfma_f32_16x16x32_bf16 v[124:127], a[0:3], v[76:79], v[124:127]\n"
"s_waitcnt lgkmcnt(9)\n"
"ds_read_b64_tr_b16 a[30:31], v179 offset:2048\n"
"ds_read_b64_tr_b16 a[32:33], v179 offset:4096\n"
"v_mfma_f32_16x16x32_bf16 v[128:131], a[4:7], v[0:3], v[128:131]\n"
"ds_read_b64_tr_b16 a[34:35], v179 offset:6144\n"
"v_mfma_f32_16x16x32_bf16 v[128:131], a[8:11], v[4:7], v[128:131]\n"
"s_waitcnt lgkmcnt(8)\n"
"buffer_load_dwordx4 v184, s[20:23], s56 offen lds\n"
"v_mfma_f32_16x16x32_bf16 v[128:131], a[12:15], v[8:11], v[128:131]\n"
"s_nop 0\n"
"ds_read_b64_tr_b16 a[36:37], v179 offset:8192\n"
"ds_read_b64_tr_b16 a[38:39], v179 offset:10240\n"
"v_mfma_f32_16x16x32_bf16 v[128:131], a[16:19], v[12:15], v[128:131]\n"
"s_waitcnt lgkmcnt(6)\n"
"ds_read_b64_tr_b16 a[40:41], v179 offset:12288\n"
"v_mfma_f32_16x16x32_bf16 v[128:131], a[20:23], v[16:19], v[128:131]\n"
"ds_read_b64_tr_b16 a[42:43], v179 offset:14336\n"
"v_mfma_f32_16x16x32_bf16 v[128:131], a[24:27], v[20:23], v[128:131]\n"
"ds_read_b64_tr_b16 a[44:45], v179 offset:16384\n"
"v_mfma_f32_16x16x32_bf16 v[132:135], a[4:7], v[56:59], v[132:135]\n"
"s_waitcnt vmcnt(13)\n"
"v_add_u32_e64 v199, s65, 2\n"
"buffer_store_dwordx2 v[198:199], v197, s[44:47], s83 offen offset:0 sc1\n"
"v_mfma_f32_16x16x32_bf16 v[132:135], a[8:11], v[60:63], v[132:135]\n"
"s_nop 0\n"
"ds_read_b64_tr_b16 a[46:47], v179 offset:18432\n"
"ds_read_b64_tr_b16 a[48:49], v179 offset:20480\n"
"v_mfma_f32_16x16x32_bf16 v[132:135], a[12:15], v[64:67], v[132:135]\n"
"ds_read_b64_tr_b16 a[50:51], v179 offset:22528\n"
"v_mfma_f32_16x16x32_bf16 v[132:135], a[16:19], v[68:71], v[132:135]\n"
"v_mfma_f32_16x16x32_bf16 v[132:135], a[20:23], v[72:75], v[132:135]\n"
"v_mfma_f32_16x16x32_bf16 v[132:135], a[24:27], v[76:79], v[132:135]\n"
"s_waitcnt lgkmcnt(8)\n"
"ds_read_b64_tr_b16 a[52:53], v176 offset:24576\n"
"v_mfma_f32_16x16x32_bf16 v[136:139], a[28:31], v[0:3], v[136:139]\n"
"ds_read_b64_tr_b16 a[54:55], v176 offset:26624\n"
"v_mfma_f32_16x16x32_bf16 v[136:139], a[32:35], v[4:7], v[136:139]\n"
"s_waitcnt lgkmcnt(6)\n"
"ds_read_b64_tr_b16 a[56:57], v176 offset:28672\n"
"v_mfma_f32_16x16x32_bf16 v[136:139], a[36:39], v[8:11], v[136:139]\n"
"ds_read_b64_tr_b16 a[58:59], v176 offset:30720\n"
"v_mfma_f32_16x16x32_bf16 v[136:139], a[40:43], v[12:15], v[136:139]\n"
"s_waitcnt lgkmcnt(4)\n"
"ds_read_b64_tr_b16 a[0:1], v176 offset:32768\n"
"v_mfma_f32_16x16x32_bf16 v[136:139], a[44:47], v[16:19], v[136:139]\n"
"ds_read_b64_tr_b16 a[2:3], v176 offset:34816\n"
"v_mfma_f32_16x16x32_bf16 v[136:139], a[48:51], v[20:23], v[136:139]\n"
"ds_read_b64_tr_b16 a[4:5], v176 offset:36864\n"
"v_mfma_f32_16x16x32_bf16 v[140:143], a[28:31], v[56:59], v[140:143]\n"
"ds_read_b64_tr_b16 a[6:7], v176 offset:38912\n"
"v_mfma_f32_16x16x32_bf16 v[140:143], a[32:35], v[60:63], v[140:143]\n"
"ds_read_b64_tr_b16 a[8:9], v176 offset:40960\n"
"v_mfma_f32_16x16x32_bf16 v[140:143], a[36:39], v[64:67], v[140:143]\n"
"ds_read_b64_tr_b16 a[10:11], v176 offset:43008\n"
"v_mfma_f32_16x16x32_bf16 v[140:143], a[40:43], v[68:71], v[140:143]\n"
"v_mfma_f32_16x16x32_bf16 v[140:143], a[44:47], v[72:75], v[140:143]\n"
"ds_read_b64_tr_b16 a[12:13], v202 offset:256\n"
"v_mfma_f32_16x16x32_bf16 v[140:143], a[48:51], v[76:79], v[140:143]\n"
"s_waitcnt lgkmcnt(7)\n"
"ds_read_b64_tr_b16 a[16:17], v177 offset:24576\n"
"v_mfma_f32_16x16x32_bf16 v[112:115], a[52:55], v[32:35], v[112:115]\n"
"ds_read_b64_tr_b16 a[18:19], v177 offset:26624\n"
"v_mfma_f32_16x16x32_bf16 v[112:115], a[56:59], v[24:27], v[112:115]\n"
"s_waitcnt lgkmcnt(5)\n"
"ds_read_b64_tr_b16 a[20:21], v177 offset:28672\n"
"v_mfma_f32_16x16x32_bf16 v[112:115], a[0:3], v[28:31], v[112:115]\n"
"ds_read_b64_tr_b16 a[22:23], v177 offset:30720\n"
"v_mfma_f32_16x16x32_bf16 v[112:115], a[4:7], a[96:99], v[112:115]\n"
"s_waitcnt lgkmcnt(5)\n"
"ds_read_b64_tr_b16 a[24:25], v177 offset:32768\n"
"v_mfma_f32_16x16x32_bf16 v[112:115], a[8:11], v[36:39], v[112:115]\n"
"ds_read_b64_tr_b16 a[26:27], v177 offset:34816\n"
"v_mfma_f32_16x16x32_bf16 v[116:119], a[52:55], v[88:91], v[116:119]\n"
"ds_read_b64_tr_b16 a[28:29], v177 offset:36864\n"
"v_mfma_f32_16x16x32_bf16 v[116:119], a[56:59], v[80:83], v[116:119]\n"
"ds_read_b64_tr_b16 a[30:31], v177 offset:38912\n"
"v_mfma_f32_16x16x32_bf16 v[116:119], a[0:3], v[84:87], v[116:119]\n"
"ds_read_b64_tr_b16 a[32:33], v177 offset:40960\n"
"v_mfma_f32_16x16x32_bf16 v[116:119], a[4:7], a[138:141], v[116:119]\n"
"ds_read_b64_tr_b16 a[34:35], v177 offset:43008\n"
"v_mfma_f32_16x16x32_bf16 v[116:119], a[8:11], v[92:95], v[116:119]\n"
"s_waitcnt lgkmcnt(10)\n"
"ds_read_b64_tr_b16 a[36:37], v202 offset:288\n"
"v_mfma_f32_16x16x16_bf16 v[112:115], a[12:13], a[100:101], v[112:115]\n"
"ds_read_b64_tr_b16 a[40:41], v178 offset:24576\n"
"s_nop 1\n"
"v_mfma_f32_16x16x16_bf16 v[116:119], a[12:13], a[142:143], v[116:119]\n"
"s_waitcnt lgkmcnt(8)\n"
"ds_read_b64_tr_b16 a[42:43], v178 offset:26624\n"
"v_mfma_f32_16x16x32_bf16 v[120:123], a[16:19], v[32:35], v[120:123]\n"
"ds_read_b64_tr_b16 a[44:45], v178 offset:28672\n"
"v_mfma_f32_16x16x32_bf16 v[120:123], a[20:23], v[24:27], v[120:123]\n"
"s_waitcnt lgkmcnt(6)\n"
"ds_read_b64_tr_b16 a[46:47], v178 offset:30720\n"
"v_mfma_f32_16x16x32_bf16 v[120:123], a[24:27], v[28:31], v[120:123]\n"
"ds_read_b64_tr_b16 a[48:49], v178 offset:32768\n"
"v_mfma_f32_16x16x32_bf16 v[120:123], a[28:31], a[96:99], v[120:123]\n"
"s_waitcnt lgkmcnt(6)\n"
"ds_read_b64_tr_b16 a[50:51], v178 offset:34816\n"
"v_mfma_f32_16x16x32_bf16 v[120:123], a[32:35], v[36:39], v[120:123]\n"
"ds_read_b64_tr_b16 a[52:53], v178 offset:36864\n"
"v_mfma_f32_16x16x32_bf16 v[124:127], a[16:19], v[88:91], v[124:127]\n"
"ds_read_b64_tr_b16 a[54:55], v178 offset:38912\n"
"v_mfma_f32_16x16x32_bf16 v[124:127], a[20:23], v[80:83], v[124:127]\n"
"ds_read_b64_tr_b16 a[56:57], v178 offset:40960\n"
"v_mfma_f32_16x16x32_bf16 v[124:127], a[24:27], v[84:87], v[124:127]\n"
"ds_read_b64_tr_b16 a[58:59], v178 offset:43008\n"
"v_mfma_f32_16x16x32_bf16 v[124:127], a[28:31], a[138:141], v[124:127]\n"
"ds_read_b64_tr_b16 a[0:1], v202 offset:320\n"
"v_mfma_f32_16x16x32_bf16 v[124:127], a[32:35], v[92:95], v[124:127]\n"
"s_waitcnt lgkmcnt(11)\n"
"ds_read_b64_tr_b16 a[4:5], v179 offset:24576\n"
"v_mfma_f32_16x16x16_bf16 v[120:123], a[36:37], a[100:101], v[120:123]\n"
"v_cvt_pk_bf16_f32 v112, v112, v113\n"
"v_cvt_pk_bf16_f32 v113, v114, v115\n"
"s_nop 0\n"
"v_mfma_f32_16x16x16_bf16 v[124:127], a[36:37], a[142:143], v[124:127]\n"
"s_waitcnt lgkmcnt(8)\n"
"ds_read_b64_tr_b16 a[6:7], v179 offset:26624\n"
"ds_read_b64_tr_b16 a[8:9], v179 offset:28672\n"
"v_mfma_f32_16x16x32_bf16 v[128:131], a[40:43], v[32:35], v[128:131]\n"
"v_cvt_pk_bf16_f32 v114, v120, v121\n"
"v_cvt_pk_bf16_f32 v115, v122, v123\n"
"v_mfma_f32_16x16x32_bf16 v[128:131], a[44:47], v[24:27], v[128:131]\n"
"s_waitcnt lgkmcnt(6)\n"
"ds_read_b64_tr_b16 a[10:11], v179 offset:30720\n"
"ds_read_b64_tr_b16 a[12:13], v179 offset:32768\n"
"v_mfma_f32_16x16x32_bf16 v[128:131], a[48:51], v[28:31], v[128:131]\n"
"s_add_u32 s69, s77, 53248\n"
"ds_read_b64_tr_b16 a[14:15], v179 offset:34816\n"
"ds_read_b64_tr_b16 a[16:17], v179 offset:36864\n"
"v_mfma_f32_16x16x32_bf16 v[128:131], a[52:55], a[96:99], v[128:131]\n"
"s_waitcnt lgkmcnt(8)\n"
"ds_read_b64_tr_b16 a[18:19], v179 offset:38912\n"
"ds_read_b64_tr_b16 a[20:21], v179 offset:40960\n"
"v_mfma_f32_16x16x32_bf16 v[128:131], a[56:59], v[36:39], v[128:131]\n"
"s_nop 0\n"
"ds_read_b64_tr_b16 a[22:23], v179 offset:43008\n"
"ds_read_b64_tr_b16 a[24:25], v202 offset:352\n"
"v_mfma_f32_16x16x32_bf16 v[132:135], a[40:43], v[88:91], v[132:135]\n"
"s_nop 0\n"
"buffer_store_dwordx4 v[112:115], v182, s[44:47], s69 offen sc1\n"
"v_mfma_f32_16x16x32_bf16 v[132:135], a[44:47], v[80:83], v[132:135]\n"
"v_cvt_pk_bf16_f32 v116, v116, v117\n"
"v_cvt_pk_bf16_f32 v117, v118, v119\n"
"v_mfma_f32_16x16x32_bf16 v[132:135], a[48:51], v[84:87], v[132:135]\n"
"v_cvt_pk_bf16_f32 v118, v124, v125\n"
"v_cvt_pk_bf16_f32 v119, v126, v127\n"
"v_mfma_f32_16x16x32_bf16 v[132:135], a[52:55], a[138:141], v[132:135]\n"
"s_add_u32 s69, s77, 69632\n"
"buffer_store_dwordx4 v[116:119], v182, s[44:47], s69 offen sc1\n"
"v_mfma_f32_16x16x32_bf16 v[132:135], a[56:59], v[92:95], v[132:135]\n"
"s_waitcnt vmcnt(14) lgkmcnt(0)\n"
"v_mfma_f32_16x16x16_bf16 v[128:131], a[0:1], a[100:101], v[128:131]\n"
"s_barrier\n"
"ds_read_b64_tr_b16 a[28:29], v176 offset:45056\n"
"ds_read_b64_tr_b16 a[30:31], v176 offset:47104\n"
"s_nop 0\n"
"v_mfma_f32_16x16x16_bf16 v[132:135], a[0:1], a[142:143], v[132:135]\n"
"ds_read_b64_tr_b16 a[32:33], v176 offset:49152\n"
"v_mfma_f32_16x16x32_bf16 v[136:139], a[4:7], v[32:35], v[136:139]\n"
"ds_read_b64_tr_b16 a[34:35], v176 offset:51200\n"
"v_mfma_f32_16x16x32_bf16 v[136:139], a[8:11], v[24:27], v[136:139]\n"
"ds_read_b64_tr_b16 a[36:37], v176 offset:53248\n"
"v_mfma_f32_16x16x32_bf16 v[136:139], a[12:15], v[28:31], v[136:139]\n"
"ds_read_b64_tr_b16 a[38:39], v176 offset:55296\n"
"v_mfma_f32_16x16x32_bf16 v[136:139], a[16:19], a[96:99], v[136:139]\n"
"ds_read_b64_tr_b16 a[40:41], v176 offset:57344\n"
"v_mfma_f32_16x16x32_bf16 v[136:139], a[20:23], v[36:39], v[136:139]\n"
"s_add_u32 m0, s55, 0\n"
"s_add_u32 s56, s72, 0\n"
"ds_read_b64_tr_b16 a[42:43], v176 offset:59392\n"
"v_mfma_f32_16x16x32_bf16 v[140:143], a[4:7], v[88:91], v[140:143]\n"
"s_nop 0\n"
"buffer_load_dwordx4 v180, s[36:39], s56 offen lds\n"
"v_mfma_f32_16x16x32_bf16 v[140:143], a[8:11], v[80:83], v[140:143]\n"
"s_nop 0\n"
"ds_read_b64_tr_b16 a[44:45], v176 offset:61440\n"
"ds_read_b64_tr_b16 a[46:47], v176 offset:63488\n"
"v_mfma_f32_16x16x32_bf16 v[140:143], a[12:15], v[84:87], v[140:143]\n"
"ds_read_b64_tr_b16 a[48:49], v177 offset:45056\n"
"v_mfma_f32_16x16x32_bf16 v[140:143], a[16:19], a[138:141], v[140:143]\n"
"ds_read_b64_tr_b16 a[50:51], v177 offset:47104\n"
"v_mfma_f32_16x16x32_bf16 v[140:143], a[20:23], v[92:95], v[140:143]\n"
"s_addk_i32 m0, 4096\n"
"s_add_u32 s56, s72, 73728\n"
"ds_read_b64_tr_b16 a[52:53], v177 offset:49152\n"
"v_mfma_f32_16x16x16_bf16 v[136:139], a[24:25], a[100:101], v[136:139]\n"
"s_nop 0\n"
"buffer_load_dwordx4 v180, s[36:39], s56 offen lds\n"
"v_mfma_f32_16x16x16_bf16 v[140:143], a[24:25], a[142:143], v[140:143]\n"
"s_waitcnt lgkmcnt(9)\n"
"v_cvt_pk_bf16_f32 v128, v128, v129\n"
"v_cvt_pk_bf16_f32 v129, v130, v131\n"
"v_mfma_f32_16x16x32_bf16 v[144:147], a[28:31], a[60:63], 0\n"
"ds_read_b64_tr_b16 a[54:55], v177 offset:51200\n"
"v_cvt_pk_bf16_f32 v130, v136, v137\n"
"v_mfma_f32_16x16x32_bf16 v[144:147], a[32:35], a[64:67], v[144:147]\n"
"s_waitcnt lgkmcnt(6)\n"
"ds_read_b64_tr_b16 a[56:57], v177 offset:53248\n"
"v_cvt_pk_bf16_f32 v131, v138, v139\n"
"v_mfma_f32_16x16x32_bf16 v[144:147], a[36:39], a[68:71], v[144:147]\n"
"s_addk_i32 m0, 4096\n"
"ds_read_b64_tr_b16 a[58:59], v177 offset:55296\n"
"s_add_u32 s69, s77, 54272\n"
"v_mfma_f32_16x16x32_bf16 v[144:147], a[40:43], a[72:75], v[144:147]\n"
"s_waitcnt lgkmcnt(6)\n"
"ds_read_b64_tr_b16 a[0:1], v177 offset:57344\n"
"ds_read_b64_tr_b16 a[2:3], v177 offset:59392\n"
"v_mfma_f32_16x16x32_bf16 v[144:147], a[44:47], a[76:79], v[144:147]\n"
"s_add_u32 s56, s72, 147456\n"
"buffer_store_dwordx4 v[128:131], v182, s[44:47], s69 offen sc1\n"
"v_mfma_f32_16x16x32_bf16 v[148:151], a[28:31], a[102:105], 0\n"
"s_nop 0\n"
"ds_read_b64_tr_b16 a[4:5], v177 offset:61440\n"
"ds_read_b64_tr_b16 a[6:7], v177 offset:63488\n"
"v_mfma_f32_16x16x32_bf16 v[148:151], a[32:35], a[106:109], v[148:151]\n"
"s_nop 0\n"
"buffer_load_dwordx4 v180, s[36:39], s56 offen lds\n"
"v_mfma_f32_16x16x32_bf16 v[148:151], a[36:39], a[110:113], v[148:151]\n"
"s_addk_i32 m0, 4096\n"
"ds_read_b64_tr_b16 a[8:9], v178 offset:45056\n"
"v_cvt_pk_bf16_f32 v132, v132, v133\n"
"v_mfma_f32_16x16x32_bf16 v[148:151], a[40:43], a[114:117], v[148:151]\n"
"s_add_u32 s56, s72, 221184\n"
"ds_read_b64_tr_b16 a[10:11], v178 offset:47104\n"
"v_cvt_pk_bf16_f32 v133, v134, v135\n"
"v_mfma_f32_16x16x32_bf16 v[148:151], a[44:47], a[118:121], v[148:151]\n"
"s_waitcnt lgkmcnt(8)\n"
"ds_read_b64_tr_b16 a[12:13], v178 offset:49152\n"
"ds_read_b64_tr_b16 a[14:15], v178 offset:51200\n"
"v_mfma_f32_16x16x32_bf16 v[152:155], a[48:51], a[60:63], 0\n"
"s_nop 0\n"
"buffer_load_dwordx4 v180, s[36:39], s56 offen lds\n"
"v_mfma_f32_16x16x32_bf16 v[152:155], a[52:55], a[64:67], v[152:155]\n"
"s_waitcnt lgkmcnt(6)\n"
"ds_read_b64_tr_b16 a[16:17], v178 offset:53248\n"
"v_cvt_pk_bf16_f32 v134, v140, v141\n"
"v_mfma_f32_16x16x32_bf16 v[152:155], a[56:59], a[68:71], v[152:155]\n"
"s_addk_i32 m0, 4096\n"
"ds_read_b64_tr_b16 a[18:19], v178 offset:55296\n"
"v_cvt_pk_bf16_f32 v135, v142, v143\n"
"v_mfma_f32_16x16x32_bf16 v[152:155], a[0:3], a[72:75], v[152:155]\n"
"s_waitcnt lgkmcnt(6)\n"
"ds_read_b64_tr_b16 a[20:21], v178 offset:57344\n"
"v_add_u32 v176, 65536, v176\n"
"v_mfma_f32_16x16x32_bf16 v[152:155], a[4:7], a[76:79], v[152:155]\n"
"s_mov_b32 s56, s76\n"
"ds_read_b64_tr_b16 a[22:23], v178 offset:59392\n"
"s_add_u32 s69, s77, 70656\n"
"v_mfma_f32_16x16x32_bf16 v[156:159], a[48:51], a[102:105], 0\n"
"s_nop 0\n"
"ds_read_b64_tr_b16 a[24:25], v178 offset:61440\n"
"ds_read_b64_tr_b16 a[26:27], v178 offset:63488\n"
"v_mfma_f32_16x16x32_bf16 v[156:159], a[52:55], a[106:109], v[156:159]\n"
"s_nop 0\n"
"ds_read_b64_tr_b16 a[28:29], v179 offset:45056\n"
"ds_read_b64_tr_b16 a[30:31], v179 offset:47104\n"
"v_mfma_f32_16x16x32_bf16 v[156:159], a[56:59], a[110:113], v[156:159]\n"
"ds_read_b64_tr_b16 a[32:33], v179 offset:49152\n"
"v_mfma_f32_16x16x32_bf16 v[156:159], a[0:3], a[114:117], v[156:159]\n"
"ds_read_b64_tr_b16 a[34:35], v179 offset:51200\n"
"v_mfma_f32_16x16x32_bf16 v[156:159], a[4:7], a[118:121], v[156:159]\n"
"s_waitcnt lgkmcnt(10)\n"
"ds_read_b64_tr_b16 a[36:37], v179 offset:53248\n"
"v_add_u32 v177, 65536, v177\n"
"v_mfma_f32_16x16x32_bf16 v[160:163], a[8:11], a[60:63], 0\n"
"ds_read_b64_tr_b16 a[38:39], v179 offset:55296\n"
"v_mfma_f32_16x16x32_bf16 v[160:163], a[12:15], a[64:67], v[160:163]\n"
"s_waitcnt lgkmcnt(8)\n"
"ds_read_b64_tr_b16 a[40:41], v179 offset:57344\n"
"v_mfma_f32_16x16x32_bf16 v[160:163], a[16:19], a[68:71], v[160:163]\n"
"s_nop 0\n"
"buffer_store_dwordx4 v[132:135], v182, s[44:47], s69 offen sc1\n"
"v_mfma_f32_16x16x32_bf16 v[160:163], a[20:23], a[72:75], v[160:163]\n"
"s_waitcnt lgkmcnt(7)\n"
"ds_read_b64_tr_b16 a[42:43], v179 offset:59392\n"
"ds_read_b64_tr_b16 a[44:45], v179 offset:61440\n"
"v_mfma_f32_16x16x32_bf16 v[160:163], a[24:27], a[76:79], v[160:163]\n"
"ds_read_b64_tr_b16 a[46:47], v179 offset:63488\n"
"v_mfma_f32_16x16x32_bf16 v[164:167], a[8:11], a[102:105], 0\n"
"ds_read_b64_tr_b16 a[48:49], v176 offset:0\n"
"v_mfma_f32_16x16x32_bf16 v[164:167], a[12:15], a[106:109], v[164:167]\n"
"ds_read_b64_tr_b16 a[50:51], v176 offset:2048\n"
"v_mfma_f32_16x16x32_bf16 v[164:167], a[16:19], a[110:113], v[164:167]\n"
"ds_read_b64_tr_b16 a[52:53], v176 offset:4096\n"
"v_add_u32 v178, 65536, v178\n"
"v_mfma_f32_16x16x32_bf16 v[164:167], a[20:23], a[114:117], v[164:167]\n"
"ds_read_b64_tr_b16 a[54:55], v176 offset:6144\n"
"v_mfma_f32_16x16x32_bf16 v[164:167], a[24:27], a[118:121], v[164:167]\n"
"s_waitcnt lgkmcnt(10)\n"
"buffer_load_dwordx2 v[212:213], v191, s[44:47], s66 offen sc1\n"
"v_mfma_f32_16x16x32_bf16 v[168:171], a[28:31], a[60:63], 0\n"
"s_nop 0\n"
"ds_read_b64_tr_b16 a[56:57], v176 offset:8192\n"
"ds_read_b64_tr_b16 a[58:59], v176 offset:10240\n"
"v_mfma_f32_16x16x32_bf16 v[168:171], a[32:35], a[64:67], v[168:171]\n"
"s_waitcnt lgkmcnt(8)\n"
"ds_read_b64_tr_b16 a[0:1], v176 offset:12288\n"
"v_mfma_f32_16x16x32_bf16 v[168:171], a[36:39], a[68:71], v[168:171]\n"
"ds_read_b64_tr_b16 a[2:3], v176 offset:14336\n"
"v_mfma_f32_16x16x32_bf16 v[168:171], a[40:43], a[72:75], v[168:171]\n"
"s_waitcnt lgkmcnt(8)\n"
"ds_read_b64_tr_b16 a[4:5], v177 offset:0\n"
"v_mfma_f32_16x16x32_bf16 v[168:171], a[44:47], a[76:79], v[168:171]\n"
"ds_read_b64_tr_b16 a[6:7], v177 offset:2048\n"
"v_mfma_f32_16x16x32_bf16 v[172:175], a[28:31], a[102:105], 0\n"
"ds_read_b64_tr_b16 a[8:9], v177 offset:4096\n"
"v_add_u32 v179, 65536, v179\n"
"v_mfma_f32_16x16x32_bf16 v[172:175], a[32:35], a[106:109], v[172:175]\n"
"ds_read_b64_tr_b16 a[10:11], v177 offset:6144\n"
"v_mfma_f32_16x16x32_bf16 v[172:175], a[36:39], a[110:113], v[172:175]\n"
"ds_read_b64_tr_b16 a[12:13], v177 offset:8192\n"
"v_mfma_f32_16x16x32_bf16 v[172:175], a[40:43], a[114:117], v[172:175]\n"
"s_nop 0\n"
"buffer_load_dwordx2 v[216:217], v192, s[8:11], s67 offen offset:0\n"
"v_mfma_f32_16x16x32_bf16 v[172:175], a[44:47], a[118:121], v[172:175]\n"
"s_waitcnt lgkmcnt(9)\n"
"ds_read_b64_tr_b16 a[14:15], v177 offset:10240\n"
"ds_read_b64_tr_b16 a[16:17], v177 offset:12288\n"
"v_mfma_f32_16x16x32_bf16 v[144:147], a[48:51], a[80:83], v[144:147]\n"
"ds_read_b64_tr_b16 a[18:19], v177 offset:14336\n"
"v_mfma_f32_16x16x32_bf16 v[144:147], a[52:55], a[92:95], v[144:147]\n"
"s_waitcnt lgkmcnt(8)\n"
"ds_read_b64_tr_b16 a[20:21], v178 offset:0\n"
"v_mfma_f32_16x16x32_bf16 v[144:147], a[56:59], a[84:87], v[144:147]\n"
"s_nop 0\n"
"buffer_load_dwordx2 v[218:219], v192, s[8:11], s67 offen offset:128\n"
"v_mfma_f32_16x16x32_bf16 v[144:147], a[0:3], a[88:91], v[144:147]\n"
"s_nop 0\n"
"buffer_load_dwordx2 v[220:221], v192, s[8:11], s67 offen offset:256\n"
"v_mfma_f32_16x16x32_bf16 v[148:151], a[48:51], a[122:125], v[148:151]\n"
"s_nop 0\n"
"ds_read_b64_tr_b16 a[22:23], v178 offset:2048\n"
"ds_read_b64_tr_b16 a[24:25], v178 offset:4096\n"
"v_mfma_f32_16x16x32_bf16 v[148:151], a[52:55], a[134:137], v[148:151]\n"
"s_nop 0\n"
"buffer_load_dwordx2 v[222:223], v192, s[8:11], s67 offen offset:384\n"
"v_mfma_f32_16x16x32_bf16 v[148:151], a[56:59], a[126:129], v[148:151]\n"
"s_nop 0\n"
"ds_read_b64_tr_b16 a[26:27], v178 offset:6144\n"
"ds_read_b64_tr_b16 a[28:29], v178 offset:8192\n"
"v_mfma_f32_16x16x32_bf16 v[148:151], a[0:3], a[130:133], v[148:151]\n"
"s_waitcnt lgkmcnt(9)\n"
"buffer_load_dwordx2 v[224:225], v194, s[8:11], s67 offen offset:512\n"
"v_mfma_f32_16x16x32_bf16 v[152:155], a[4:7], a[80:83], v[152:155]\n"
"s_nop 0\n"
"ds_read_b64_tr_b16 a[30:31], v178 offset:10240\n"
"ds_read_b64_tr_b16 a[32:33], v178 offset:12288\n"
"v_mfma_f32_16x16x32_bf16 v[152:155], a[8:11], a[92:95], v[152:155]\n"
"s_waitcnt lgkmcnt(7)\n"
"ds_read_b64_tr_b16 a[34:35], v178 offset:14336\n"
"ds_read_b64_tr_b16 a[36:37], v179 offset:0\n"
"v_mfma_f32_16x16x32_bf16 v[152:155], a[12:15], a[84:87], v[152:155]\n"
"s_nop 0\n"
"ds_read_b64_tr_b16 a[38:39], v179 offset:2048\n"
"ds_read_b64_tr_b16 a[40:41], v179 offset:4096\n"
"v_mfma_f32_16x16x32_bf16 v[152:155], a[16:19], a[88:91], v[152:155]\n"
"s_nop 0\n"
"ds_read_b64_tr_b16 a[42:43], v179 offset:6144\n"
"ds_read_b64_tr_b16 a[44:45], v179 offset:8192\n"
"v_mfma_f32_16x16x32_bf16 v[156:159], a[4:7], a[122:125], v[156:159]\n"
"114:\n"
"s_cbranch_execz 98b\n"
"s_nop 0\n"
"ds_read_b64_tr_b16 a[46:47], v179 offset:10240\n"
"ds_read_b64_tr_b16 a[48:49], v179 offset:12288\n"
"v_mfma_f32_16x16x32_bf16 v[156:159], a[8:11], a[134:137], v[156:159]\n"
"ds_read_b64_tr_b16 a[50:51], v179 offset:14336\n"
"v_mfma_f32_16x16x32_bf16 v[156:159], a[12:15], a[126:129], v[156:159]\n"
"v_mfma_f32_16x16x32_bf16 v[156:159], a[16:19], a[130:133], v[156:159]\n"
"s_waitcnt lgkmcnt(12)\n"
"buffer_load_dwordx4 v180, s[36:39], s56 offen lds\n"
"v_mfma_f32_16x16x32_bf16 v[160:163], a[20:23], a[80:83], v[160:163]\n"
"s_addk_i32 m0, 4096\n"
"s_mov_b32 s56, 0\n"
"v_mfma_f32_16x16x32_bf16 v[160:163], a[24:27], a[92:95], v[160:163]\n"
"s_waitcnt lgkmcnt(8)\n"
"buffer_load_dwordx4 v181, s[20:23], s56 offen lds\n"
"v_mfma_f32_16x16x32_bf16 v[160:163], a[28:31], a[84:87], v[160:163]\n"
"s_waitcnt vmcnt(17) lgkmcnt(0)\n"
"v_mfma_f32_16x16x32_bf16 v[160:163], a[32:35], a[88:91], v[160:163]\n"
"s_barrier\n"
"ds_read_b64_tr_b16 a[52:53], v176 offset:16384\n"
"ds_read_b64_tr_b16 a[54:55], v176 offset:18432\n"
"v_mfma_f32_16x16x32_bf16 v[164:167], a[20:23], a[122:125], v[164:167]\n"
"s_nop 0\n"
"ds_read_b64_tr_b16 a[56:57], v176 offset:20480\n"
"ds_read_b64_tr_b16 a[58:59], v176 offset:22528\n"
"v_mfma_f32_16x16x32_bf16 v[164:167], a[24:27], a[134:137], v[164:167]\n"
"ds_read_b64_tr_b16 a[0:1], v176 offset:24576\n"
"v_mfma_f32_16x16x32_bf16 v[164:167], a[28:31], a[126:129], v[164:167]\n"
"ds_read_b64_tr_b16 a[2:3], v176 offset:26624\n"
"v_mfma_f32_16x16x32_bf16 v[164:167], a[32:35], a[130:133], v[164:167]\n"
"ds_read_b64_tr_b16 a[4:5], v176 offset:28672\n"
"v_mfma_f32_16x16x32_bf16 v[168:171], a[36:39], a[80:83], v[168:171]\n"
"ds_read_b64_tr_b16 a[6:7], v176 offset:30720\n"
"v_mfma_f32_16x16x32_bf16 v[168:171], a[40:43], a[92:95], v[168:171]\n"
"ds_read_b64_tr_b16 a[8:9], v176 offset:32768\n"
"v_mfma_f32_16x16x32_bf16 v[168:171], a[44:47], a[84:87], v[168:171]\n"
"ds_read_b64_tr_b16 a[10:11], v176 offset:34816\n"
"v_mfma_f32_16x16x32_bf16 v[168:171], a[48:51], a[88:91], v[168:171]\n"
"ds_read_b64_tr_b16 a[12:13], v176 offset:36864\n"
"v_mfma_f32_16x16x32_bf16 v[172:175], a[36:39], a[122:125], v[172:175]\n"
"ds_read_b64_tr_b16 a[14:15], v176 offset:38912\n"
"v_mfma_f32_16x16x32_bf16 v[172:175], a[40:43], a[134:137], v[172:175]\n"
"ds_read_b64_tr_b16 a[16:17], v177 offset:16384\n"
"v_mfma_f32_16x16x32_bf16 v[172:175], a[44:47], a[126:129], v[172:175]\n"
"ds_read_b64_tr_b16 a[18:19], v177 offset:18432\n"
"v_mfma_f32_16x16x32_bf16 v[172:175], a[48:51], a[130:133], v[172:175]\n"
"s_waitcnt lgkmcnt(10)\n"
"ds_read_b64_tr_b16 a[20:21], v177 offset:20480\n"
"v_mfma_f32_16x16x32_bf16 v[144:147], a[52:55], v[0:3], v[144:147]\n"
"ds_read_b64_tr_b16 a[22:23], v177 offset:22528\n"
"v_mfma_f32_16x16x32_bf16 v[144:147], a[56:59], v[4:7], v[144:147]\n"
"s_waitcnt lgkmcnt(8)\n"
"ds_read_b64_tr_b16 a[24:25], v177 offset:24576\n"
"v_mfma_f32_16x16x32_bf16 v[144:147], a[0:3], v[8:11], v[144:147]\n"
"ds_read_b64_tr_b16 a[26:27], v177 offset:26624\n"
"v_mfma_f32_16x16x32_bf16 v[144:147], a[4:7], v[12:15], v[144:147]\n"
"s_waitcnt lgkmcnt(6)\n"
"ds_read_b64_tr_b16 a[28:29], v177 offset:28672\n"
"v_mfma_f32_16x16x32_bf16 v[144:147], a[8:11], v[16:19], v[144:147]\n"
"ds_read_b64_tr_b16 a[30:31], v177 offset:30720\n"
"v_mfma_f32_16x16x32_bf16 v[144:147], a[12:15], v[20:23], v[144:147]\n"
"ds_read_b64_tr_b16 a[32:33], v177 offset:32768\n"
"v_mfma_f32_16x16x32_bf16 v[148:151], a[52:55], v[56:59], v[148:151]\n"
"ds_read_b64_tr_b16 a[34:35], v177 offset:34816\n"
"v_mfma_f32_16x16x32_bf16 v[148:151], a[56:59], v[60:63], v[148:151]\n"
"ds_read_b64_tr_b16 a[36:37], v177 offset:36864\n"
"v_mfma_f32_16x16x32_bf16 v[148:151], a[0:3], v[64:67], v[148:151]\n"
"ds_read_b64_tr_b16 a[38:39], v177 offset:38912\n"
"v_mfma_f32_16x16x32_bf16 v[148:151], a[4:7], v[68:71], v[148:151]\n"
"ds_read_b64_tr_b16 a[40:41], v178 offset:16384\n"
"v_mfma_f32_16x16x32_bf16 v[148:151], a[8:11], v[72:75], v[148:151]\n"
"ds_read_b64_tr_b16 a[42:43], v178 offset:18432\n"
"v_mfma_f32_16x16x32_bf16 v[148:151], a[12:15], v[76:79], v[148:151]\n"
"s_waitcnt lgkmcnt(10)\n"
"ds_read_b64_tr_b16 a[44:45], v178 offset:20480\n"
"v_mfma_f32_16x16x32_bf16 v[152:155], a[16:19], v[0:3], v[152:155]\n"
"ds_read_b64_tr_b16 a[46:47], v178 offset:22528\n"
"v_mfma_f32_16x16x32_bf16 v[152:155], a[20:23], v[4:7], v[152:155]\n"
"s_waitcnt lgkmcnt(8)\n"
"ds_read_b64_tr_b16 a[48:49], v178 offset:24576\n"
"v_mfma_f32_16x16x32_bf16 v[152:155], a[24:27], v[8:11], v[152:155]\n"
"ds_read_b64_tr_b16 a[50:51], v178 offset:26624\n"
"v_mfma_f32_16x16x32_bf16 v[152:155], a[28:31], v[12:15], v[152:155]\n"
"s_waitcnt lgkmcnt(6)\n"
"ds_read_b64_tr_b16 a[52:53], v178 offset:28672\n"
"v_mfma_f32_16x16x32_bf16 v[152:155], a[32:35], v[16:19], v[152:155]\n"
"ds_read_b64_tr_b16 a[54:55], v178 offset:30720\n"
"v_mfma_f32_16x16x32_bf16 v[152:155], a[36:39], v[20:23], v[152:155]\n"
"ds_read_b64_tr_b16 a[56:57], v178 offset:32768\n"
"v_mfma_f32_16x16x32_bf16 v[156:159], a[16:19], v[56:59], v[156:159]\n"
"ds_read_b64_tr_b16 a[58:59], v178 offset:34816\n"
"v_mfma_f32_16x16x32_bf16 v[156:159], a[20:23], v[60:63], v[156:159]\n"
"ds_read_b64_tr_b16 a[0:1], v178 offset:36864\n"
"v_mfma_f32_16x16x32_bf16 v[156:159], a[24:27], v[64:67], v[156:159]\n"
"ds_read_b64_tr_b16 a[2:3], v178 offset:38912\n"
"v_mfma_f32_16x16x32_bf16 v[156:159], a[28:31], v[68:71], v[156:159]\n"
"ds_read_b64_tr_b16 a[4:5], v179 offset:16384\n"
"v_mfma_f32_16x16x32_bf16 v[156:159], a[32:35], v[72:75], v[156:159]\n"
"ds_read_b64_tr_b16 a[6:7], v179 offset:18432\n"
"v_mfma_f32_16x16x32_bf16 v[156:159], a[36:39], v[76:79], v[156:159]\n"
"s_waitcnt lgkmcnt(10)\n"
"ds_read_b64_tr_b16 a[8:9], v179 offset:20480\n"
"v_mfma_f32_16x16x32_bf16 v[160:163], a[40:43], v[0:3], v[160:163]\n"
"ds_read_b64_tr_b16 a[10:11], v179 offset:22528\n"
"v_mfma_f32_16x16x32_bf16 v[160:163], a[44:47], v[4:7], v[160:163]\n"
"s_waitcnt lgkmcnt(8)\n"
"ds_read_b64_tr_b16 a[12:13], v179 offset:24576\n"
"v_mfma_f32_16x16x32_bf16 v[160:163], a[48:51], v[8:11], v[160:163]\n"
"ds_read_b64_tr_b16 a[14:15], v179 offset:26624\n"
"v_mfma_f32_16x16x32_bf16 v[160:163], a[52:55], v[12:15], v[160:163]\n"
"s_waitcnt lgkmcnt(6)\n"
"ds_read_b64_tr_b16 a[16:17], v179 offset:28672\n"
"v_mfma_f32_16x16x32_bf16 v[160:163], a[56:59], v[16:19], v[160:163]\n"
"ds_read_b64_tr_b16 a[18:19], v179 offset:30720\n"
"v_mfma_f32_16x16x32_bf16 v[160:163], a[0:3], v[20:23], v[160:163]\n"
"ds_read_b64_tr_b16 a[20:21], v179 offset:32768\n"
"v_mfma_f32_16x16x32_bf16 v[164:167], a[40:43], v[56:59], v[164:167]\n"
"ds_read_b64_tr_b16 a[22:23], v179 offset:34816\n"
"v_mfma_f32_16x16x32_bf16 v[164:167], a[44:47], v[60:63], v[164:167]\n"
"ds_read_b64_tr_b16 a[24:25], v179 offset:36864\n"
"v_mfma_f32_16x16x32_bf16 v[164:167], a[48:51], v[64:67], v[164:167]\n"
"ds_read_b64_tr_b16 a[26:27], v179 offset:38912\n"
"v_mfma_f32_16x16x32_bf16 v[164:167], a[52:55], v[68:71], v[164:167]\n"
"s_waitcnt vmcnt(8)\n"
"v_add_u32_e64 v199, s65, 3\n"
"buffer_store_dwordx2 v[198:199], v197, s[44:47], s83 offen offset:0 sc1\n"
"v_mfma_f32_16x16x32_bf16 v[164:167], a[56:59], v[72:75], v[164:167]\n"
"s_waitcnt vmcnt(8)\n"
"v_subrev_u32 v213, s65, v213\n"
"v_xor_b32 v212, s64, v212\n"
"v_mfma_f32_16x16x32_bf16 v[164:167], a[0:3], v[76:79], v[164:167]\n"
"s_waitcnt lgkmcnt(8)\n"
"v_and_b32 v214, -8, v213\n"
"v_or_b32 v212, v212, v214\n"
"v_mfma_f32_16x16x32_bf16 v[168:171], a[4:7], v[0:3], v[168:171]\n"
"v_min_u32 v212, 1, v212\n"
"v_add_u32 v212, -1, v212\n"
"v_mfma_f32_16x16x32_bf16 v[168:171], a[8:11], v[4:7], v[168:171]\n"
"s_waitcnt lgkmcnt(4)\n"
"ds_read_b64_tr_b16 a[28:29], v176 offset:40960\n"
"v_and_b32 v214, v212, v213\n"
"v_mfma_f32_16x16x32_bf16 v[168:171], a[12:15], v[8:11], v[168:171]\n"
"s_nop 0\n"
"ds_read_b64_tr_b16 a[30:31], v176 offset:43008\n"
"ds_read_b64_tr_b16 a[32:33], v176 offset:45056\n"
"v_mfma_f32_16x16x32_bf16 v[168:171], a[16:19], v[12:15], v[168:171]\n"
"s_waitcnt lgkmcnt(3)\n"
"v_cmp_ge_u32 vcc, 0, v214\n"
"s_nop 1\n"
"s_cmp_eq_u64 vcc, 0\n"
"s_cselect_b32 s60, 0, 1879048192\n"
"s_cselect_b32 s61, 0, 1\n"
"v_mfma_f32_16x16x32_bf16 v[168:171], a[20:23], v[16:19], v[168:171]\n"
"s_or_b32 s68, s68, s61\n"
"ds_read_b64_tr_b16 a[34:35], v176 offset:47104\n"
"s_addk_i32 s60, 0\n"
"v_mfma_f32_16x16x32_bf16 v[168:171], a[24:27], v[20:23], v[168:171]\n"
"ds_read_b64_tr_b16 a[36:37], v176 offset:49152\n"
"v_add_u32 v190, s60, v189\n"
"v_mfma_f32_16x16x32_bf16 v[172:175], a[4:7], v[56:59], v[172:175]\n"
"ds_read_b64_tr_b16 a[38:39], v176 offset:51200\n"
"v_add_u32 v227, s60, v226\n"
"v_mfma_f32_16x16x32_bf16 v[172:175], a[8:11], v[60:63], v[172:175]\n"
"s_nop 0\n"
"ds_read_b64_tr_b16 a[40:41], v176 offset:53248\n"
"ds_read_b64_tr_b16 a[42:43], v176 offset:55296\n"
"v_mfma_f32_16x16x32_bf16 v[172:175], a[12:15], v[64:67], v[172:175]\n"
"s_nop 0\n"
"buffer_load_dwordx4 v[40:43], v190, s[44:47], s58 offen offset:0 sc1\n"
"v_mfma_f32_16x16x32_bf16 v[172:175], a[16:19], v[68:71], v[172:175]\n"
"s_nop 0\n"
"ds_read_b64_tr_b16 a[44:45], v176 offset:57344\n"
"ds_read_b64_tr_b16 a[46:47], v176 offset:59392\n"
"v_mfma_f32_16x16x32_bf16 v[172:175], a[20:23], v[72:75], v[172:175]\n"
"s_nop 0\n"
"buffer_load_dwordx4 v[44:47], v227, s[44:47], s59 offen offset:0 sc1\n"
"v_mfma_f32_16x16x32_bf16 v[172:175], a[24:27], v[76:79], v[172:175]\n"
"s_waitcnt lgkmcnt(6)\n"
"buffer_load_dwordx4 v[48:51], v190, s[44:47], s58 offen offset:1024 sc1\n"
"v_mfma_f32_16x16x32_bf16 v[144:147], a[28:31], v[32:35], v[144:147]\n"
"s_nop 0\n"
"ds_read_b64_tr_b16 a[48:49], v202 offset:384\n"
"ds_read_b64_tr_b16 a[52:53], v177 offset:40960\n"
"v_mfma_f32_16x16x32_bf16 v[144:147], a[32:35], v[24:27], v[144:147]\n"
"s_waitcnt lgkmcnt(4)\n"
"buffer_load_dwordx4 v[52:55], v227, s[44:47], s59 offen offset:1024 sc1\n"
"v_mfma_f32_16x16x32_bf16 v[144:147], a[36:39], v[28:31], v[144:147]\n"
"s_nop 0\n"
"ds_read_b64_tr_b16 a[54:55], v177 offset:43008\n"
"ds_read_b64_tr_b16 a[56:57], v177 offset:45056\n"
"v_mfma_f32_16x16x32_bf16 v[144:147], a[40:43], a[96:99], v[144:147]\n"
"s_waitcnt lgkmcnt(4)\n"
"v_cmp_ge_u32 vcc, 1, v214\n"
"s_nop 1\n"
"s_cmp_eq_u64 vcc, 0\n"
"s_cselect_b32 s60, 0, 1879048192\n"
"s_cselect_b32 s61, 0, 2\n"
"v_mfma_f32_16x16x32_bf16 v[144:147], a[44:47], v[36:39], v[144:147]\n"
"s_or_b32 s68, s68, s61\n"
"ds_read_b64_tr_b16 a[58:59], v177 offset:47104\n"
"s_addk_i32 s60, 2048\n"
"v_mfma_f32_16x16x32_bf16 v[148:151], a[28:31], v[88:91], v[148:151]\n"
"ds_read_b64_tr_b16 a[0:1], v177 offset:49152\n"
"v_add_u32 v190, s60, v189\n"
"v_mfma_f32_16x16x32_bf16 v[148:151], a[32:35], v[80:83], v[148:151]\n"
"ds_read_b64_tr_b16 a[2:3], v177 offset:51200\n"
"v_add_u32 v227, s60, v226\n"
"v_mfma_f32_16x16x32_bf16 v[148:151], a[36:39], v[84:87], v[148:151]\n"
"s_nop 0\n"
"ds_read_b64_tr_b16 a[4:5], v177 offset:53248\n"
"ds_read_b64_tr_b16 a[6:7], v177 offset:55296\n"
"v_mfma_f32_16x16x32_bf16 v[148:151], a[40:43], a[138:141], v[148:151]\n"
"s_nop 0\n"
"buffer_load_dwordx4 v[96:99], v190, s[44:47], s58 offen offset:0 sc1\n"
"v_mfma_f32_16x16x32_bf16 v[148:151], a[44:47], v[92:95], v[148:151]\n"
"s_waitcnt lgkmcnt(8)\n"
"ds_read_b64_tr_b16 a[8:9], v177 offset:57344\n"
"ds_read_b64_tr_b16 a[10:11], v177 offset:59392\n"
"v_mfma_f32_16x16x16_bf16 v[144:147], a[48:49], a[100:101], v[144:147]\n"
"s_nop 0\n"
"buffer_load_dwordx4 v[100:103], v227, s[44:47], s59 offen offset:0 sc1\n"
"v_mfma_f32_16x16x16_bf16 v[148:151], a[48:49], a[142:143], v[148:151]\n"
"s_waitcnt lgkmcnt(6)\n"
"ds_read_b64_tr_b16 a[12:13], v202 offset:416\n"
"ds_read_b64_tr_b16 a[16:17], v178 offset:40960\n"
"v_mfma_f32_16x16x32_bf16 v[152:155], a[52:55], v[32:35], v[152:155]\n"
"ds_read_b64_tr_b16 a[18:19], v178 offset:43008\n"
"v_mfma_f32_16x16x32_bf16 v[152:155], a[56:59], v[24:27], v[152:155]\n"
"s_waitcnt lgkmcnt(5)\n"
"buffer_load_dwordx4 v[104:107], v190, s[44:47], s58 offen offset:1024 sc1\n"
"v_mfma_f32_16x16x32_bf16 v[152:155], a[0:3], v[28:31], v[152:155]\n"
"s_nop 0\n"
"ds_read_b64_tr_b16 a[20:21], v178 offset:45056\n"
"ds_read_b64_tr_b16 a[22:23], v178 offset:47104\n"
"v_mfma_f32_16x16x32_bf16 v[152:155], a[4:7], a[96:99], v[152:155]\n"
"s_waitcnt lgkmcnt(5)\n"
"ds_read_b64_tr_b16 a[24:25], v178 offset:49152\n"
"v_mfma_f32_16x16x32_bf16 v[152:155], a[8:11], v[36:39], v[152:155]\n"
"ds_read_b64_tr_b16 a[26:27], v178 offset:51200\n"
"v_mfma_f32_16x16x32_bf16 v[156:159], a[52:55], v[88:91], v[156:159]\n"
"ds_read_b64_tr_b16 a[28:29], v178 offset:53248\n"
"v_mfma_f32_16x16x32_bf16 v[156:159], a[56:59], v[80:83], v[156:159]\n"
"ds_read_b64_tr_b16 a[30:31], v178 offset:55296\n"
"v_mfma_f32_16x16x32_bf16 v[156:159], a[0:3], v[84:87], v[156:159]\n"
"ds_read_b64_tr_b16 a[32:33], v178 offset:57344\n"
"v_add_u32 v200, 4294942720, v200\n"
"v_mfma_f32_16x16x32_bf16 v[156:159], a[4:7], a[138:141], v[156:159]\n"
"ds_read_b64_tr_b16 a[34:35], v178 offset:59392\n"
"v_mfma_f32_16x16x32_bf16 v[156:159], a[8:11], v[92:95], v[156:159]\n"
"s_waitcnt lgkmcnt(10)\n"
"v_mfma_f32_16x16x16_bf16 v[152:155], a[12:13], a[100:101], v[152:155]\n"
"v_cvt_pk_bf16_f32 v144, v144, v145\n"
"v_cvt_pk_bf16_f32 v145, v146, v147\n"
"s_nop 1\n"
"v_mfma_f32_16x16x16_bf16 v[156:159], a[12:13], a[142:143], v[156:159]\n"
"s_waitcnt lgkmcnt(6)\n"
"ds_read_b64_tr_b16 a[36:37], v202 offset:448\n"
"ds_read_b64_tr_b16 a[40:41], v179 offset:40960\n"
"v_mfma_f32_16x16x32_bf16 v[160:163], a[16:19], v[32:35], v[160:163]\n"
"v_cvt_pk_bf16_f32 v146, v152, v153\n"
"v_cvt_pk_bf16_f32 v147, v154, v155\n"
"v_mfma_f32_16x16x32_bf16 v[160:163], a[20:23], v[24:27], v[160:163]\n"
"s_waitcnt lgkmcnt(4)\n"
"s_add_u32 s69, s77, 55296\n"
"ds_read_b64_tr_b16 a[42:43], v179 offset:43008\n"
"v_mfma_f32_16x16x32_bf16 v[160:163], a[24:27], v[28:31], v[160:163]\n"
"s_nop 0\n"
"ds_read_b64_tr_b16 a[44:45], v179 offset:45056\n"
"ds_read_b64_tr_b16 a[46:47], v179 offset:47104\n"
"v_mfma_f32_16x16x32_bf16 v[160:163], a[28:31], a[96:99], v[160:163]\n"
"s_waitcnt lgkmcnt(5)\n"
"ds_read_b64_tr_b16 a[48:49], v179 offset:49152\n"
"ds_read_b64_tr_b16 a[50:51], v179 offset:51200\n"
"v_mfma_f32_16x16x32_bf16 v[160:163], a[32:35], v[36:39], v[160:163]\n"
"ds_read_b64_tr_b16 a[52:53], v179 offset:53248\n"
"v_add_u32 v201, 4294942720, v201\n"
"v_mfma_f32_16x16x32_bf16 v[164:167], a[16:19], v[88:91], v[164:167]\n"
"s_nop 0\n"
"ds_read_b64_tr_b16 a[54:55], v179 offset:55296\n"
"ds_read_b64_tr_b16 a[56:57], v179 offset:57344\n"
"v_mfma_f32_16x16x32_bf16 v[164:167], a[20:23], v[80:83], v[164:167]\n"
"s_nop 0\n"
"ds_read_b64_tr_b16 a[58:59], v179 offset:59392\n"
"ds_read_b64_tr_b16 a[0:1], v202 offset:480\n"
"v_mfma_f32_16x16x32_bf16 v[164:167], a[24:27], v[84:87], v[164:167]\n"
"v_mfma_f32_16x16x32_bf16 v[164:167], a[28:31], a[138:141], v[164:167]\n"
"s_nop 0\n"
"buffer_store_dwordx4 v[144:147], v182, s[44:47], s69 offen sc1\n"
"v_mfma_f32_16x16x32_bf16 v[164:167], a[32:35], v[92:95], v[164:167]\n"
"s_waitcnt lgkmcnt(11)\n"
"v_cvt_pk_bf16_f32 v148, v148, v149\n"
"v_cvt_pk_bf16_f32 v149, v150, v151\n"
"v_mfma_f32_16x16x16_bf16 v[160:163], a[36:37], a[100:101], v[160:163]\n"
"v_cvt_pk_bf16_f32 v150, v156, v157\n"
"v_cvt_pk_bf16_f32 v151, v158, v159\n"
"v_mfma_f32_16x16x16_bf16 v[164:167], a[36:37], a[142:143], v[164:167]\n"
"s_waitcnt vmcnt(9) lgkmcnt(0)\n"
"buffer_load_dwordx4 v[108:111], v227, s[44:47], s59 offen offset:1024 sc1\n"
"v_mfma_f32_16x16x32_bf16 v[168:171], a[40:43], v[32:35], v[168:171]\n"
"s_barrier\n"
"ds_read_b64_tr_b16 a[4:5], v200 offset:0\n"
"s_add_u32 s69, s77, 71680\n"
"v_mfma_f32_16x16x32_bf16 v[168:171], a[44:47], v[24:27], v[168:171]\n"
"v_mfma_f32_16x16x32_bf16 v[168:171], a[48:51], v[28:31], v[168:171]\n"
"s_nop 0\n"
"ds_read_b64_tr_b16 a[6:7], v200 offset:1024\n"
"ds_read_b64_tr_b16 a[8:9], v200 offset:2048\n"
"v_mfma_f32_16x16x32_bf16 v[168:171], a[52:55], a[96:99], v[168:171]\n"
"s_nop 0\n"
"buffer_store_dwordx4 v[148:151], v182, s[44:47], s69 offen sc1\n"
"v_mfma_f32_16x16x32_bf16 v[168:171], a[56:59], v[36:39], v[168:171]\n"
"ds_read_b64_tr_b16 a[10:11], v200 offset:3072\n"
"v_mfma_f32_16x16x32_bf16 v[172:175], a[40:43], v[88:91], v[172:175]\n"
"ds_read_b64_tr_b16 a[12:13], v200 offset:4096\n"
"v_mfma_f32_16x16x32_bf16 v[172:175], a[44:47], v[80:83], v[172:175]\n"
"ds_read_b64_tr_b16 a[14:15], v200 offset:5120\n"
"v_mfma_f32_16x16x32_bf16 v[172:175], a[48:51], v[84:87], v[172:175]\n"
"ds_read_b64_tr_b16 a[16:17], v200 offset:6144\n"
"v_mfma_f32_16x16x32_bf16 v[172:175], a[52:55], a[138:141], v[172:175]\n"
"ds_read_b64_tr_b16 a[18:19], v200 offset:7168\n"
"v_mfma_f32_16x16x32_bf16 v[172:175], a[56:59], v[92:95], v[172:175]\n"
"ds_read_b64_tr_b16 a[20:21], v201 offset:0\n"
"v_mfma_f32_16x16x16_bf16 v[168:171], a[0:1], a[100:101], v[168:171]\n"
"ds_read_b64_tr_b16 a[22:23], v201 offset:1024\n"
"v_cvt_pk_bf16_f32 v160, v160, v161\n"
"s_nop 1\n"
"v_mfma_f32_16x16x16_bf16 v[172:175], a[0:1], a[142:143], v[172:175]\n"
"s_waitcnt lgkmcnt(6)\n"
"ds_read_b64_tr_b16 a[24:25], v201 offset:2048\n"
"v_cvt_pk_bf16_f32 v161, v162, v163\n"
"v_mfma_f32_16x16x32_bf16 v[240:243], a[4:7], v[0:3], v[240:243]\n"
"ds_read_b64_tr_b16 a[26:27], v201 offset:3072\n"
"v_cvt_pk_bf16_f32 v162, v168, v169\n"
"v_mfma_f32_16x16x32_bf16 v[240:243], a[8:11], v[4:7], v[240:243]\n"
"s_waitcnt lgkmcnt(4)\n"
"ds_read_b64_tr_b16 a[28:29], v201 offset:4096\n"
"v_cvt_pk_bf16_f32 v163, v170, v171\n"
"v_mfma_f32_16x16x32_bf16 v[240:243], a[12:15], v[8:11], v[240:243]\n"
"s_add_u32 s69, s77, 56320\n"
"ds_read_b64_tr_b16 a[30:31], v201 offset:5120\n"
"ds_read_b64_tr_b16 a[32:33], v201 offset:6144\n"
"v_mfma_f32_16x16x32_bf16 v[240:243], a[16:19], v[12:15], v[240:243]\n"
"s_nop 0\n"
"buffer_store_dwordx4 v[160:163], v182, s[44:47], s69 offen sc1\n"
"v_mfma_f32_16x16x32_bf16 v[244:247], a[4:7], v[56:59], v[244:247]\n"
"ds_read_b64_tr_b16 a[34:35], v201 offset:7168\n"
"v_cvt_pk_bf16_f32 v164, v164, v165\n"
"v_mfma_f32_16x16x32_bf16 v[244:247], a[8:11], v[60:63], v[244:247]\n"
"ds_read_b64_tr_b16 a[36:37], v200 offset:8192\n"
"v_cvt_pk_bf16_f32 v165, v166, v167\n"
"v_mfma_f32_16x16x32_bf16 v[244:247], a[12:15], v[64:67], v[244:247]\n"
"ds_read_b64_tr_b16 a[38:39], v200 offset:9216\n"
"v_cvt_pk_bf16_f32 v166, v172, v173\n"
"v_mfma_f32_16x16x32_bf16 v[244:247], a[16:19], v[68:71], v[244:247]\n"
"s_waitcnt lgkmcnt(6)\n"
"ds_read_b64_tr_b16 a[40:41], v200 offset:10240\n"
"v_cvt_pk_bf16_f32 v167, v174, v175\n"
"v_mfma_f32_16x16x32_bf16 v[248:251], a[20:23], v[0:3], v[248:251]\n"
"s_add_u32 s69, s77, 72704\n"
"ds_read_b64_tr_b16 a[42:43], v200 offset:11264\n"
"ds_read_b64_tr_b16 a[44:45], v200 offset:12288\n"
"v_mfma_f32_16x16x32_bf16 v[248:251], a[24:27], v[4:7], v[248:251]\n"
"s_waitcnt lgkmcnt(5)\n"
"buffer_store_dwordx4 v[164:167], v182, s[44:47], s69 offen sc1\n"
"v_mfma_f32_16x16x32_bf16 v[248:251], a[28:31], v[8:11], v[248:251]\n"
"ds_read_b64_tr_b16 a[46:47], v200 offset:13312\n"
"v_mfma_f32_16x16x32_bf16 v[248:251], a[32:35], v[12:15], v[248:251]\n"
"ds_read_b64_tr_b16 a[48:49], v200 offset:14336\n"
"v_mfma_f32_16x16x32_bf16 v[252:255], a[20:23], v[56:59], v[252:255]\n"
"ds_read_b64_tr_b16 a[50:51], v200 offset:15360\n"
"v_mfma_f32_16x16x32_bf16 v[252:255], a[24:27], v[60:63], v[252:255]\n"
"ds_read_b64_tr_b16 a[52:53], v201 offset:8192\n"
"v_mfma_f32_16x16x32_bf16 v[252:255], a[28:31], v[64:67], v[252:255]\n"
"ds_read_b64_tr_b16 a[54:55], v201 offset:9216\n"
"v_mfma_f32_16x16x32_bf16 v[252:255], a[32:35], v[68:71], v[252:255]\n"
"s_waitcnt lgkmcnt(6)\n"
"ds_read_b64_tr_b16 a[56:57], v201 offset:10240\n"
"v_mfma_f32_16x16x32_bf16 v[240:243], a[36:39], v[16:19], v[240:243]\n"
"ds_read_b64_tr_b16 a[58:59], v201 offset:11264\n"
"v_mfma_f32_16x16x32_bf16 v[240:243], a[40:43], v[20:23], v[240:243]\n"
"s_waitcnt lgkmcnt(4)\n"
"ds_read_b64_tr_b16 a[0:1], v201 offset:12288\n"
"v_mfma_f32_16x16x32_bf16 v[240:243], a[44:47], v[24:27], v[240:243]\n"
"ds_read_b64_tr_b16 a[2:3], v201 offset:13312\n"
"v_mfma_f32_16x16x32_bf16 v[240:243], a[48:51], v[28:31], v[240:243]\n"
"ds_read_b64_tr_b16 a[4:5], v201 offset:14336\n"
"v_mfma_f32_16x16x32_bf16 v[244:247], a[36:39], v[72:75], v[244:247]\n"
"ds_read_b64_tr_b16 a[6:7], v201 offset:15360\n"
"v_mfma_f32_16x16x32_bf16 v[244:247], a[40:43], v[76:79], v[244:247]\n"
"ds_read_b64_tr_b16 a[8:9], v200 offset:16384\n"
"v_mfma_f32_16x16x32_bf16 v[244:247], a[44:47], v[80:83], v[244:247]\n"
"ds_read_b64_tr_b16 a[10:11], v200 offset:17408\n"
"v_mfma_f32_16x16x32_bf16 v[244:247], a[48:51], v[84:87], v[244:247]\n"
"s_waitcnt lgkmcnt(6)\n"
"ds_read_b64_tr_b16 a[12:13], v200 offset:18432\n"
"v_mfma_f32_16x16x32_bf16 v[248:251], a[52:55], v[16:19], v[248:251]\n"
"ds_read_b64_tr_b16 a[14:15], v200 offset:19456\n"
"v_mfma_f32_16x16x32_bf16 v[248:251], a[56:59], v[20:23], v[248:251]\n"
"s_waitcnt lgkmcnt(4)\n"
"ds_read_b64_tr_b16 a[16:17], v200 offset:20480\n"
"v_mfma_f32_16x16x32_bf16 v[248:251], a[0:3], v[24:27], v[248:251]\n"
"ds_read_b64_tr_b16 a[18:19], v200 offset:21504\n"
"v_mfma_f32_16x16x32_bf16 v[248:251], a[4:7], v[28:31], v[248:251]\n"
"ds_read_b64_tr_b16 a[20:21], v200 offset:22528\n"
"v_mfma_f32_16x16x32_bf16 v[252:255], a[52:55], v[72:75], v[252:255]\n"
"ds_read_b64_tr_b16 a[22:23], v200 offset:23552\n"
"v_mfma_f32_16x16x32_bf16 v[252:255], a[56:59], v[76:79], v[252:255]\n"
"v_mfma_f32_16x16x32_bf16 v[252:255], a[0:3], v[80:83], v[252:255]\n"
"ds_read_b64_tr_b16 a[24:25], v196 offset:0\n"
"v_mfma_f32_16x16x32_bf16 v[252:255], a[4:7], v[84:87], v[252:255]\n"
"s_waitcnt lgkmcnt(5)\n"
"ds_read_b64_tr_b16 a[28:29], v201 offset:16384\n"
"v_mfma_f32_16x16x32_bf16 v[240:243], a[8:11], a[92:95], v[240:243]\n"
"ds_read_b64_tr_b16 a[30:31], v201 offset:17408\n"
"v_mfma_f32_16x16x32_bf16 v[240:243], a[12:15], v[32:35], v[240:243]\n"
"s_waitcnt lgkmcnt(3)\n"
"ds_read_b64_tr_b16 a[32:33], v201 offset:18432\n"
"v_mfma_f32_16x16x32_bf16 v[240:243], a[16:19], a[96:99], v[240:243]\n"
"ds_read_b64_tr_b16 a[34:35], v201 offset:19456\n"
"v_mfma_f32_16x16x32_bf16 v[240:243], a[20:23], v[36:39], v[240:243]\n"
"ds_read_b64_tr_b16 a[36:37], v201 offset:20480\n"
"v_mfma_f32_16x16x32_bf16 v[244:247], a[8:11], a[134:137], v[244:247]\n"
"ds_read_b64_tr_b16 a[38:39], v201 offset:21504\n"
"v_mfma_f32_16x16x32_bf16 v[244:247], a[12:15], v[88:91], v[244:247]\n"
"ds_read_b64_tr_b16 a[40:41], v201 offset:22528\n"
"v_mfma_f32_16x16x32_bf16 v[244:247], a[16:19], a[138:141], v[244:247]\n"
"ds_read_b64_tr_b16 a[42:43], v201 offset:23552\n"
"v_mfma_f32_16x16x32_bf16 v[244:247], a[20:23], v[92:95], v[244:247]\n"
"s_waitcnt lgkmcnt(8)\n"
"v_mfma_f32_16x16x16_bf16 v[240:243], a[24:25], a[100:101], v[240:243]\n"
"s_nop 3\n"
"v_mfma_f32_16x16x16_bf16 v[244:247], a[24:25], a[142:143], v[244:247]\n"
"s_waitcnt lgkmcnt(4)\n"
"v_mfma_f32_16x16x32_bf16 v[248:251], a[28:31], a[92:95], v[248:251]\n"
"v_mfma_f32_16x16x32_bf16 v[248:251], a[32:35], v[32:35], v[248:251]\n"
"s_waitcnt lgkmcnt(0)\n"
"v_mfma_f32_16x16x32_bf16 v[248:251], a[36:39], a[96:99], v[248:251]\n"
"ds_read_b64_tr_b16 a[44:45], v196 offset:32\n"
"v_mfma_f32_16x16x32_bf16 v[248:251], a[40:43], v[36:39], v[248:251]\n"
"v_mfma_f32_16x16x32_bf16 v[252:255], a[28:31], a[134:137], v[252:255]\n"
"v_mfma_f32_16x16x32_bf16 v[252:255], a[32:35], v[88:91], v[252:255]\n"
"v_mfma_f32_16x16x32_bf16 v[252:255], a[36:39], a[138:141], v[252:255]\n"
"v_mfma_f32_16x16x32_bf16 v[252:255], a[40:43], v[92:95], v[252:255]\n"
"s_waitcnt lgkmcnt(0)\n"
"s_nop 0\n"
"v_mfma_f32_16x16x16_bf16 v[248:251], a[44:45], a[100:101], v[248:251]\n"
"v_cvt_pk_bf16_f32 v240, v240, v241\n"
"v_cvt_pk_bf16_f32 v241, v242, v243\n"
"s_nop 0\n"
"v_mfma_f32_16x16x16_bf16 v[252:255], a[44:45], a[142:143], v[252:255]\n"
"s_nop 3\n"
"v_cvt_pk_bf16_f32 v242, v248, v249\n"
"v_cvt_pk_bf16_f32 v243, v250, v251\n"
"s_add_u32 s69, s77, 37888\n"
"buffer_store_dwordx4 v[240:243], v182, s[44:47], s69 offen sc1\n"
"v_cvt_pk_bf16_f32 v244, v244, v245\n"
"v_cvt_pk_bf16_f32 v245, v246, v247\n"
"v_cvt_pk_bf16_f32 v246, v252, v253\n"
"v_cvt_pk_bf16_f32 v247, v254, v255\n"
"s_add_u32 s69, s77, 38912\n"
"buffer_store_dwordx4 v[244:247], v182, s[44:47], s69 offen sc1\n"
"buffer_load_dwordx2 v[212:213], v191, s[44:47], s66 offen sc1\n"
"v_lshlrev_b32 v144, 16, v216\n"
"v_and_b32 v145, 0xffff0000, v216\n"
"v_lshlrev_b32 v146, 16, v217\n"
"v_and_b32 v147, 0xffff0000, v217\n"
"v_lshlrev_b32 v148, 16, v218\n"
"v_and_b32 v149, 0xffff0000, v218\n"
"v_lshlrev_b32 v150, 16, v219\n"
"v_and_b32 v151, 0xffff0000, v219\n"
"v_lshlrev_b32 v152, 16, v220\n"
"v_and_b32 v153, 0xffff0000, v220\n"
"v_lshlrev_b32 v154, 16, v221\n"
"v_and_b32 v155, 0xffff0000, v221\n"
"v_lshlrev_b32 v156, 16, v222\n"
"v_and_b32 v157, 0xffff0000, v222\n"
"v_lshlrev_b32 v158, 16, v223\n"
"v_and_b32 v159, 0xffff0000, v223\n"
"v_lshlrev_b32 v160, 16, v224\n"
"v_and_b32 v161, 0xffff0000, v224\n"
"v_lshlrev_b32 v162, 16, v225\n"
"v_and_b32 v163, 0xffff0000, v225\n"
"s_andn2_b32 s79, 0x1f, s68\n"
"s_mov_b32 s60, 0\n"
"s_waitcnt vmcnt(1)\n"
"v_add_u32_e64 v199, s65, 5\n"
"buffer_store_dwordx2 v[198:199], v197, s[44:47], s83 offen offset:0 sc1\n"
"s_waitcnt vmcnt(1)\n"
"s_branch 34f\n"
"30:\n"
"s_waitcnt vmcnt(0)\n"
"34:\n"
"s_mov_b32 s80, s79\n"
"s_mov_b32 s79, 0\n"
"s_cmp_eq_u32 s68, 0\n"
"s_cbranch_scc1 33f\n"
"v_subrev_u32 v213, s65, v213\n"
"v_xor_b32 v212, s64, v212\n"
"v_and_b32 v214, -8, v213\n"
"v_or_b32 v212, v212, v214\n"
"v_min_u32 v212, 1, v212\n"
"v_add_u32 v212, -1, v212\n"
"v_and_b32 v214, v212, v213\n"
"s_bitcmp1_b32 s68, 0\n"
"s_cbranch_scc0 31f\n"
"v_cmp_ge_u32 vcc, 0, v214\n"
"v_add_u32 v190, 0, v189\n"
"v_add_u32 v227, 0, v226\n"
"s_nop 0\n"
"s_cbranch_vccnz 32f\n"
"s_bitset0_b32 s68, 0\n"
"s_bitset1_b32 s79, 0\n"
"buffer_load_dwordx4 v[40:43], v190, s[44:47], s58 offen offset:0 sc1\n"
"buffer_load_dwordx4 v[44:47], v227, s[44:47], s59 offen offset:0 sc1\n"
"buffer_load_dwordx4 v[48:51], v190, s[44:47], s58 offen offset:1024 sc1\n"
"buffer_load_dwordx4 v[52:55], v227, s[44:47], s59 offen offset:1024 sc1\n"
"31:\n"
"s_bitcmp1_b32 s68, 1\n"
"s_cbranch_scc0 31f\n"
"v_cmp_ge_u32 vcc, 1, v214\n"
"v_add_u32 v190, 2048, v189\n"
"v_add_u32 v227, 2048, v226\n"
"s_nop 0\n"
"s_cbranch_vccnz 32f\n"
"s_bitset0_b32 s68, 1\n"
"s_bitset1_b32 s79, 1\n"
"buffer_load_dwordx4 v[96:99], v190, s[44:47], s58 offen offset:0 sc1\n"
"buffer_load_dwordx4 v[100:103], v227, s[44:47], s59 offen offset:0 sc1\n"
"buffer_load_dwordx4 v[104:107], v190, s[44:47], s58 offen offset:1024 sc1\n"
"buffer_load_dwordx4 v[108:111], v227, s[44:47], s59 offen offset:1024 sc1\n"
"31:\n"
"s_bitcmp1_b32 s68, 2\n"
"s_cbranch_scc0 31f\n"
"v_cmp_ge_u32 vcc, 2, v214\n"
"v_add_u32 v190, 4096, v189\n"
"v_add_u32 v227, 4096, v226\n"
"s_nop 0\n"
"s_cbranch_vccnz 32f\n"
"s_bitset0_b32 s68, 2\n"
"s_bitset1_b32 s79, 2\n"
"buffer_load_dwordx4 a[172:175], v190, s[44:47], s58 offen offset:0 sc1\n"
"buffer_load_dwordx4 a[176:179], v227, s[44:47], s59 offen offset:0 sc1\n"
"buffer_load_dwordx4 a[180:183], v190, s[44:47], s58 offen offset:1024 sc1\n"
"buffer_load_dwordx4 a[184:187], v227, s[44:47], s59 offen offset:1024 sc1\n"
"31:\n"
"s_bitcmp1_b32 s68, 3\n"
"s_cbranch_scc0 31f\n"
"v_cmp_ge_u32 vcc, 3, v214\n"
"v_add_u32 v190, 6144, v189\n"
"v_add_u32 v227, 6144, v226\n"
"s_nop 0\n"
"s_cbranch_vccnz 32f\n"
"s_bitset0_b32 s68, 3\n"
"s_bitset1_b32 s79, 3\n"
"buffer_load_dwordx4 a[188:191], v190, s[44:47], s58 offen offset:0 sc1\n"
"buffer_load_dwordx4 a[192:195], v227, s[44:47], s59 offen offset:0 sc1\n"
"buffer_load_dwordx4 a[196:199], v190, s[44:47], s58 offen offset:1024 sc1\n"
"buffer_load_dwordx4 a[200:203], v227, s[44:47], s59 offen offset:1024 sc1\n"
"31:\n"
"s_bitcmp1_b32 s68, 4\n"
"s_cbranch_scc0 31f\n"
"v_cmp_ge_u32 vcc, 4, v214\n"
"v_add_u32 v190, 0, v189\n"
"v_add_u32 v227, 0, v226\n"
"s_nop 0\n"
"s_cbranch_vccnz 32f\n"
"s_bitset0_b32 s68, 4\n"
"s_bitset1_b32 s79, 4\n"
"buffer_load_dwordx4 v[204:207], v190, s[44:47], s81 offen offset:0 sc1\n"
"buffer_load_dwordx4 v[208:211], v227, s[44:47], s82 offen offset:0 sc1\n"
"31:\n"
"s_branch 33f\n"
"32:\n"
"buffer_load_dwordx2 v[212:213], v191, s[44:47], s66 offen sc1\n"
"s_add_u32 s60, s60, 1\n"
"s_cmp_lt_u32 s60, 0x80000\n"
"s_cselect_b32 s68, s68, 0\n"
"33:\n"
"s_bitcmp1_b32 s80, 0\n"
"s_cbranch_scc0 35f\n"
"v_mfma_f32_16x16x32_bf16 v[112:115], a[164:167], v[40:43], 0\n"
"v_mfma_f32_16x16x32_bf16 v[112:115], a[164:167], v[44:47], v[112:115]\n"
"v_mfma_f32_16x16x32_bf16 v[112:115], a[168:171], v[48:51], v[112:115]\n"
"v_mfma_f32_16x16x32_bf16 v[112:115], a[168:171], v[52:55], v[112:115]\n"
"s_nop 7\n"
"ds_bpermute_b32 v112, v195, v112\n"
"ds_bpermute_b32 v113, v195, v113\n"
"ds_bpermute_b32 v114, v195, v114\n"
"ds_bpermute_b32 v115, v195, v115\n"
"s_waitcnt lgkmcnt(3)\n"
"v_fmac_f32 v144, v112, v193\n"
"s_waitcnt lgkmcnt(2)\n"
"v_fmac_f32 v145, v113, v193\n"
"s_waitcnt lgkmcnt(1)\n"
"v_fmac_f32 v146, v114, v193\n"
"s_waitcnt lgkmcnt(0)\n"
"v_fmac_f32 v147, v115, v193\n"
"v_cvt_pk_bf16_f32 v144, v144, v145\n"
"v_cvt_pk_bf16_f32 v145, v146, v147\n"
"35:\n"
"s_bitcmp1_b32 s80, 1\n"
"s_cbranch_scc0 35f\n"
"v_mfma_f32_16x16x32_bf16 v[116:119], a[164:167], v[96:99], 0\n"
"v_mfma_f32_16x16x32_bf16 v[116:119], a[164:167], v[100:103], v[116:119]\n"
"v_mfma_f32_16x16x32_bf16 v[116:119], a[168:171], v[104:107], v[116:119]\n"
"v_mfma_f32_16x16x32_bf16 v[116:119], a[168:171], v[108:111], v[116:119]\n"
"s_nop 7\n"
"ds_bpermute_b32 v116, v195, v116\n"
"ds_bpermute_b32 v117, v195, v117\n"
"ds_bpermute_b32 v118, v195, v118\n"
"ds_bpermute_b32 v119, v195, v119\n"
"s_waitcnt lgkmcnt(3)\n"
"v_fmac_f32 v148, v116, v193\n"
"s_waitcnt lgkmcnt(2)\n"
"v_fmac_f32 v149, v117, v193\n"
"115:\n"
"s_cbranch_execz 98b\n"
"s_waitcnt lgkmcnt(1)\n"
"v_fmac_f32 v150, v118, v193\n"
"s_waitcnt lgkmcnt(0)\n"
"v_fmac_f32 v151, v119, v193\n"
"v_cvt_pk_bf16_f32 v148, v148, v149\n"
"v_cvt_pk_bf16_f32 v149, v150, v151\n"
"35:\n"
"s_bitcmp1_b32 s80, 2\n"
"s_cbranch_scc0 35f\n"
"v_mfma_f32_16x16x32_bf16 v[120:123], a[164:167], a[172:175], 0\n"
"v_mfma_f32_16x16x32_bf16 v[120:123], a[164:167], a[176:179], v[120:123]\n"
"v_mfma_f32_16x16x32_bf16 v[120:123], a[168:171], a[180:183], v[120:123]\n"
"v_mfma_f32_16x16x32_bf16 v[120:123], a[168:171], a[184:187], v[120:123]\n"
"s_nop 7\n"
"ds_bpermute_b32 v120, v195, v120\n"
"ds_bpermute_b32 v121, v195, v121\n"
"ds_bpermute_b32 v122, v195, v122\n"
"ds_bpermute_b32 v123, v195, v123\n"
"s_waitcnt lgkmcnt(3)\n"
"v_fmac_f32 v152, v120, v193\n"
"s_waitcnt lgkmcnt(2)\n"
"v_fmac_f32 v153, v121, v193\n"
"s_waitcnt lgkmcnt(1)\n"
"v_fmac_f32 v154, v122, v193\n"
"s_waitcnt lgkmcnt(0)\n"
"v_fmac_f32 v155, v123, v193\n"
"v_cvt_pk_bf16_f32 v152, v152, v153\n"
"v_cvt_pk_bf16_f32 v153, v154, v155\n"
"35:\n"
"s_bitcmp1_b32 s80, 3\n"
"s_cbranch_scc0 35f\n"
"v_mfma_f32_16x16x32_bf16 v[124:127], a[164:167], a[188:191], 0\n"
"v_mfma_f32_16x16x32_bf16 v[124:127], a[164:167], a[192:195], v[124:127]\n"
"v_mfma_f32_16x16x32_bf16 v[124:127], a[168:171], a[196:199], v[124:127]\n"
"v_mfma_f32_16x16x32_bf16 v[124:127], a[168:171], a[200:203], v[124:127]\n"
"s_nop 7\n"
"ds_bpermute_b32 v124, v195, v124\n"
"ds_bpermute_b32 v125, v195, v125\n"
"ds_bpermute_b32 v126, v195, v126\n"
"ds_bpermute_b32 v127, v195, v127\n"
"s_waitcnt lgkmcnt(3)\n"
"v_fmac_f32 v156, v124, v193\n"
"s_waitcnt lgkmcnt(2)\n"
"v_fmac_f32 v157, v125, v193\n"
"s_waitcnt lgkmcnt(1)\n"
"v_fmac_f32 v158, v126, v193\n"
"s_waitcnt lgkmcnt(0)\n"
"v_fmac_f32 v159, v127, v193\n"
"v_cvt_pk_bf16_f32 v156, v156, v157\n"
"v_cvt_pk_bf16_f32 v157, v158, v159\n"
"35:\n"
"s_bitcmp1_b32 s80, 4\n"
"s_cbranch_scc0 35f\n"
"v_mfma_f32_16x16x32_bf16 v[128:131], a[164:167], v[204:207], 0\n"
"v_mfma_f32_16x16x32_bf16 v[128:131], a[164:167], v[208:211], v[128:131]\n"
"s_nop 7\n"
"ds_bpermute_b32 v128, v195, v128\n"
"ds_bpermute_b32 v129, v195, v129\n"
"ds_bpermute_b32 v130, v195, v130\n"
"ds_bpermute_b32 v131, v195, v131\n"
"s_waitcnt lgkmcnt(3)\n"
"v_fmac_f32 v160, v128, v193\n"
"s_waitcnt lgkmcnt(2)\n"
"v_fmac_f32 v161, v129, v193\n"
"s_waitcnt lgkmcnt(1)\n"
"v_fmac_f32 v162, v130, v193\n"
"s_waitcnt lgkmcnt(0)\n"
"v_fmac_f32 v163, v131, v193\n"
"v_cvt_pk_bf16_f32 v160, v160, v161\n"
"v_cvt_pk_bf16_f32 v161, v162, v163\n"
"35:\n"
"s_or_b32 s61, s68, s79\n"
"s_cbranch_scc1 30b\n"
"buffer_store_dwordx2 v[160:161], v194, s[40:43], s67 offen offset:512\n"
"buffer_store_dwordx2 v[156:157], v192, s[40:43], s67 offen offset:384\n"
"buffer_store_dwordx2 v[152:153], v192, s[40:43], s67 offen offset:256\n"
"buffer_store_dwordx2 v[148:149], v192, s[40:43], s67 offen offset:128\n"
"buffer_store_dwordx2 v[144:145], v192, s[40:43], s67 offen offset:0\n"
"s_or_b32 s60, %[bid], s54\n"
"s_min_u32 s60, s60, 1\n"
"s_lshl_b32 s60, s60, 28\n"
"v_or_b32 v197, s60, v197\n"
"v_add_u32_e64 v199, s65, 0\n"
"buffer_store_dwordx2 v[198:199], v197, s[44:47], 0 offen offset:0 sc1\n"
"s_endpgm\n"
"50:\n"
"s_mov_b32 s98, 0\n"
"52:\n"
"s_sleep 2\n"
"buffer_load_dwordx2 v[244:245], v181, s[44:47], s79 offen sc1\n"
"s_waitcnt vmcnt(0)\n"
"v_cmp_ne_u64 vcc, s[64:65], v[244:245]\n"
"s_nop 4\n"
"s_cbranch_vccz 51b\n"
"s_add_u32 s98, s98, 1\n"
"s_cmp_lt_u32 s98, 0x80000\n"
"s_cbranch_scc1 52b\n"
"s_branch 51b\n"
"53:\n"
"s_mov_b32 s98, 0\n"
"55:\n"
"s_sleep 2\n"
"buffer_load_dwordx2 v[192:193], v189, s[44:47], s59 offen sc1\n"
"s_waitcnt vmcnt(0)\n"
"v_subrev_u32 v193, s65, v193\n"
"v_xor_b32 v192, s64, v192\n"
"v_and_b32 v199, -2, v193\n"
"v_or_b32 v192, v192, v199\n"
"v_cmp_ne_u32 vcc, 0, v192\n"
"s_nop 4\n"
"s_cbranch_vccz 54b\n"
"s_add_u32 s98, s98, 1\n"
"s_cmp_lt_u32 s98, 0x80000\n"
"s_cbranch_scc1 55b\n"
"s_branch 54b\n"
"56:\n"
"s_mov_b32 s98, 0\n"
"58:\n"
"s_sleep 2\n"
"buffer_load_dwordx2 v[192:193], v191, s[44:47], s59 offen sc1\n"
"s_waitcnt vmcnt(0)\n"
"v_cmp_ne_u64 vcc, s[66:67], v[192:193]\n"
"s_nop 4\n"
"s_cbranch_vccz 57b\n"
"s_add_u32 s98, s98, 1\n"
"s_cmp_lt_u32 s98, 0x80000\n"
"s_cbranch_scc1 58b\n"
"s_branch 57b\n"
"59:\n"
"s_mov_b32 s98, 0\n"
"61:\n"
"s_sleep 2\n"
"buffer_load_dwordx2 a[252:253], off, s[44:47], s80 sc1\n"
"s_waitcnt vmcnt(0)\n"
"v_accvgpr_read_b32 v203, a252\n"
"v_accvgpr_read_b32 v199, a253\n"
"v_xor_b32 v203, s64, v203\n"
"v_xor_b32 v199, s65, v199\n"
"v_or_b32 v203, v203, v199\n"
"v_cmp_ne_u32 vcc, 0, v203\n"
"s_nop 4\n"
"s_cbranch_vccz 60b\n"
"s_add_u32 s98, s98, 1\n"
"s_cmp_lt_u32 s98, 0x80000\n"
"s_cbranch_scc1 61b\n"
"s_branch 60b\n"
"62:\n"
"s_mov_b32 s98, 0\n"
"64:\n"
"s_sleep 2\n"
"buffer_load_dwordx2 a[252:253], off, s[44:47], s80 sc1\n"
"s_waitcnt vmcnt(0)\n"
"v_accvgpr_read_b32 v203, a252\n"
"v_accvgpr_read_b32 v199, a253\n"
"v_xor_b32 v203, s64, v203\n"
"v_xor_b32 v199, s65, v199\n"
"v_or_b32 v203, v203, v199\n"
"v_cmp_ne_u32 vcc, 0, v203\n"
"s_nop 4\n"
"s_cbranch_vccz 63b\n"
"s_add_u32 s98, s98, 1\n"
"s_cmp_lt_u32 s98, 0x80000\n"
"s_cbranch_scc1 64b\n"
"s_branch 63b\n"
"99:\n"
  :
  : [ka] "s"(ka), [wv] "s"(wv), [bid] "s"(bid), [lb] "s"(lb)
  : "memory", "vcc", "scc", "v0","v1","v2","v3","v4","v5","v6","v7","v8","v9","v10","v11","v12","v13","v14","v15","v16","v17","v18","v19","v20","v21","v22","v23","v24","v25","v26","v27","v28","v29","v30","v31","v32","v33","v34","v35","v36","v37","v38","v39","v40","v41","v42","v43","v44","v45","v46","v47","v48","v49","v50","v51","v52","v53","v54","v55","v56","v57","v58","v59","v60","v61","v62","v63","v64","v65","v66","v67","v68","v69","v70","v71","v72","v73","v74","v75","v76","v77","v78","v79","v80","v81","v82","v83","v84","v85","v86","v87","v88","v89","v90","v91","v92","v93","v94","v95","v96","v97","v98","v99","v100","v101","v102","v103","v104","v105","v106","v107","v108","v109","v110","v111","v112","v113","v114","v115","v116","v117","v118","v119","v120","v121","v122","v123","v124","v125","v126","v127","v128","v129","v130","v131","v132","v133","v134","v135","v136","v137","v138","v139","v140","v141","v142","v143","v144","v145","v146","v147","v148","v149","v150","v151","v152","v153","v154","v155","v156","v157","v158","v159","v160","v161","v162","v163","v164","v165","v166","v167","v168","v169","v170","v171","v172","v173","v174","v175","v176","v177","v178","v179","v180","v181","v182","v183","v184","v185","v186","v187","v188","v189","v190","v191","v192","v193","v194","v195","v196","v197","v198","v199","v200","v201","v202","v203","v204","v205","v206","v207","v208","v209","v210","v211","v212","v213","v214","v215","v216","v217","v218","v219","v220","v221","v222","v223","v224","v225","v226","v227","v228","v229","v230","v231","v232","v233","v234","v235","v236","v237","v238","v239","v240","v241","v242","v243","v244","v245","v246","v247","v248","v249","v250","v251","v252","v253","v254","v255","a0","a1","a2","a3","a4","a5","a6","a7","a8","a9","a10","a11","a12","a13","a14","a15","a16","a17","a18","a19","a20","a21","a22","a23","a24","a25","a26","a27","a28","a29","a30","a31","a32","a33","a34","a35","a36","a37","a38","a39","a40","a41","a42","a43","a44","a45","a46","a47","a48","a49","a50","a51","a52","a53","a54","a55","a56","a57","a58","a59","a60","a61","a62","a63","a64","a65","a66","a67","a68","a69","a70","a71","a72","a73","a74","a75","a76","a77","a78","a79","a80","a81","a82","a83","a84","a85","a86","a87","a88","a89","a90","a91","a92","a93","a94","a95","a96","a97","a98","a99","a100","a101","a102","a103","a104","a105","a106","a107","a108","a109","a110","a111","a112","a113","a114","a115","a116","a117","a118","a119","a120","a121","a122","a123","a124","a125","a126","a127","a128","a129","a130","a131","a132","a133","a134","a135","a136","a137","a138","a139","a140","a141","a142","a143","a144","a145","a146","a147","a148","a149","a150","a151","a152","a153","a154","a155","a156","a157","a158","a159","a160","a161","a162","a163","a164","a165","a166","a167","a168","a169","a170","a171","a172","a173","a174","a175","a176","a177","a178","a179","a180","a181","a182","a183","a184","a185","a186","a187","a188","a189","a190","a191","a192","a193","a194","a195","a196","a197","a198","a199","a200","a201","a202","a203","a204","a205","a206","a207","a208","a209","a210","a211","a212","a213","a214","a215","a216","a217","a218","a219","a220","a221","a222","a223","a224","a225","a226","a227","a228","a229","a230","a231","a232","a233","a234","a235","a236","a237","a238","a239","a240","a241","a242","a243","a244","a245","a246","a247","a248","a249","a250","a251","a252","a253","a254","a255","s5","s6","s7","s8","s9","s10","s11","s12","s13","s14","s15","s16","s17","s18","s19","s20","s21","s22","s23","s24","s25","s26","s27","s28","s29","s30","s31","s36","s37","s38","s39","s40","s41","s42","s43","s44","s45","s46","s47","s48","s49","s50","s51","s52","s53","s54","s55","s56","s57","s58","s59","s60","s61","s62","s63","s64","s65","s66","s67","s68","s69","s70","s71","s72","s73","s74","s75","s76","s77","s78","s79","s80","s81","s82","s83","s84","s85","s86","s87","s88","s89","s90","s91","s92","s93","s94","s95","s96","s97","s98","s99");
}

extern "C" void kernel_launch(void* const* d_in, const int* in_sizes, int n_in, void* d_out, int out_size, void* d_ws, size_t ws_size,
                              hipStream_t stream) {
  fused_main<<<256, 256, 0, stream>>>((const u16*)d_in[0], (const u16*)d_in[1], (const u16*)d_in[2], (const u16*)d_in[3], (const u16*)d_in[5],
                                      (const u16*)d_in[6], (const u16*)d_in[7], (u16*)d_out, (char*)d_ws);
}
```
